```python
import torch

def _softmax(x, axis=-1):
    return torch.softmax(x, dim=axis)
def _gelu(x):
    return torch.nn.functional.gelu(x, approximate="tanh")
def _silu(x):
    return torch.nn.functional.silu(x)
def _sigmoid(x):
    return torch.sigmoid(x)
def _layernorm(x, axis=-1, eps=1e-5):
    m = x.mean(dim=axis, keepdim=True)
    v = x.var(dim=axis, keepdim=True, unbiased=False)
    return (x - m) / torch.sqrt(v + eps)
def _scatter_add(tgt, idx, src, axis=0):
    return tgt.clone().index_add_(axis, idx, src)
def _segment_sum(src, seg, n):
    out = torch.zeros((n,) + src.shape[1:], dtype=src.dtype)
    return out.index_add_(0, seg, src)
def _pad(x, pw):
    flat = tuple(v for pair in reversed(pw) for v in pair)
    return torch.nn.functional.pad(x, flat)

kernel_name = 'parallel_sum__ssm_conv_s5__geglu_ffn__prompt'

def setup_inputs():

    _w = (lambda a: (lambda t: t.to(torch.bfloat16) if t.is_floating_point() else t)(torch.as_tensor(a)))
    def _A_setup():
        B, S, D = 1, 2048, 640
        P = D
        Lambda_re = -0.5 * (lambda _x: torch.exp(torch.as_tensor(_x)))((lambda *s: torch.randn(*s, dtype=torch.bfloat16) * 0.8)(P))
        Lambda_im = (lambda *s: torch.randn(*s, dtype=torch.bfloat16) * 0.8)(P) * 1.0
        B_re = (lambda *s: torch.randn(*s, dtype=torch.bfloat16) * 0.8)(P, D) * (D ** -0.5)
        B_im = (lambda *s: torch.randn(*s, dtype=torch.bfloat16) * 0.8)(P, D) * (D ** -0.5)
        C_re = (lambda *s: torch.randn(*s, dtype=torch.bfloat16) * 0.8)(D, P) * (P ** -0.5)
        C_im = (lambda *s: torch.randn(*s, dtype=torch.bfloat16) * 0.8)(D, P) * (P ** -0.5)
        log_dt = (lambda *s: torch.randn(*s, dtype=torch.bfloat16) * 0.8)(P) * 0.3 - 2.0
        Dskip = (lambda *s: torch.randn(*s, dtype=torch.bfloat16) * 0.8)(D)
        return {
            "x": _w((lambda *s: torch.randn(*s, dtype=torch.bfloat16) * 0.05)(B, S, D)),
            "Lambda_re": _w(Lambda_re),
            "Lambda_im": _w(Lambda_im),
            "B_re": _w(B_re),
            "B_im": _w(B_im),
            "C_re": _w(C_re),
            "C_im": _w(C_im),
            "log_dt": _w(log_dt),
            "Dskip": _w(Dskip),
        }
    _A_in = _A_setup()

    def _B_setup():
        B, S, D, F = 1, 2048, 640, 1536
        return {
            "x": _w((lambda *s: torch.randn(*s, dtype=torch.bfloat16) * 0.05)(B, S, D)),
            "w_gate": _w((lambda *s: torch.randn(*s, dtype=torch.bfloat16) * 0.05)(D, F)),
            "w_up": _w((lambda *s: torch.randn(*s, dtype=torch.bfloat16) * 0.05)(D, F)),
            "w_down": _w((lambda *s: torch.randn(*s, dtype=torch.bfloat16) * 0.05)(F, D)),
        }
    _B_in = _B_setup()

    return {
        "A_x": _A_in["x"],
        "A_Lambda_re": _A_in["Lambda_re"],
        "A_Lambda_im": _A_in["Lambda_im"],
        "A_B_re": _A_in["B_re"],
        "A_B_im": _A_in["B_im"],
        "A_C_re": _A_in["C_re"],
        "A_C_im": _A_in["C_im"],
        "A_log_dt": _A_in["log_dt"],
        "A_Dskip": _A_in["Dskip"],
        "B_x": _B_in["x"],
        "B_w_gate": _B_in["w_gate"],
        "B_w_up": _B_in["w_up"],
        "B_w_down": _B_in["w_down"],
    }

def reference(A_x, A_Lambda_re, A_Lambda_im, A_B_re, A_B_im, A_C_re, A_C_im, A_log_dt, A_Dskip, B_x, B_w_gate, B_w_up, B_w_down):
    def _A_ref(x, Lambda_re, Lambda_im, B_re, B_im, C_re, C_im, log_dt, Dskip):
        Bsz, L, H = x.shape
        Lam = Lambda_re + 1j * Lambda_im
        Bmat = B_re + 1j * B_im
        Cmat = C_re + 1j * C_im
        dt = torch.exp(log_dt)
        Lam_bar = torch.exp(dt * Lam)
        B_bar = ((Lam_bar - 1.0) / Lam)[:, None] * Bmat
        Bu = torch.einsum("bld,pd->blp", x + 0j, B_bar)
        h = torch.zeros_like(Bu[:, 0, :])
        hs = []
        for t in range(L):
            h = Lam_bar[None, :] * h + Bu[:, t, :]
            hs.append(h)
        hseq = torch.stack(hs, 1)
        y = 2.0 * torch.einsum("blp,dp->bld", hseq, Cmat).real + x * Dskip[None, None, :]
        return y

    def _B_ref(x, w_gate, w_up, w_down):
        return (_gelu(x @ w_gate) * (x @ w_up)) @ w_down

    x = A_x
    xn = _layernorm(x, axis=-1)
    return x + _A_ref(xn, A_Lambda_re, A_Lambda_im, A_B_re, A_B_im, A_C_re, A_C_im, A_log_dt, A_Dskip) + _B_ref(xn, B_w_gate, B_w_up, B_w_down)
```

```cpp
#include <hip/hip_runtime.h>
#include <hip/hip_bf16.h>
#include <stdint.h>
typedef unsigned short u16;
static constexpr int L=2048, D=640, P=640, F=1536;
__device__ __forceinline__ float b2f(u16 v){ return __uint_as_float(((unsigned)v)<<16); }
__device__ __forceinline__ u16 f2b(float f){ unsigned u=__float_as_uint(f); u += 0x7fff + ((u>>16)&1); return (u16)(u>>16);}
__device__ __forceinline__ float rb(float f){ return b2f(f2b(f)); }

__global__ void k_ln(const u16* x, u16* xn){
  int row=blockIdx.x; int t=threadIdx.x;
  float v[10]; float s=0;
  for(int i=0;i<10;i++){ v[i]=b2f(x[row*D+i*64+t]); s+=v[i]; }
  for(int o=32;o;o>>=1) s+=__shfl_xor(s,o);
  float m=s/D; float q=0;
  for(int i=0;i<10;i++){ float d=v[i]-m; q+=d*d; }
  for(int o=32;o;o>>=1) q+=__shfl_xor(q,o);
  float var=rb(q/D); float mb=rb(m);
  float sd=rb(sqrtf(rb(var+1e-5f)));
  for(int i=0;i<10;i++){ xn[row*D+i*64+t]=f2b(rb(v[i]-mb)/sd); }
}
__global__ void k_bu(const u16* xn, const u16* Bre, const u16* Bim, float* ur, float* ui){
  int p=blockIdx.x*64+threadIdx.x; int l=blockIdx.y;
  float a=0,b=0;
  for(int d=0;d<D;d++){ float xv=b2f(xn[l*D+d]); a+=xv*b2f(Bre[p*D+d]); b+=xv*b2f(Bim[p*D+d]); }
  ur[l*P+p]=a; ui[l*P+p]=b;
}
__global__ void k_scan(const u16* Lre,const u16* Lim,const u16* ldt,float* ur,float* ui){
  int p=blockIdx.x*64+threadIdx.x;
  float lr=b2f(Lre[p]), li=b2f(Lim[p]); float dt=rb(expf(b2f(ldt[p])));
  float e=expf(dt*lr); float ar=e*cosf(dt*li), ai=e*sinf(dt*li);
  float nr=ar-1.f, ni=ai; float den=lr*lr+li*li;
  float cr=(nr*lr+ni*li)/den, ci=(ni*lr-nr*li)/den;
  float hr=0,hi=0;
  for(int l=0;l<L;l++){ float a=ur[l*P+p], b=ui[l*P+p];
    float br=cr*a-ci*b, bi=cr*b+ci*a;
    float nhr=ar*hr-ai*hi+br, nhi=ar*hi+ai*hr+bi; hr=nhr; hi=nhi;
    ur[l*P+p]=hr; ui[l*P+p]=hi; }
}
__global__ void k_ffn1(const u16* xn,const u16* wg,const u16* wu,u16* act){
  int f=blockIdx.x*64+threadIdx.x; int l=blockIdx.y;
  float a=0,b=0;
  for(int d=0;d<D;d++){ float xv=b2f(xn[l*D+d]); a+=xv*b2f(wg[d*F+f]); b+=xv*b2f(wu[d*F+f]); }
  a=rb(a); b=rb(b);
  float g=0.5f*a*(1.f+tanhf(0.7978845608028654f*(a+0.044715f*a*a*a)));
  act[l*F+f]=f2b(rb(g)*b);
}
__global__ void k_out(const u16* x,const u16* xn,const float* hr,const float* hi,const u16* Cre,const u16* Cim,const u16* Dsk,const u16* act,const u16* wd,float* out){
  int d=blockIdx.x*64+threadIdx.x; int l=blockIdx.y;
  float a=0,b=0;
  for(int p=0;p<P;p++){ a+=hr[l*P+p]*b2f(Cre[d*P+p])-hi[l*P+p]*b2f(Cim[d*P+p]); }
  for(int f=0;f<F;f++){ b+=b2f(act[l*F+f])*b2f(wd[f*D+d]); }
  float y=2.f*a+rb(b2f(xn[l*D+d])*b2f(Dsk[d]));
  out[l*D+d]=b2f(x[l*D+d])+y+rb(b);
}
extern "C" void kernel_launch(void* const* d_in, const int* in_sizes, int n_in, void* d_out, int out_size, void* d_ws, size_t ws_size, hipStream_t stream) {
  const u16* x=(const u16*)d_in[0]; const u16* Lre=(const u16*)d_in[1]; const u16* Lim=(const u16*)d_in[2];
  const u16* Bre=(const u16*)d_in[3]; const u16* Bim=(const u16*)d_in[4]; const u16* Cre=(const u16*)d_in[5]; const u16* Cim=(const u16*)d_in[6];
  const u16* ldt=(const u16*)d_in[7]; const u16* Dsk=(const u16*)d_in[8];
  const u16* wg=(const u16*)d_in[10]; const u16* wu=(const u16*)d_in[11]; const u16* wd=(const u16*)d_in[12];
  char* ws=(char*)d_ws; u16* xn=(u16*)ws; float* ur=(float*)(ws+(4<<20)); float* ui=(float*)(ws+(12<<20)); u16* act=(u16*)(ws+(20<<20));
  k_ln<<<L,64,0,stream>>>(x,xn);
  k_bu<<<dim3(P/64,L),64,0,stream>>>(xn,Bre,Bim,ur,ui);
  k_scan<<<P/64,64,0,stream>>>(Lre,Lim,ldt,ur,ui);
  k_ffn1<<<dim3(F/64,L),64,0,stream>>>(xn,wg,wu,act);
  k_out<<<dim3(D/64,L),64,0,stream>>>(x,xn,ur,ui,Cre,Cim,Dsk,act,wd,(float*)d_out);
}
```

```cpp
#include <hip/hip_runtime.h>
#include <stdint.h>

__global__ __launch_bounds__(256, 1) void fused_s5_geglu(const void* x, unsigned x_n, unsigned x_f, const void* Bre, unsigned Bre_n, unsigned Bre_f, const void* Bim, unsigned Bim_n, unsigned Bim_f, const void* wg, unsigned wg_n, unsigned wg_f, const void* wu, unsigned wu_n, unsigned wu_f, const void* wd, unsigned wd_n, unsigned wd_f, const void* Cre, unsigned Cre_n, unsigned Cre_f, const void* Cim, unsigned Cim_n, unsigned Cim_f, const void* ws, unsigned ws_n, unsigned ws_f, const void* out, unsigned out_n, unsigned out_f, const void* Lre, unsigned Lre_n, unsigned Lre_f, const void* Lim, const void* ldt, const void* Dsk) {
  __shared__ __attribute__((aligned(16))) char lds[163840];
  unsigned lb = (unsigned)(unsigned long long)(__attribute__((address_space(3))) char*)lds;
  asm volatile(
"s_load_dwordx16 s[8:23], %0, 0\n"
"s_load_dwordx16 s[24:39], %0, 64\n"
"s_load_dwordx8 s[40:47], %0, 128\n"
"s_load_dwordx4 s[48:51], %0, 160\n"
"s_load_dwordx4 s[72:75], %0, 176\n"
"s_load_dwordx2 s[76:77], %0, 192\n"
"s_mov_b32 s98, %1\n"
"v_mov_b32 v1, %2\n"
"v_lshrrev_b32 v61, 6, v1\n"
"v_and_b32 v2, 63, v1\n"
"v_readfirstlane_b32 s52, v61\n"
"s_and_b32 s60, s98, 7\n"
"s_lshr_b32 s61, s98, 3\n"
"s_lshr_b32 s53, s60, 2\n"
"s_xor_b32 s53, s53, s60\n"
"s_and_b32 s53, s53, 1\n"
"s_and_b32 s54, s60, 3\n"
"s_lshl_b32 s53, s53, 3\n"
"s_and_b32 s62, s61, 7\n"
"s_add_u32 s53, s53, s62\n"
"s_lshr_b32 s55, s61, 3\n"
"s_lshl_b32 s57, s52, 12\n"
"s_lshl_b32 s101, s55, 1\n"
"s_add_u32 s101, s101, s53\n"
"s_and_b32 s101, s101, 3\n"
"s_mul_i32 s60, s101, 320\n"
"s_add_u32 s60, s60, 64\n"
"s_and_b32 s68, s60, 0xffffff80\n"
"s_cmp_lt_u32 s53, 4\n"
"s_cselect_b32 s101, s101, 4\n"
"v_lshrrev_b32 v61, 3, v2\n"
"v_and_b32 v62, 7, v2\n"
"v_lshrrev_b32 v63, 4, v2\n"
"v_xor_b32 v64, v62, v63\n"
"v_lshlrev_b32 v64, 4, v64\n"
"v_xor_b32 v65, 64, v64\n"
"v_and_b32 v86, 3, v61\n"
"v_and_b32 v87, 4, v61\n"
"v_lshl_add_u32 v86, v87, 1, v86\n"
"s_lshl_b32 s60, s53, 2\n"
"s_add_u32 s60, s60, s52\n"
"s_mul_i32 s65, s60, 181248\n"
"s_mul_i32 s60, s60, 40960\n"
"v_mul_u32_u24 v86, 1280, v86\n"
"v_add3_u32 v30, v86, v64, s60\n"
"s_add_u32 s60, s60, 4096\n"
"v_add3_u32 v31, v86, v65, s60\n"
"v_add_u32 v32, 18432, v30\n"
"v_add_u32 v33, 18432, v31\n"
"s_mul_i32 s63, s54, 10\n"
"s_mul_i32 s64, s54, 6\n"
"s_getpc_b64 s[4:5]\n"
"s_mov_b32 s6, 0xe380\n"
"s_mov_b32 s7, 0x20000\n"
"s_lshr_b32 s62, s98, 3\n"
"s_lshl_b32 s62, s62, 2\n"
"s_add_u32 s62, s62, s52\n"
"s_lshl_b32 s62, s62, 7\n"
"v_lshlrev_b32 v218, 14, v2\n"
"buffer_load_dword v3, v218, s[4:7], s62 offen\n"
"s_waitcnt lgkmcnt(0)\n"
"s_add_u32 m0, s57, 102400\n"
"s_nop 0\n"
"buffer_load_dwordx4 v30, s[8:11], s68 offen offset:0 lds\n"
"buffer_load_dwordx4 v31, s[8:11], s68 offen offset:1024 lds\n"
"buffer_load_dwordx4 v32, s[8:11], s68 offen offset:2048 lds\n"
"buffer_load_dwordx4 v33, s[8:11], s68 offen offset:3072 lds\n"
"s_cmp_lt_u32 s55, 2\n"
"s_cbranch_scc1 LtypeB%=\n"
"s_and_b32 s60, s54, 1\n"
"s_xor_b32 s60, s60, 1\n"
"s_lshl_b32 s60, s60, 3\n"
"s_add_u32 s63, s63, s60\n"
"s_sub_u32 s61, s55, 2\n"
"s_add_u32 s63, s63, s61\n"
"s_lshl_b32 s61, s61, 1\n"
"s_add_u32 s64, s64, s61\n"
"s_mul_i32 s60, s52, 49152\n"
"s_lshl_b32 s61, s64, 7\n"
"s_add_u32 s60, s60, s61\n"
"v_mul_u32_u24 v86, 3072, v61\n"
"v_and_b32 v87, 1, v63\n"
"v_lshlrev_b32 v87, 1, v87\n"
"v_xor_b32 v87, v87, v62\n"
"v_lshlrev_b32 v87, 4, v87\n"
"v_add3_u32 v34, v86, v87, s60\n"
"v_add_u32 v35, 23552, v34\n"
"v_xor_b32 v36, 64, v34\n"
"v_xor_b32 v37, 64, v35\n"
"s_sub_u32 s24, s24, 2048\n"
"s_subb_u32 s25, s25, 0\n"
"s_lshl_b32 s58, s52, 10\n"
"s_and_b32 s60, s52, 1\n"
"s_lshl_b32 s61, s63, 1\n"
"s_add_u32 s61, s61, s60\n"
"s_mul_i32 s61, s61, 10240\n"
"v_mul_u32_u24 v86, 1280, v61\n"
"s_lshl_b32 s60, s60, 6\n"
"v_xor_b32 v38, s60, v64\n"
"v_add3_u32 v38, v38, v86, s61\n"
"s_cmp_ge_u32 s52, 2\n"
"s_cselect_b64 s[12:13], s[16:17], s[12:13]\n"
"s_mul_i32 s66, s68, 1536\n"
"s_add_u32 m0, s57, 0\n"
"s_nop 0\n"
"buffer_load_dwordx4 v34, s[20:23], s66 offen offset:0 lds\n"
"buffer_load_dwordx4 v35, s[20:23], s66 offen offset:1024 lds\n"
"buffer_load_dwordx4 v36, s[24:27], s66 offen offset:2048 lds\n"
"buffer_load_dwordx4 v37, s[24:27], s66 offen offset:3072 lds\n"
"s_add_u32 m0, s58, 16384\n"
"s_nop 0\n"
"buffer_load_dwordx4 v38, s[12:15], s68 offen lds\n"
"v_mov_b32 v4, 0\n"
"s_mov_b32 s81, 0x1708000\n"
"buffer_load_dword v7, v4, s[40:43], s81 offen sc1\n"
"s_lshl_b32 s4, s53, 2\n"
"s_add_u32 s4, s4, s54\n"
"s_lshl_b32 s4, s4, 2\n"
"s_add_u32 s4, s4, s55\n"
"s_lshl_b32 s4, s4, 2\n"
"s_add_u32 s4, s4, s52\n"
"s_mul_i32 s67, s4, 2560\n"
"v_lshrrev_b32 v214, 5, v2\n"
"v_and_b32 v215, 31, v2\n"
"v_mul_u32_u24 v212, 1280, v214\n"
"v_lshl_add_u32 v212, v215, 3, v212\n"
"buffer_load_dwordx2 v[192:193], v212, s[8:11], s67 offen offset:0\n"
"buffer_load_dwordx2 v[194:195], v212, s[8:11], s67 offen offset:256\n"
"buffer_load_dwordx2 v[196:197], v212, s[8:11], s67 offen offset:512\n"
"buffer_load_dwordx2 v[198:199], v212, s[8:11], s67 offen offset:768\n"
"buffer_load_dwordx2 v[200:201], v212, s[8:11], s67 offen offset:1024\n"
"s_add_u32 s69, s68, 128\n"
"s_cmp_eq_u32 s69, 1280\n"
"s_cselect_b32 s69, 0, s69\n"
"s_add_u32 s70, s69, 128\n"
"s_cmp_eq_u32 s70, 1280\n"
"s_cselect_b32 s70, 0, s70\n"
"s_add_u32 m0, s57, 118784\n"
"s_nop 0\n"
"buffer_load_dwordx4 v30, s[8:11], s69 offen offset:0 lds\n"
"s_add_u32 s71, s70, 128\n"
"s_cmp_eq_u32 s71, 1280\n"
"s_cselect_b32 s71, 0, s71\n"
"s_mov_b32 s100, 0xffff0000\n"
"v_and_b32 v61, 31, v2\n"
"v_lshrrev_b32 v62, 5, v2\n"
"buffer_load_dwordx4 v31, s[8:11], s69 offen offset:1024 lds\n"
"v_bfe_u32 v64, v2, 1, 3\n"
"v_xor_b32 v64, v64, v62\n"
"v_lshlrev_b32 v64, 4, v64\n"
"v_lshl_add_u32 v10, v61, 7, v64\n"
"v_xor_b32 v11, 32, v10\n"
"v_xor_b32 v12, 64, v10\n"
"buffer_load_dwordx4 v32, s[8:11], s69 offen offset:2048 lds\n"
"v_xor_b32 v13, 96, v10\n"
"s_add_u32 s60, s57, 102400\n"
"v_add_u32 v26, s60, v10\n"
"v_add_u32 v27, s60, v11\n"
"v_add_u32 v28, s60, v12\n"
"v_add_u32 v29, s60, v13\n"
"buffer_load_dwordx4 v33, s[8:11], s69 offen offset:3072 lds\n"
"v_bfe_u32 v64, v2, 2, 2\n"
"v_and_b32 v65, 1, v2\n"
"v_lshlrev_b32 v65, 4, v65\n"
"v_and_b32 v86, 2, v2\n"
"v_lshl_or_b32 v65, v86, 2, v65\n"
"v_and_b32 v86, 1, v63\n"
"s_mul_i32 s66, s69, 1536\n"
"s_add_u32 m0, s57, 20480\n"
"s_nop 0\n"
"buffer_load_dwordx4 v34, s[20:23], s66 offen offset:0 lds\n"
"v_lshl_add_u32 v87, v62, 3, v64\n"
"v_lshlrev_b32 v18, 7, v87\n"
"v_lshl_add_u32 v18, v86, 11, v18\n"
"v_add_u32 v18, v18, v65\n"
"v_lshrrev_b32 v61, 1, v64\n"
"v_lshl_or_b32 v61, v86, 1, v61\n"
"buffer_load_dwordx4 v35, s[20:23], s66 offen offset:1024 lds\n"
"v_lshl_add_u32 v18, v61, 5, v18\n"
"v_xor_b32 v19, 32, v18\n"
"v_xor_b32 v20, 64, v18\n"
"v_xor_b32 v21, 96, v18\n"
"s_lshl_b32 s61, s52, 5\n"
"v_xor_b32 v8, s61, v18\n"
"buffer_load_dwordx4 v36, s[24:27], s66 offen offset:2048 lds\n"
"v_lshrrev_b32 v61, 2, v2\n"
"v_xor_b32 v61, v61, v63\n"
"v_and_b32 v61, 1, v61\n"
"v_and_or_b32 v61, v2, 3, v61\n"
"v_cmp_eq_u32 vcc, 0, v61\n"
"s_mov_b32 s62, 0x3f803f80\n"
"buffer_load_dwordx4 v37, s[24:27], s66 offen offset:3072 lds\n"
"v_mov_b32 v61, s62\n"
"v_cndmask_b32 v61, 0, v61, vcc\n"
"v_accvgpr_write_b32 a244, v61\n"
"v_accvgpr_write_b32 a245, v61\n"
"v_accvgpr_write_b32 a246, v61\n"
"v_accvgpr_write_b32 a247, v61\n"
"s_add_u32 m0, s58, 36864\n"
"s_nop 0\n"
"buffer_load_dwordx4 v38, s[12:15], s69 offen lds\n"
"s_waitcnt vmcnt(20)\n"
"ds_read_b128 a[0:3], v26 offset:0\n"
"ds_read_b128 a[4:7], v27 offset:0\n"
"ds_read_b128 a[8:11], v28 offset:0\n"
"ds_read_b128 a[12:15], v29 offset:0\n"
"s_waitcnt vmcnt(15)\n"
"s_barrier\n"
"ds_read_b128 a[160:163], v10 offset:16384\n"
"ds_read_b64_tr_b16 a[164:165], v18 offset:0\n"
"ds_read_b64_tr_b16 a[166:167], v18 offset:512\n"
"ds_read_b64_tr_b16 a[168:169], v19 offset:0\n"
"ds_read_b64_tr_b16 a[170:171], v19 offset:512\n"
"ds_read_b64_tr_b16 a[172:173], v8 offset:0\n"
"ds_read_b64_tr_b16 a[174:175], v8 offset:512\n"
"s_waitcnt lgkmcnt(6)\n"
"v_mfma_f32_32x32x16_bf16 v[176:191], a[0:3], a[160:163], 0\n"
"s_waitcnt lgkmcnt(4)\n"
"ds_read_b64_tr_b16 a[176:177], v20 offset:0\n"
"ds_read_b64_tr_b16 a[178:179], v20 offset:512\n"
"s_add_u32 m0, s57, 135168\n"
"s_nop 0\n"
"buffer_load_dwordx4 v30, s[8:11], s70 offen offset:0 lds\n"
"v_and_b32 v106, 15, v2\n"
"v_mfma_f32_16x16x32_bf16 v[236:239], a[244:247], a[160:163], 0\n"
"ds_read_b64_tr_b16 a[180:181], v21 offset:0\n"
"ds_read_b64_tr_b16 a[182:183], v21 offset:512\n"
"v_mfma_f32_32x32x16_bf16 v[112:127], a[164:167], a[0:3], 0\n"
"s_waitcnt lgkmcnt(4)\n"
"ds_read_b128 a[184:187], v11 offset:16384\n"
"buffer_load_dwordx4 v31, s[8:11], s70 offen offset:1024 lds\n"
"v_lshlrev_b32 v106, 1, v106\n"
"s_lshl_b32 s60, s63, 5\n"
"v_mfma_f32_32x32x16_bf16 v[128:143], a[168:171], a[0:3], 0\n"
"buffer_load_ushort v107, v106, s[48:51], s60 offen\n"
"s_mov_b64 s[48:49], s[72:73]\n"
"v_mfma_f32_16x16x32_bf16 a[248:251], a[244:247], a[172:175], 0\n"
"s_waitcnt lgkmcnt(1)\n"
"ds_read_b64_tr_b16 a[188:189], v18 offset:4096\n"
"ds_read_b64_tr_b16 a[190:191], v18 offset:4608\n"
"v_mfma_f32_32x32x16_bf16 v[144:159], a[176:179], a[0:3], 0\n"
"ds_read_b64_tr_b16 a[160:161], v19 offset:4096\n"
"ds_read_b64_tr_b16 a[162:163], v19 offset:4608\n"
"buffer_load_ushort v108, v106, s[48:51], s60 offen\n"
"s_mov_b64 s[48:49], s[74:75]\n"
"buffer_load_ushort v109, v106, s[48:51], s60 offen\n"
"v_mfma_f32_32x32x16_bf16 v[160:175], a[180:183], a[0:3], 0\n"
"s_waitcnt lgkmcnt(4)\n"
"ds_read_b64_tr_b16 a[164:165], v8 offset:4096\n"
"ds_read_b64_tr_b16 a[166:167], v8 offset:4608\n"
"s_mov_b64 s[48:49], s[76:77]\n"
"s_add_u32 s72, s71, 128\n"
"s_cmp_eq_u32 s72, 1280\n"
"s_cselect_b32 s72, 0, s72\n"
"v_mfma_f32_32x32x16_bf16 v[176:191], a[4:7], a[184:187], v[176:191]\n"
"ds_read_b64_tr_b16 a[168:169], v20 offset:4096\n"
"ds_read_b64_tr_b16 a[170:171], v20 offset:4608\n"
"s_add_u32 s73, s72, 128\n"
"s_cmp_eq_u32 s73, 1280\n"
"s_cselect_b32 s73, 0, s73\n"
"s_add_u32 s74, s73, 128\n"
"buffer_load_dwordx4 v32, s[8:11], s70 offen offset:2048 lds\n"
"v_mfma_f32_16x16x32_bf16 v[236:239], a[244:247], a[184:187], v[236:239]\n"
"s_waitcnt lgkmcnt(4)\n"
"ds_read_b64_tr_b16 a[172:173], v21 offset:4096\n"
"ds_read_b64_tr_b16 a[174:175], v21 offset:4608\n"
"v_mfma_f32_32x32x16_bf16 v[112:127], a[188:191], a[4:7], v[112:127]\n"
"ds_read_b128 a[176:179], v12 offset:16384\n"
"s_cmp_eq_u32 s74, 1280\n"
"s_cselect_b32 s74, 0, s74\n"
"s_add_u32 s75, s74, 128\n"
"s_cmp_eq_u32 s75, 1280\n"
"s_cselect_b32 s75, 0, s75\n"
"buffer_load_dwordx4 v33, s[8:11], s70 offen offset:3072 lds\n"
"v_mfma_f32_32x32x16_bf16 v[128:143], a[160:163], a[4:7], v[128:143]\n"
"s_waitcnt lgkmcnt(3)\n"
"s_add_u32 s76, s75, 128\n"
"s_cmp_eq_u32 s76, 1280\n"
"s_cselect_b32 s76, 0, s76\n"
"s_add_u32 s77, s76, 128\n"
"s_cmp_eq_u32 s77, 1280\n"
"s_cselect_b32 s77, 0, s77\n"
"s_mul_i32 s66, s70, 1536\n"
"v_mfma_f32_16x16x32_bf16 a[248:251], a[244:247], a[164:167], a[248:251]\n"
"ds_read_b64_tr_b16 a[180:181], v18 offset:8192\n"
"ds_read_b64_tr_b16 a[182:183], v18 offset:8704\n"
"s_add_u32 m0, s57, 40960\n"
"v_mfma_f32_32x32x16_bf16 v[144:159], a[168:171], a[4:7], v[144:159]\n"
"s_waitcnt lgkmcnt(2)\n"
"ds_read_b64_tr_b16 a[184:185], v19 offset:8192\n"
"ds_read_b64_tr_b16 a[186:187], v19 offset:8704\n"
"buffer_load_dwordx4 v34, s[20:23], s66 offen offset:0 lds\n"
"v_mfma_f32_32x32x16_bf16 v[160:175], a[172:175], a[4:7], v[160:175]\n"
"ds_read_b64_tr_b16 a[188:189], v8 offset:8192\n"
"ds_read_b64_tr_b16 a[190:191], v8 offset:8704\n"
"v_mfma_f32_32x32x16_bf16 v[176:191], a[8:11], a[176:179], v[176:191]\n"
"s_waitcnt lgkmcnt(4)\n"
"ds_read_b64_tr_b16 a[160:161], v20 offset:8192\n"
"ds_read_b64_tr_b16 a[162:163], v20 offset:8704\n"
"buffer_load_dwordx4 v35, s[20:23], s66 offen offset:1024 lds\n"
"v_mfma_f32_16x16x32_bf16 v[236:239], a[244:247], a[176:179], v[236:239]\n"
"ds_read_b64_tr_b16 a[164:165], v21 offset:8192\n"
"ds_read_b64_tr_b16 a[166:167], v21 offset:8704\n"
"v_mfma_f32_32x32x16_bf16 v[112:127], a[180:183], a[8:11], v[112:127]\n"
"s_waitcnt lgkmcnt(4)\n"
"ds_read_b128 a[168:171], v13 offset:16384\n"
"buffer_load_dwordx4 v36, s[24:27], s66 offen offset:2048 lds\n"
"v_mfma_f32_32x32x16_bf16 v[128:143], a[184:187], a[8:11], v[128:143]\n"
"v_mfma_f32_16x16x32_bf16 a[248:251], a[244:247], a[188:191], a[248:251]\n"
"s_waitcnt lgkmcnt(1)\n"
"ds_read_b64_tr_b16 a[172:173], v18 offset:12288\n"
"ds_read_b64_tr_b16 a[174:175], v18 offset:12800\n"
"v_mfma_f32_32x32x16_bf16 v[144:159], a[160:163], a[8:11], v[144:159]\n"
"ds_read_b64_tr_b16 a[176:177], v19 offset:12288\n"
"ds_read_b64_tr_b16 a[178:179], v19 offset:12800\n"
"buffer_load_dwordx4 v37, s[24:27], s66 offen offset:3072 lds\n"
"s_add_u32 m0, s58, 57344\n"
"s_mov_b32 s80, 0x3f803f80\n"
"s_waitcnt vmcnt(25)\n"
"v_readfirstlane_b32 s56, v7\n"
"v_mov_b32 v216, 0\n"
"v_mov_b32 v217, 0\n"
"s_add_u32 s56, s56, 1\n"
"v_mov_b32 v7, s56\n"
"s_waitcnt vmcnt(24)\n"
"v_dot2c_f32_bf16 v216, s80, v192\n"
"v_dot2c_f32_bf16 v217, v192, v192\n"
"v_dot2c_f32_bf16 v216, s80, v193\n"
"v_dot2c_f32_bf16 v217, v193, v193\n"
"s_waitcnt vmcnt(23)\n"
"v_dot2c_f32_bf16 v216, s80, v194\n"
"v_dot2c_f32_bf16 v217, v194, v194\n"
"v_dot2c_f32_bf16 v216, s80, v195\n"
"v_dot2c_f32_bf16 v217, v195, v195\n"
"s_waitcnt vmcnt(22)\n"
"v_dot2c_f32_bf16 v216, s80, v196\n"
"v_dot2c_f32_bf16 v217, v196, v196\n"
"v_dot2c_f32_bf16 v216, s80, v197\n"
"v_dot2c_f32_bf16 v217, v197, v197\n"
"s_waitcnt vmcnt(21)\n"
"v_dot2c_f32_bf16 v216, s80, v198\n"
"v_dot2c_f32_bf16 v217, v198, v198\n"
"v_dot2c_f32_bf16 v216, s80, v199\n"
"v_dot2c_f32_bf16 v217, v199, v199\n"
"s_waitcnt vmcnt(20)\n"
"v_dot2c_f32_bf16 v216, s80, v200\n"
"v_dot2c_f32_bf16 v217, v200, v200\n"
"v_dot2c_f32_bf16 v216, s80, v201\n"
"v_dot2c_f32_bf16 v217, v201, v201\n"
"v_mfma_f32_32x32x16_bf16 v[160:175], a[164:167], a[8:11], v[160:175]\n"
"s_waitcnt lgkmcnt(4)\n"
"s_waitcnt vmcnt(16)\n"
"ds_read_b128 a[16:19], v26 offset:16384\n"
"ds_read_b128 a[20:23], v27 offset:16384\n"
"ds_read_b64_tr_b16 a[180:181], v8 offset:12288\n"
"ds_read_b64_tr_b16 a[182:183], v8 offset:12800\n"
"buffer_load_dwordx4 v38, s[12:15], s70 offen lds\n"
"v_mfma_f32_32x32x16_bf16 v[176:191], a[12:15], a[168:171], v[176:191]\n"
"ds_read_b128 a[24:27], v28 offset:16384\n"
"ds_read_b128 a[28:31], v29 offset:16384\n"
"ds_read_b64_tr_b16 a[184:185], v20 offset:12288\n"
"ds_read_b64_tr_b16 a[186:187], v20 offset:12800\n"
"s_add_u32 m0, s57, 102400\n"
"v_mov_b32 v193, s56\n"
"v_mfma_f32_16x16x32_bf16 v[236:239], a[244:247], a[168:171], v[236:239]\n"
"s_waitcnt vmcnt(12)\n"
"s_barrier\n"
"s_waitcnt lgkmcnt(8)\n"
"ds_read_b64_tr_b16 a[188:189], v21 offset:12288\n"
"ds_read_b64_tr_b16 a[190:191], v21 offset:12800\n"
"v_mfma_f32_32x32x16_bf16 v[112:127], a[172:175], a[12:15], v[112:127]\n"
"ds_read_b128 a[160:163], v10 offset:36864\n"
"buffer_load_dwordx4 v30, s[8:11], s71 offen offset:0 lds\n"
"v_mov_b32 v194, 0x70000000\n"
"v_cmp_eq_u32 vcc, 0, v215\n"
"v_add_f32_dpp v216, v216, v216 quad_perm:[1,0,3,2] row_mask:0xf bank_mask:0xf\n"
"v_mfma_f32_32x32x16_bf16 v[128:143], a[176:179], a[12:15], v[128:143]\n"
"s_waitcnt lgkmcnt(3)\n"
"v_add_f32_dpp v217, v217, v217 quad_perm:[1,0,3,2] row_mask:0xf bank_mask:0xf\n"
"v_add_f32_dpp v216, v216, v216 quad_perm:[2,3,0,1] row_mask:0xf bank_mask:0xf\n"
"s_nop 0\n"
"v_add_f32_dpp v217, v217, v217 quad_perm:[2,3,0,1] row_mask:0xf bank_mask:0xf\n"
"v_add_f32_dpp v216, v216, v216 row_half_mirror row_mask:0xf bank_mask:0xf\n"
"s_nop 0\n"
"v_add_f32_dpp v217, v217, v217 row_half_mirror row_mask:0xf bank_mask:0xf\n"
"v_mfma_f32_16x16x32_bf16 a[248:251], a[244:247], a[180:183], a[248:251]\n"
"ds_read_b64_tr_b16 a[164:165], v18 offset:20480\n"
"ds_read_b64_tr_b16 a[166:167], v18 offset:20992\n"
"buffer_load_dwordx4 v31, s[8:11], s71 offen offset:1024 lds\n"
"v_mfma_f32_32x32x16_bf16 v[144:159], a[184:187], a[12:15], v[144:159]\n"
"s_waitcnt lgkmcnt(2)\n"
"ds_read_b64_tr_b16 a[168:169], v19 offset:20480\n"
"ds_read_b64_tr_b16 a[170:171], v19 offset:20992\n"
"v_add_f32_dpp v216, v216, v216 row_mirror row_mask:0xf bank_mask:0xf\n"
"v_add_f32_dpp v217, v217, v217 row_mirror row_mask:0xf bank_mask:0xf\n"
"v_mov_b32 v196, v216\n"
"v_mov_b32 v197, v217\n"
"v_mfma_f32_32x32x16_bf16 v[160:175], a[188:191], a[12:15], v[160:175]\n"
"ds_read_b64_tr_b16 a[172:173], v8 offset:20480\n"
"ds_read_b64_tr_b16 a[174:175], v8 offset:20992\n"
"buffer_load_dwordx4 v32, s[8:11], s71 offen offset:2048 lds\n"
"v_lshlrev_b32 v214, 3, v214\n"
"v_cndmask_b32 v214, v194, v214, vcc\n"
"v_mfma_f32_32x32x16_bf16 v[176:191], a[16:19], a[160:163], v[176:191]\n"
"s_waitcnt lgkmcnt(4)\n"
"ds_read_b64_tr_b16 a[176:177], v20 offset:20480\n"
"ds_read_b64_tr_b16 a[178:179], v20 offset:20992\n"
"v_permlane16_swap_b32 v216, v196\n"
"v_permlane16_swap_b32 v217, v197\n"
"v_add_f32 v216, v216, v196\n"
"v_add_f32 v217, v217, v197\n"
"v_mfma_f32_16x16x32_bf16 v[236:239], a[244:247], a[160:163], v[236:239]\n"
"ds_read_b64_tr_b16 a[180:181], v21 offset:20480\n"
"ds_read_b64_tr_b16 a[182:183], v21 offset:20992\n"
"buffer_load_dwordx4 v33, s[8:11], s71 offen offset:3072 lds\n"
"v_mfma_f32_32x32x16_bf16 v[112:127], a[164:167], a[16:19], v[112:127]\n"
"s_waitcnt lgkmcnt(4)\n"
"ds_read_b128 a[184:187], v11 offset:36864\n"
"s_mul_i32 s66, s71, 1536\n"
"s_add_u32 m0, s57, 61440\n"
"v_mul_f32 v216, 0x3acccccd, v216\n"
"v_mul_f32 v217, 0x3acccccd, v217\n"
"v_mfma_f32_32x32x16_bf16 v[128:143], a[168:171], a[16:19], v[128:143]\n"
"buffer_load_dwordx4 v34, s[20:23], s66 offen offset:0 lds\n"
"v_fma_f32 v217, -v216, v216, v217\n"
"v_max_f32 v217, 0, v217\n"
"v_cvt_pk_bf16_f32 v217, v217, v217\n"
"v_and_b32 v217, s100, v217\n"
"v_mfma_f32_16x16x32_bf16 a[248:251], a[244:247], a[172:175], a[248:251]\n"
"s_waitcnt lgkmcnt(1)\n"
"ds_read_b64_tr_b16 a[188:189], v18 offset:24576\n"
"ds_read_b64_tr_b16 a[190:191], v18 offset:25088\n"
"v_mfma_f32_32x32x16_bf16 v[144:159], a[176:179], a[16:19], v[144:159]\n"
"ds_read_b64_tr_b16 a[160:161], v19 offset:24576\n"
"ds_read_b64_tr_b16 a[162:163], v19 offset:25088\n"
"buffer_load_dwordx4 v35, s[20:23], s66 offen offset:1024 lds\n"
"v_add_f32 v217, 0x3727c5ac, v217\n"
"v_cvt_pk_bf16_f32 v217, v217, v217\n"
"v_mfma_f32_32x32x16_bf16 v[160:175], a[180:183], a[16:19], v[160:175]\n"
"s_waitcnt lgkmcnt(4)\n"
"ds_read_b64_tr_b16 a[164:165], v8 offset:24576\n"
"ds_read_b64_tr_b16 a[166:167], v8 offset:25088\n"
"v_and_b32 v217, s100, v217\n"
"v_sqrt_f32 v217, v217\n"
"s_lshl_b32 s60, s4, 4\n"
"v_mfma_f32_32x32x16_bf16 v[176:191], a[20:23], a[184:187], v[176:191]\n"
"ds_read_b64_tr_b16 a[168:169], v20 offset:24576\n"
"ds_read_b64_tr_b16 a[170:171], v20 offset:25088\n"
"buffer_load_dwordx4 v36, s[24:27], s66 offen offset:2048 lds\n"
"s_add_u32 s60, s60, 0x1700000\n"
"v_cvt_pk_bf16_f32 v192, v216, v217\n"
"v_mfma_f32_16x16x32_bf16 v[236:239], a[244:247], a[184:187], v[236:239]\n"
"s_waitcnt lgkmcnt(4)\n"
"ds_read_b64_tr_b16 a[172:173], v21 offset:24576\n"
"ds_read_b64_tr_b16 a[174:175], v21 offset:25088\n"
"v_mfma_f32_32x32x16_bf16 v[112:127], a[188:191], a[20:23], v[112:127]\n"
"ds_read_b128 a[176:179], v12 offset:36864\n"
"buffer_store_dwordx2 v[192:193], v214, s[40:43], s60 offen sc1\n"
"v_add_u32 v14, 61440, v10\n"
"v_add_u32 v15, 61440, v11\n"
"v_mfma_f32_32x32x16_bf16 v[128:143], a[160:163], a[20:23], v[128:143]\n"
"s_waitcnt lgkmcnt(3)\n"
"buffer_load_dwordx4 v37, s[24:27], s66 offen offset:3072 lds\n"
"s_add_u32 m0, s58, 77824\n"
"v_add_u32 v16, 61440, v12\n"
"v_add_u32 v17, 61440, v13\n"
"s_bitcmp1_b32 s101, 0\n"
"s_cselect_b64 vcc, 0, -1\n"
"v_mfma_f32_16x16x32_bf16 a[248:251], a[244:247], a[164:167], a[248:251]\n"
"ds_read_b64_tr_b16 a[180:181], v18 offset:28672\n"
"ds_read_b64_tr_b16 a[182:183], v18 offset:29184\n"
"v_mfma_f32_32x32x16_bf16 v[144:159], a[168:171], a[20:23], v[144:159]\n"
"s_waitcnt lgkmcnt(2)\n"
"ds_read_b64_tr_b16 a[184:185], v19 offset:28672\n"
"ds_read_b64_tr_b16 a[186:187], v19 offset:29184\n"
"buffer_load_dwordx4 v38, s[12:15], s71 offen lds\n"
"s_add_u32 m0, s57, 118784\n"
"v_mfma_f32_32x32x16_bf16 v[160:175], a[172:175], a[20:23], v[160:175]\n"
"ds_read_b64_tr_b16 a[188:189], v8 offset:28672\n"
"ds_read_b64_tr_b16 a[190:191], v8 offset:29184\n"
"v_cndmask_b32 v61, 0, v61, vcc\n"
"v_accvgpr_write_b32 a252, v61\n"
"v_accvgpr_write_b32 a253, v61\n"
"v_accvgpr_write_b32 a254, v61\n"
"v_mfma_f32_32x32x16_bf16 v[176:191], a[24:27], a[176:179], v[176:191]\n"
"s_waitcnt lgkmcnt(4)\n"
"ds_read_b64_tr_b16 a[160:161], v20 offset:28672\n"
"ds_read_b64_tr_b16 a[162:163], v20 offset:29184\n"
"buffer_load_dwordx4 v30, s[8:11], s72 offen offset:0 lds\n"
"v_accvgpr_write_b32 a255, v61\n"
"v_add_u32 v22, 61440, v18\n"
"v_mfma_f32_16x16x32_bf16 v[236:239], a[244:247], a[176:179], v[236:239]\n"
"ds_read_b64_tr_b16 a[164:165], v21 offset:28672\n"
"ds_read_b64_tr_b16 a[166:167], v21 offset:29184\n"
"v_mfma_f32_32x32x16_bf16 v[112:127], a[180:183], a[24:27], v[112:127]\n"
"s_waitcnt lgkmcnt(4)\n"
"ds_read_b128 a[168:171], v13 offset:36864\n"
"buffer_load_dwordx4 v31, s[8:11], s72 offen offset:1024 lds\n"
"v_add_u32 v23, 61440, v19\n"
"v_add_u32 v24, 61440, v20\n"
"v_add_u32 v25, 61440, v21\n"
"v_mfma_f32_32x32x16_bf16 v[128:143], a[184:187], a[24:27], v[128:143]\n"
"v_add_u32 v9, 61440, v8\n"
"v_lshlrev_b32 v44, 4, v2\n"
"v_and_b32 v61, 31, v2\n"
"v_and_b32 v88, 0xfffffff3, v61\n"
"v_and_b32 v60, 4, v61\n"
"v_and_b32 v89, 8, v61\n"
"v_mfma_f32_16x16x32_bf16 a[248:251], a[244:247], a[188:191], a[248:251]\n"
"s_waitcnt lgkmcnt(1)\n"
"ds_read_b64_tr_b16 a[172:173], v18 offset:32768\n"
"ds_read_b64_tr_b16 a[174:175], v18 offset:33280\n"
"v_mfma_f32_32x32x16_bf16 v[144:159], a[160:163], a[24:27], v[144:159]\n"
"ds_read_b64_tr_b16 a[176:177], v19 offset:32768\n"
"ds_read_b64_tr_b16 a[178:179], v19 offset:33280\n"
"buffer_load_dwordx4 v32, s[8:11], s72 offen offset:2048 lds\n"
"v_lshl_or_b32 v88, v60, 1, v88\n"
"v_lshrrev_b32 v89, 1, v89\n"
"v_mfma_f32_32x32x16_bf16 v[160:175], a[164:167], a[24:27], v[160:175]\n"
"s_waitcnt lgkmcnt(4)\n"
"s_waitcnt vmcnt(18)\n"
"ds_read_b128 a[32:35], v26 offset:32768\n"
"ds_read_b128 a[36:39], v27 offset:32768\n"
"ds_read_b64_tr_b16 a[180:181], v8 offset:32768\n"
"ds_read_b64_tr_b16 a[182:183], v8 offset:33280\n"
"v_or_b32 v88, v88, v89\n"
"v_mfma_f32_32x32x16_bf16 v[176:191], a[28:31], a[168:171], v[176:191]\n"
"ds_read_b128 a[40:43], v28 offset:32768\n"
"ds_read_b128 a[44:47], v29 offset:32768\n"
"ds_read_b64_tr_b16 a[184:185], v20 offset:32768\n"
"ds_read_b64_tr_b16 a[186:187], v20 offset:33280\n"
"buffer_load_dwordx4 v33, s[8:11], s72 offen offset:3072 lds\n"
"s_mul_i32 s66, s72, 1536\n"
"v_mfma_f32_16x16x32_bf16 v[236:239], a[244:247], a[168:171], v[236:239]\n"
"s_waitcnt vmcnt(14)\n"
"s_barrier\n"
"s_waitcnt lgkmcnt(8)\n"
"ds_read_b64_tr_b16 a[188:189], v21 offset:32768\n"
"ds_read_b64_tr_b16 a[190:191], v21 offset:33280\n"
"v_mfma_f32_32x32x16_bf16 v[112:127], a[172:175], a[28:31], v[112:127]\n"
"ds_read_b128 a[160:163], v10 offset:57344\n"
"s_add_u32 m0, s57, 81920\n"
"s_nop 0\n"
"buffer_load_dwordx4 v34, s[20:23], s66 offen offset:0 lds\n"
"v_lshlrev_b32 v60, 3, v88\n"
"s_sub_u32 s32, s32, 3072\n"
"s_subb_u32 s33, s33, 0\n"
"v_mfma_f32_32x32x16_bf16 v[128:143], a[176:179], a[28:31], v[128:143]\n"
"s_waitcnt lgkmcnt(3)\n"
"s_sub_u32 s36, s36, 3072\n"
"s_subb_u32 s37, s37, 0\n"
"s_mul_i32 s59, s52, 5120\n"
"s_mul_i32 s60, s52, 1280\n"
"s_add_u32 s60, s60, 152576\n"
"v_mfma_f32_16x16x32_bf16 a[248:251], a[244:247], a[180:183], a[248:251]\n"
"ds_read_b64_tr_b16 a[164:165], v18 offset:40960\n"
"ds_read_b64_tr_b16 a[166:167], v18 offset:41472\n"
"buffer_load_dwordx4 v35, s[20:23], s66 offen offset:1024 lds\n"
"v_mfma_f32_32x32x16_bf16 v[144:159], a[184:187], a[28:31], v[144:159]\n"
"s_waitcnt lgkmcnt(2)\n"
"ds_read_b64_tr_b16 a[168:169], v19 offset:40960\n"
"ds_read_b64_tr_b16 a[170:171], v19 offset:41472\n"
"v_mul_u32_u24 v81, 80, v87\n"
"v_lshl_add_u32 v81, v86, 5, v81\n"
"v_add3_u32 v81, v81, v65, s60\n"
"v_mfma_f32_32x32x16_bf16 v[160:175], a[188:191], a[28:31], v[160:175]\n"
"ds_read_b64_tr_b16 a[172:173], v8 offset:40960\n"
"ds_read_b64_tr_b16 a[174:175], v8 offset:41472\n"
"buffer_load_dwordx4 v36, s[24:27], s66 offen offset:2048 lds\n"
"v_mul_u32_u24 v87, 320, v87\n"
"v_lshl_add_u32 v87, v86, 5, v87\n"
"v_mfma_f32_32x32x16_bf16 v[176:191], a[32:35], a[160:163], v[176:191]\n"
"s_waitcnt lgkmcnt(4)\n"
"ds_read_b64_tr_b16 a[176:177], v20 offset:40960\n"
"ds_read_b64_tr_b16 a[178:179], v20 offset:41472\n"
"v_and_b32 v65, 3, v2\n"
"v_lshl_add_u32 v87, v65, 3, v87\n"
"v_accvgpr_write_b32 a198, v87\n"
"v_mfma_f32_16x16x32_bf16 v[236:239], a[252:255], a[160:163], v[236:239]\n"
"ds_read_b64_tr_b16 a[180:181], v21 offset:40960\n"
"ds_read_b64_tr_b16 a[182:183], v21 offset:41472\n"
"buffer_load_dwordx4 v37, s[24:27], s66 offen offset:3072 lds\n"
"v_mfma_f32_32x32x16_bf16 v[112:127], a[164:167], a[32:35], v[112:127]\n"
"s_waitcnt lgkmcnt(4)\n"
"ds_read_b128 a[184:187], v11 offset:57344\n"
"s_add_u32 m0, s58, 98304\n"
"v_lshl_add_u32 v61, v86, 1, v62\n"
"v_and_b32 v64, 15, v2\n"
"v_mul_u32_u24 v45, 80, v64\n"
"v_mfma_f32_32x32x16_bf16 v[128:143], a[168:171], a[32:35], v[128:143]\n"
"buffer_load_dwordx4 v38, s[12:15], s72 offen lds\n"
"s_add_u32 m0, s57, 135168\n"
"v_lshl_add_u32 v45, v61, 4, v45\n"
"v_add_u32 v45, s60, v45\n"
"v_mul_u32_u24 v65, 144, v64\n"
"v_mfma_f32_16x16x32_bf16 a[248:251], a[252:255], a[172:175], a[248:251]\n"
"s_waitcnt lgkmcnt(1)\n"
"ds_read_b64_tr_b16 a[188:189], v18 offset:45056\n"
"ds_read_b64_tr_b16 a[190:191], v18 offset:45568\n"
"v_mfma_f32_32x32x16_bf16 v[144:159], a[176:179], a[32:35], v[144:159]\n"
"ds_read_b64_tr_b16 a[160:161], v19 offset:45056\n"
"ds_read_b64_tr_b16 a[162:163], v19 offset:45568\n"
"buffer_load_dwordx4 v30, s[8:11], s73 offen offset:0 lds\n"
"s_lshl_b32 s60, s52, 5\n"
"s_add_u32 s60, s60, 143360\n"
"v_mfma_f32_32x32x16_bf16 v[160:175], a[180:183], a[32:35], v[160:175]\n"
"s_waitcnt lgkmcnt(4)\n"
"ds_read_b64_tr_b16 a[164:165], v8 offset:45056\n"
"ds_read_b64_tr_b16 a[166:167], v8 offset:45568\n"
"v_lshl_add_u32 v74, v61, 3, v65\n"
"v_add_u32 v74, s60, v74\n"
"s_mov_b32 s60, 143360\n"
"v_mfma_f32_32x32x16_bf16 v[176:191], a[36:39], a[184:187], v[176:191]\n"
"ds_read_b64_tr_b16 a[168:169], v20 offset:45056\n"
"ds_read_b64_tr_b16 a[170:171], v20 offset:45568\n"
"buffer_load_dwordx4 v31, s[8:11], s73 offen offset:1024 lds\n"
"v_lshl_add_u32 v75, v61, 5, v65\n"
"v_add_u32 v75, s60, v75\n"
"v_mfma_f32_16x16x32_bf16 v[236:239], a[252:255], a[184:187], v[236:239]\n"
"s_waitcnt lgkmcnt(4)\n"
"ds_read_b64_tr_b16 a[172:173], v21 offset:45056\n"
"ds_read_b64_tr_b16 a[174:175], v21 offset:45568\n"
"v_mfma_f32_32x32x16_bf16 v[112:127], a[188:191], a[36:39], v[112:127]\n"
"ds_read_b128 a[176:179], v12 offset:57344\n"
"buffer_load_dwordx4 v32, s[8:11], s73 offen offset:2048 lds\n"
"v_cvt_f32_u32 v53, v62\n"
"s_mul_i32 s5, s54, 10\n"
"s_and_b32 s78, s54, 1\n"
"v_mfma_f32_32x32x16_bf16 v[128:143], a[160:163], a[36:39], v[128:143]\n"
"s_waitcnt lgkmcnt(3)\n"
"s_xor_b32 s79, s78, 1\n"
"s_lshl_b32 s79, s79, 3\n"
"s_add_u32 s6, s5, s79\n"
"s_lshl_b32 s79, s78, 1\n"
"s_add_u32 s5, s5, s79\n"
"v_and_b32 v62, 7, v2\n"
"v_mfma_f32_16x16x32_bf16 a[248:251], a[252:255], a[164:167], a[248:251]\n"
"ds_read_b64_tr_b16 a[180:181], v18 offset:49152\n"
"ds_read_b64_tr_b16 a[182:183], v18 offset:49664\n"
"buffer_load_dwordx4 v33, s[8:11], s73 offen offset:3072 lds\n"
"v_mfma_f32_32x32x16_bf16 v[144:159], a[168:171], a[36:39], v[144:159]\n"
"s_waitcnt lgkmcnt(2)\n"
"ds_read_b64_tr_b16 a[184:185], v19 offset:49152\n"
"ds_read_b64_tr_b16 a[186:187], v19 offset:49664\n"
"s_mul_i32 s66, s73, 1536\n"
"s_add_u32 m0, s57, 0\n"
"v_xor_b32 v61, v63, v62\n"
"v_lshlrev_b32 v61, 4, v61\n"
"v_mfma_f32_32x32x16_bf16 v[160:175], a[172:175], a[36:39], v[160:175]\n"
"ds_read_b64_tr_b16 a[188:189], v8 offset:49152\n"
"ds_read_b64_tr_b16 a[190:191], v8 offset:49664\n"
"buffer_load_dwordx4 v34, s[20:23], s66 offen offset:0 lds\n"
"v_lshrrev_b32 v62, 3, v2\n"
"v_mul_u32_u24 v62, 1280, v62\n"
"v_mfma_f32_32x32x16_bf16 v[176:191], a[40:43], a[176:179], v[176:191]\n"
"s_waitcnt lgkmcnt(4)\n"
"ds_read_b64_tr_b16 a[160:161], v20 offset:49152\n"
"ds_read_b64_tr_b16 a[162:163], v20 offset:49664\n"
"s_mul_i32 s60, s52, 5\n"
"s_mul_i32 s62, s55, 20\n"
"s_add_u32 s78, s60, 0\n"
"s_lshl_b32 s79, s54, 2\n"
"v_mfma_f32_16x16x32_bf16 v[236:239], a[252:255], a[176:179], v[236:239]\n"
"ds_read_b64_tr_b16 a[164:165], v21 offset:49152\n"
"ds_read_b64_tr_b16 a[166:167], v21 offset:49664\n"
"buffer_load_dwordx4 v35, s[20:23], s66 offen offset:1024 lds\n"
"v_mfma_f32_32x32x16_bf16 v[112:127], a[180:183], a[40:43], v[112:127]\n"
"s_waitcnt lgkmcnt(4)\n"
"ds_read_b128 a[168:171], v13 offset:57344\n"
"s_add_u32 s79, s79, s78\n"
"s_and_b32 s79, s79, 15\n"
"s_cmp_ge_u32 s78, 16\n"
"s_cselect_b32 s79, s78, s79\n"
"s_add_u32 s79, s79, s62\n"
"v_mfma_f32_32x32x16_bf16 v[128:143], a[184:187], a[40:43], v[128:143]\n"
"buffer_load_dwordx4 v36, s[24:27], s66 offen offset:2048 lds\n"
"s_mul_i32 s79, s79, 10240\n"
"s_add_u32 s79, s79, 3072\n"
"s_and_b32 s78, s78, 1\n"
"s_lshl_b32 s78, s78, 6\n"
"v_mfma_f32_16x16x32_bf16 a[248:251], a[252:255], a[188:191], a[248:251]\n"
"s_waitcnt lgkmcnt(1)\n"
"ds_read_b64_tr_b16 a[172:173], v18 offset:53248\n"
"ds_read_b64_tr_b16 a[174:175], v18 offset:53760\n"
"v_mfma_f32_32x32x16_bf16 v[144:159], a[160:163], a[40:43], v[144:159]\n"
"ds_read_b64_tr_b16 a[176:177], v19 offset:53248\n"
"ds_read_b64_tr_b16 a[178:179], v19 offset:53760\n"
"buffer_load_dwordx4 v37, s[24:27], s66 offen offset:3072 lds\n"
"s_add_u32 m0, s58, 16384\n"
"v_xor_b32 v64, s78, v61\n"
"v_mfma_f32_32x32x16_bf16 v[160:175], a[164:167], a[40:43], v[160:175]\n"
"s_waitcnt lgkmcnt(4)\n"
"s_waitcnt vmcnt(23)\n"
"ds_read_b128 a[48:51], v26 offset:0\n"
"ds_read_b128 a[52:55], v27 offset:0\n"
"ds_read_b64_tr_b16 a[180:181], v8 offset:53248\n"
"ds_read_b64_tr_b16 a[182:183], v8 offset:53760\n"
"v_mfma_f32_32x32x16_bf16 v[176:191], a[44:47], a[168:171], v[176:191]\n"
"ds_read_b128 a[56:59], v28 offset:0\n"
"ds_read_b128 a[60:63], v29 offset:0\n"
"ds_read_b64_tr_b16 a[184:185], v20 offset:53248\n"
"ds_read_b64_tr_b16 a[186:187], v20 offset:53760\n"
"buffer_load_dwordx4 v38, s[12:15], s73 offen lds\n"
"v_mfma_f32_16x16x32_bf16 v[236:239], a[252:255], a[168:171], v[236:239]\n"
"s_waitcnt vmcnt(18)\n"
"s_barrier\n"
"s_waitcnt lgkmcnt(8)\n"
"ds_read_b64_tr_b16 a[188:189], v21 offset:53248\n"
"ds_read_b64_tr_b16 a[190:191], v21 offset:53760\n"
"v_mfma_f32_32x32x16_bf16 v[112:127], a[172:175], a[44:47], v[112:127]\n"
"ds_read_b128 a[160:163], v14 offset:16384\n"
"v_add3_u32 v64, v64, v62, s79\n"
"v_accvgpr_write_b32 a192, v64\n"
"s_add_u32 s78, s60, 1\n"
"s_lshl_b32 s79, s54, 2\n"
"s_add_u32 s79, s79, s78\n"
"v_mfma_f32_32x32x16_bf16 v[128:143], a[176:179], a[44:47], v[128:143]\n"
"s_waitcnt lgkmcnt(3)\n"
"ds_read_b64_tr_b16 a[164:165], v22 offset:0\n"
"ds_read_b64_tr_b16 a[166:167], v22 offset:512\n"
"s_and_b32 s79, s79, 15\n"
"s_cmp_ge_u32 s78, 16\n"
"s_cselect_b32 s79, s78, s79\n"
"s_add_u32 s79, s79, s62\n"
"v_mfma_f32_16x16x32_bf16 a[248:251], a[252:255], a[180:183], a[248:251]\n"
"ds_read_b64_tr_b16 a[168:169], v23 offset:0\n"
"ds_read_b64_tr_b16 a[170:171], v23 offset:512\n"
"v_mfma_f32_32x32x16_bf16 v[144:159], a[184:187], a[44:47], v[144:159]\n"
"s_waitcnt lgkmcnt(4)\n"
"ds_read_b64_tr_b16 a[172:173], v24 offset:0\n"
"ds_read_b64_tr_b16 a[174:175], v24 offset:512\n"
"s_mul_i32 s79, s79, 10240\n"
"s_add_u32 s79, s79, 2048\n"
"s_and_b32 s78, s78, 1\n"
"s_lshl_b32 s78, s78, 6\n"
"v_mfma_f32_32x32x16_bf16 v[160:175], a[188:191], a[44:47], v[160:175]\n"
"ds_read_b64_tr_b16 a[176:177], v25 offset:0\n"
"ds_read_b64_tr_b16 a[178:179], v25 offset:512\n"
"v_accvgpr_read_b32 v76, a248\n"
"v_cmp_gt_u32 s[0:1], 32, v2\n"
"v_and_b32 v78, 31, v2\n"
"v_lshlrev_b32 v78, 4, v78\n"
"v_mfma_f32_32x32x16_bf16 v[176:191], a[48:51], a[160:163], v[176:191]\n"
"s_waitcnt lgkmcnt(4)\n"
"ds_read_b128 a[180:183], v15 offset:16384\n"
"s_lshl_b32 s2, s52, 10\n"
"v_add_u32 v79, s2, v78\n"
"v_cndmask_b32 v76, v236, v76, s[0:1]\n"
"v_add_u32 v78, 4096, v78\n"
"v_cvt_pk_bf16_f32 v77, v76, v76\n"
"v_mfma_f32_32x32x16_bf16 v[112:127], a[164:167], a[48:51], v[112:127]\n"
"ds_read_b64_tr_b16 a[184:185], v22 offset:4096\n"
"ds_read_b64_tr_b16 a[186:187], v22 offset:4608\n"
"v_cndmask_b32 v78, v78, v79, s[0:1]\n"
"v_and_b32 v77, s100, v77\n"
"s_lshl_b32 s3, s54, 2\n"
"s_add_u32 s3, s3, s55\n"
"v_mfma_f32_32x32x16_bf16 v[128:143], a[168:171], a[48:51], v[128:143]\n"
"s_waitcnt lgkmcnt(3)\n"
"ds_read_b64_tr_b16 a[188:189], v23 offset:4096\n"
"ds_read_b64_tr_b16 a[190:191], v23 offset:4608\n"
"s_mul_i32 s3, s3, 10240\n"
"s_add_u32 s3, s3, 0x1680000\n"
"s_add_u32 m0, s57, 102400\n"
"s_nop 0\n"
"buffer_load_dwordx4 v30, s[8:11], s74 offen offset:0 lds\n"
"v_mfma_f32_32x32x16_bf16 v[144:159], a[172:175], a[48:51], v[144:159]\n"
"ds_read_b64_tr_b16 a[160:161], v24 offset:4096\n"
"ds_read_b64_tr_b16 a[162:163], v24 offset:4608\n"
"v_sub_f32 v76, v76, v77\n"
"v_cvt_pk_bf16_f32 v76, v77, v76\n"
"v_mov_b32 v77, s56\n"
"s_lshr_b32 s2, s101, 1\n"
"v_mfma_f32_32x32x16_bf16 v[160:175], a[176:179], a[48:51], v[160:175]\n"
"s_waitcnt lgkmcnt(4)\n"
"ds_read_b64_tr_b16 a[164:165], v25 offset:4096\n"
"ds_read_b64_tr_b16 a[166:167], v25 offset:4608\n"
"s_lshl_b32 s2, s2, 9\n"
"s_and_b32 s7, s101, 1\n"
"s_lshl_b32 s7, s7, 3\n"
"buffer_load_dwordx4 v31, s[8:11], s74 offen offset:1024 lds\n"
"v_mfma_f32_32x32x16_bf16 v[176:191], a[52:55], a[180:183], v[176:191]\n"
"ds_read_b128 a[168:171], v16 offset:16384\n"
"s_add_u32 s2, s2, s7\n"
"s_add_u32 s2, s2, s3\n"
"s_cmp_lt_u32 s101, 4\n"
"s_cselect_b32 s2, s2, 0x60000000\n"
"buffer_store_dwordx2 v[76:77], v78, s[40:43], s2 offen sc1\n"
"v_mfma_f32_32x32x16_bf16 v[112:127], a[184:187], a[52:55], v[112:127]\n"
"s_waitcnt lgkmcnt(3)\n"
"ds_read_b64_tr_b16 a[172:173], v22 offset:8192\n"
"ds_read_b64_tr_b16 a[174:175], v22 offset:8704\n"
"buffer_load_dwordx4 v32, s[8:11], s74 offen offset:2048 lds\n"
"v_xor_b32 v64, s78, v61\n"
"v_add3_u32 v64, v64, v62, s79\n"
"v_mfma_f32_32x32x16_bf16 v[128:143], a[188:191], a[52:55], v[128:143]\n"
"ds_read_b64_tr_b16 a[176:177], v23 offset:8192\n"
"ds_read_b64_tr_b16 a[178:179], v23 offset:8704\n"
"v_accvgpr_write_b32 a193, v64\n"
"s_add_u32 s78, s60, 2\n"
"s_lshl_b32 s79, s54, 2\n"
"s_add_u32 s79, s79, s78\n"
"v_mfma_f32_32x32x16_bf16 v[144:159], a[160:163], a[52:55], v[144:159]\n"
"s_waitcnt lgkmcnt(4)\n"
"ds_read_b64_tr_b16 a[180:181], v24 offset:8192\n"
"ds_read_b64_tr_b16 a[182:183], v24 offset:8704\n"
"buffer_load_dwordx4 v33, s[8:11], s74 offen offset:3072 lds\n"
"s_mul_i32 s66, s74, 1536\n"
"s_add_u32 m0, s57, 20480\n"
"v_mfma_f32_32x32x16_bf16 v[160:175], a[164:167], a[52:55], v[160:175]\n"
"ds_read_b64_tr_b16 a[184:185], v25 offset:8192\n"
"ds_read_b64_tr_b16 a[186:187], v25 offset:8704\n"
"s_and_b32 s79, s79, 15\n"
"s_cmp_ge_u32 s78, 16\n"
"s_cselect_b32 s79, s78, s79\n"
"s_add_u32 s79, s79, s62\n"
"v_mfma_f32_32x32x16_bf16 v[176:191], a[56:59], a[168:171], v[176:191]\n"
"s_waitcnt lgkmcnt(4)\n"
"ds_read_b128 a[188:191], v17 offset:16384\n"
"buffer_load_dwordx4 v34, s[20:23], s66 offen offset:0 lds\n"
"s_mul_i32 s79, s79, 10240\n"
"s_add_u32 s79, s79, 1024\n"
"s_and_b32 s78, s78, 1\n"
"v_mfma_f32_32x32x16_bf16 v[112:127], a[172:175], a[56:59], v[112:127]\n"
"ds_read_b64_tr_b16 a[160:161], v22 offset:12288\n"
"ds_read_b64_tr_b16 a[162:163], v22 offset:12800\n"
"s_lshl_b32 s78, s78, 6\n"
"v_xor_b32 v64, s78, v61\n"
"v_add3_u32 v64, v64, v62, s79\n"
"v_accvgpr_write_b32 a194, v64\n"
"v_mfma_f32_32x32x16_bf16 v[128:143], a[176:179], a[56:59], v[128:143]\n"
"s_waitcnt lgkmcnt(3)\n"
"s_waitcnt vmcnt(20)\n"
"ds_read_b128 a[64:67], v26 offset:16384\n"
"ds_read_b128 a[68:71], v27 offset:16384\n"
"ds_read_b64_tr_b16 a[164:165], v23 offset:12288\n"
"ds_read_b64_tr_b16 a[166:167], v23 offset:12800\n"
"buffer_load_dwordx4 v35, s[20:23], s66 offen offset:1024 lds\n"
"v_mfma_f32_32x32x16_bf16 v[144:159], a[180:183], a[56:59], v[144:159]\n"
"ds_read_b128 a[72:75], v28 offset:16384\n"
"ds_read_b128 a[76:79], v29 offset:16384\n"
"ds_read_b64_tr_b16 a[168:169], v24 offset:12288\n"
"ds_read_b64_tr_b16 a[170:171], v24 offset:12800\n"
"s_add_u32 s78, s60, 3\n"
"s_lshl_b32 s79, s54, 2\n"
"v_mfma_f32_32x32x16_bf16 v[160:175], a[184:187], a[56:59], v[160:175]\n"
"s_waitcnt vmcnt(16)\n"
"s_barrier\n"
"s_waitcnt lgkmcnt(8)\n"
"ds_read_b64_tr_b16 a[172:173], v25 offset:12288\n"
"ds_read_b64_tr_b16 a[174:175], v25 offset:12800\n"
"buffer_load_dwordx4 v36, s[24:27], s66 offen offset:2048 lds\n"
"v_mfma_f32_32x32x16_bf16 v[176:191], a[60:63], a[188:191], v[176:191]\n"
"ds_read_b128 a[176:179], v14 offset:36864\n"
"s_add_u32 s79, s79, s78\n"
"s_and_b32 s79, s79, 15\n"
"s_cmp_ge_u32 s78, 16\n"
"s_cselect_b32 s79, s78, s79\n"
"s_add_u32 s79, s79, s62\n"
"v_mfma_f32_32x32x16_bf16 v[112:127], a[160:163], a[60:63], v[112:127]\n"
"s_waitcnt lgkmcnt(3)\n"
"ds_read_b64_tr_b16 a[180:181], v22 offset:20480\n"
"ds_read_b64_tr_b16 a[182:183], v22 offset:20992\n"
"buffer_load_dwordx4 v37, s[24:27], s66 offen offset:3072 lds\n"
"s_add_u32 m0, s58, 36864\n"
"s_mul_i32 s79, s79, 10240\n"
"v_mfma_f32_32x32x16_bf16 v[128:143], a[164:167], a[60:63], v[128:143]\n"
"ds_read_b64_tr_b16 a[184:185], v23 offset:20480\n"
"ds_read_b64_tr_b16 a[186:187], v23 offset:20992\n"
"s_add_u32 s79, s79, 0\n"
"s_and_b32 s78, s78, 1\n"
"s_lshl_b32 s78, s78, 6\n"
"v_xor_b32 v64, s78, v61\n"
"v_mfma_f32_32x32x16_bf16 v[144:159], a[168:171], a[60:63], v[144:159]\n"
"s_waitcnt lgkmcnt(4)\n"
"ds_read_b64_tr_b16 a[188:189], v24 offset:20480\n"
"ds_read_b64_tr_b16 a[190:191], v24 offset:20992\n"
"buffer_load_dwordx4 v38, s[12:15], s74 offen lds\n"
"v_add3_u32 v64, v64, v62, s79\n"
"v_accvgpr_write_b32 a195, v64\n"
"v_mfma_f32_32x32x16_bf16 v[160:175], a[172:175], a[60:63], v[160:175]\n"
"ds_read_b64_tr_b16 a[160:161], v25 offset:20480\n"
"ds_read_b64_tr_b16 a[162:163], v25 offset:20992\n"
"s_add_u32 s78, s60, 4\n"
"s_lshl_b32 s79, s54, 2\n"
"s_add_u32 s79, s79, s78\n"
"s_and_b32 s79, s79, 15\n"
"v_mfma_f32_32x32x16_bf16 v[176:191], a[64:67], a[176:179], v[176:191]\n"
"s_waitcnt lgkmcnt(4)\n"
"ds_read_b128 a[164:167], v15 offset:36864\n"
"s_cmp_ge_u32 s78, 16\n"
"s_cselect_b32 s79, s78, s79\n"
"s_add_u32 s79, s79, s62\n"
"s_mul_i32 s79, s79, 10240\n"
"s_add_u32 s79, s79, 3072\n"
"v_mfma_f32_32x32x16_bf16 v[112:127], a[180:183], a[64:67], v[112:127]\n"
"ds_read_b64_tr_b16 a[168:169], v22 offset:24576\n"
"ds_read_b64_tr_b16 a[170:171], v22 offset:25088\n"
"s_and_b32 s78, s78, 1\n"
"s_lshl_b32 s78, s78, 6\n"
"v_xor_b32 v64, s78, v61\n"
"v_add3_u32 v64, v64, v62, s79\n"
"v_mfma_f32_32x32x16_bf16 v[128:143], a[184:187], a[64:67], v[128:143]\n"
"s_waitcnt lgkmcnt(3)\n"
"ds_read_b64_tr_b16 a[172:173], v23 offset:24576\n"
"ds_read_b64_tr_b16 a[174:175], v23 offset:25088\n"
"s_add_u32 m0, s57, 118784\n"
"s_nop 0\n"
"buffer_load_dwordx4 v30, s[8:11], s75 offen offset:0 lds\n"
"v_mfma_f32_32x32x16_bf16 v[144:159], a[188:191], a[64:67], v[144:159]\n"
"ds_read_b64_tr_b16 a[176:177], v24 offset:24576\n"
"ds_read_b64_tr_b16 a[178:179], v24 offset:25088\n"
"v_accvgpr_write_b32 a196, v64\n"
"s_mul_i32 s78, s52, 20480\n"
"s_mul_i32 s79, s55, 320\n"
"s_add_u32 s78, s78, s79\n"
"v_mfma_f32_32x32x16_bf16 v[160:175], a[160:163], a[64:67], v[160:175]\n"
"s_waitcnt lgkmcnt(4)\n"
"ds_read_b64_tr_b16 a[180:181], v25 offset:24576\n"
"ds_read_b64_tr_b16 a[182:183], v25 offset:25088\n"
"buffer_load_dwordx4 v31, s[8:11], s75 offen offset:1024 lds\n"
"s_lshl_b32 s79, s54, 2\n"
"v_add_u32 v61, 0, v2\n"
"v_mfma_f32_32x32x16_bf16 v[176:191], a[68:71], a[164:167], v[176:191]\n"
"ds_read_b128 a[184:187], v16 offset:36864\n"
"v_mul_u32_u24 v64, 3277, v61\n"
"v_lshrrev_b32 v64, 16, v64\n"
"v_mul_u32_u24 v62, 20, v64\n"
"v_sub_u32 v61, v61, v62\n"
"v_add_u32 v62, s79, v61\n"
"v_mfma_f32_32x32x16_bf16 v[112:127], a[168:171], a[68:71], v[112:127]\n"
"s_waitcnt lgkmcnt(3)\n"
"ds_read_b64_tr_b16 a[188:189], v22 offset:28672\n"
"ds_read_b64_tr_b16 a[190:191], v22 offset:29184\n"
"buffer_load_dwordx4 v32, s[8:11], s75 offen offset:2048 lds\n"
"v_cmp_gt_u32 vcc, 16, v61\n"
"v_and_b32 v62, 15, v62\n"
"v_mfma_f32_32x32x16_bf16 v[128:143], a[172:175], a[68:71], v[128:143]\n"
"ds_read_b64_tr_b16 a[160:161], v23 offset:28672\n"
"ds_read_b64_tr_b16 a[162:163], v23 offset:29184\n"
"v_mul_u32_u24 v64, 1280, v64\n"
"v_cndmask_b32 v61, v61, v62, vcc\n"
"s_sub_u32 s60, s78, 0\n"
"v_lshlrev_b32 v61, 4, v61\n"
"v_mfma_f32_32x32x16_bf16 v[144:159], a[176:179], a[68:71], v[144:159]\n"
"s_waitcnt lgkmcnt(4)\n"
"ds_read_b64_tr_b16 a[164:165], v24 offset:28672\n"
"ds_read_b64_tr_b16 a[166:167], v24 offset:29184\n"
"buffer_load_dwordx4 v33, s[8:11], s75 offen offset:3072 lds\n"
"s_mul_i32 s66, s75, 1536\n"
"s_add_u32 m0, s57, 40960\n"
"v_mfma_f32_32x32x16_bf16 v[160:175], a[180:183], a[68:71], v[160:175]\n"
"ds_read_b64_tr_b16 a[168:169], v25 offset:28672\n"
"ds_read_b64_tr_b16 a[170:171], v25 offset:29184\n"
"v_add3_u32 v64, v64, v61, s60\n"
"v_accvgpr_write_b32 a197, v64\n"
"v_add_u32 v61, 64, v2\n"
"v_mul_u32_u24 v40, 3277, v61\n"
"v_mfma_f32_32x32x16_bf16 v[176:191], a[72:75], a[184:187], v[176:191]\n"
"s_waitcnt lgkmcnt(4)\n"
"ds_read_b128 a[172:175], v17 offset:36864\n"
"buffer_load_dwordx4 v34, s[20:23], s66 offen offset:0 lds\n"
"v_lshrrev_b32 v40, 16, v40\n"
"v_mul_u32_u24 v62, 20, v40\n"
"v_sub_u32 v61, v61, v62\n"
"v_mfma_f32_32x32x16_bf16 v[112:127], a[188:191], a[72:75], v[112:127]\n"
"ds_read_b64_tr_b16 a[176:177], v22 offset:32768\n"
"ds_read_b64_tr_b16 a[178:179], v22 offset:33280\n"
"v_add_u32 v62, s79, v61\n"
"v_cmp_gt_u32 vcc, 16, v61\n"
"v_and_b32 v62, 15, v62\n"
"v_mul_u32_u24 v40, 1280, v40\n"
"v_mfma_f32_32x32x16_bf16 v[128:143], a[160:163], a[72:75], v[128:143]\n"
"s_waitcnt lgkmcnt(3)\n"
"s_waitcnt vmcnt(20)\n"
"ds_read_b128 a[80:83], v26 offset:32768\n"
"ds_read_b128 a[84:87], v27 offset:32768\n"
"ds_read_b64_tr_b16 a[180:181], v23 offset:32768\n"
"ds_read_b64_tr_b16 a[182:183], v23 offset:33280\n"
"buffer_load_dwordx4 v35, s[20:23], s66 offen offset:1024 lds\n"
"v_mfma_f32_32x32x16_bf16 v[144:159], a[164:167], a[72:75], v[144:159]\n"
"ds_read_b128 a[88:91], v28 offset:32768\n"
"ds_read_b128 a[92:95], v29 offset:32768\n"
"ds_read_b64_tr_b16 a[184:185], v24 offset:32768\n"
"ds_read_b64_tr_b16 a[186:187], v24 offset:33280\n"
"v_cndmask_b32 v61, v61, v62, vcc\n"
"s_sub_u32 s60, s78, 1024\n"
"v_mfma_f32_32x32x16_bf16 v[160:175], a[168:171], a[72:75], v[160:175]\n"
"s_waitcnt vmcnt(16)\n"
"s_barrier\n"
"s_waitcnt lgkmcnt(8)\n"
"ds_read_b64_tr_b16 a[188:189], v25 offset:32768\n"
"ds_read_b64_tr_b16 a[190:191], v25 offset:33280\n"
"buffer_load_dwordx4 v36, s[24:27], s66 offen offset:2048 lds\n"
"v_mfma_f32_32x32x16_bf16 v[176:191], a[76:79], a[172:175], v[176:191]\n"
"ds_read_b128 a[160:163], v10 offset:16384\n"
"v_lshlrev_b32 v61, 4, v61\n"
"v_add3_u32 v40, v40, v61, s60\n"
"v_add_u32 v61, 128, v2\n"
"v_mul_u32_u24 v41, 3277, v61\n"
"v_lshrrev_b32 v41, 16, v41\n"
"v_mfma_f32_32x32x16_bf16 v[112:127], a[176:179], a[76:79], v[112:127]\n"
"s_waitcnt lgkmcnt(3)\n"
"ds_read_b64_tr_b16 a[164:165], v18 offset:0\n"
"ds_read_b64_tr_b16 a[166:167], v18 offset:512\n"
"buffer_load_dwordx4 v37, s[24:27], s66 offen offset:3072 lds\n"
"s_add_u32 m0, s58, 57344\n"
"v_mul_u32_u24 v62, 20, v41\n"
"v_mfma_f32_32x32x16_bf16 v[128:143], a[180:183], a[76:79], v[128:143]\n"
"ds_read_b64_tr_b16 a[168:169], v19 offset:0\n"
"ds_read_b64_tr_b16 a[170:171], v19 offset:512\n"
"v_sub_u32 v61, v61, v62\n"
"v_add_u32 v62, s79, v61\n"
"v_cmp_gt_u32 vcc, 16, v61\n"
"v_and_b32 v62, 15, v62\n"
"v_mfma_f32_32x32x16_bf16 v[144:159], a[184:187], a[76:79], v[144:159]\n"
"s_waitcnt lgkmcnt(4)\n"
"ds_read_b64_tr_b16 a[172:173], v20 offset:0\n"
"ds_read_b64_tr_b16 a[174:175], v20 offset:512\n"
"buffer_load_dwordx4 v38, s[12:15], s75 offen lds\n"
"v_mul_u32_u24 v41, 1280, v41\n"
"v_cndmask_b32 v61, v61, v62, vcc\n"
"v_mfma_f32_32x32x16_bf16 v[160:175], a[188:191], a[76:79], v[160:175]\n"
"ds_read_b64_tr_b16 a[176:177], v21 offset:0\n"
"ds_read_b64_tr_b16 a[178:179], v21 offset:512\n"
"s_sub_u32 s60, s78, 2048\n"
"v_lshlrev_b32 v61, 4, v61\n"
"v_add3_u32 v41, v41, v61, s60\n"
"v_add_u32 v61, 192, v2\n"
"v_mfma_f32_32x32x16_bf16 v[176:191], a[80:83], a[160:163], v[176:191]\n"
"s_waitcnt lgkmcnt(4)\n"
"ds_read_b128 a[180:183], v11 offset:16384\n"
"s_lshl_b32 s7, s53, 2\n"
"s_add_u32 s7, s7, s52\n"
"s_lshl_b32 s7, s7, 8\n"
"s_add_u32 s7, s7, 0x1700000\n"
"v_mul_u32_u24 v42, 3277, v61\n"
"v_mfma_f32_32x32x16_bf16 v[112:127], a[164:167], a[80:83], v[112:127]\n"
"ds_read_b64_tr_b16 a[184:185], v18 offset:4096\n"
"ds_read_b64_tr_b16 a[186:187], v18 offset:4608\n"
"buffer_load_dwordx2 v[58:59], v60, s[40:43], s7 offen sc1\n"
"v_lshrrev_b32 v42, 16, v42\n"
"v_mul_u32_u24 v62, 20, v42\n"
"v_mfma_f32_32x32x16_bf16 v[128:143], a[168:171], a[80:83], v[128:143]\n"
"s_waitcnt lgkmcnt(3)\n"
"ds_read_b64_tr_b16 a[188:189], v19 offset:4096\n"
"ds_read_b64_tr_b16 a[190:191], v19 offset:4608\n"
"s_add_u32 m0, s57, 135168\n"
"s_nop 0\n"
"buffer_load_dwordx4 v30, s[8:11], s76 offen offset:0 lds\n"
"v_mfma_f32_32x32x16_bf16 v[144:159], a[172:175], a[80:83], v[144:159]\n"
"ds_read_b64_tr_b16 a[160:161], v20 offset:4096\n"
"ds_read_b64_tr_b16 a[162:163], v20 offset:4608\n"
"v_sub_u32 v61, v61, v62\n"
"v_add_u32 v62, s79, v61\n"
"v_cmp_gt_u32 vcc, 16, v61\n"
"v_and_b32 v62, 15, v62\n"
"v_mfma_f32_32x32x16_bf16 v[160:175], a[176:179], a[80:83], v[160:175]\n"
"s_waitcnt lgkmcnt(4)\n"
"ds_read_b64_tr_b16 a[164:165], v21 offset:4096\n"
"ds_read_b64_tr_b16 a[166:167], v21 offset:4608\n"
"buffer_load_dwordx4 v31, s[8:11], s76 offen offset:1024 lds\n"
"v_mul_u32_u24 v42, 1280, v42\n"
"v_cndmask_b32 v61, v61, v62, vcc\n"
"v_mfma_f32_32x32x16_bf16 v[176:191], a[84:87], a[180:183], v[176:191]\n"
"ds_read_b128 a[168:171], v12 offset:16384\n"
"s_sub_u32 s60, s78, 3072\n"
"v_lshlrev_b32 v61, 4, v61\n"
"v_add3_u32 v42, v42, v61, s60\n"
"v_add_u32 v61, 256, v2\n"
"v_mul_u32_u24 v43, 3277, v61\n"
"v_mfma_f32_32x32x16_bf16 v[112:127], a[184:187], a[84:87], v[112:127]\n"
"s_waitcnt lgkmcnt(3)\n"
"ds_read_b64_tr_b16 a[172:173], v18 offset:8192\n"
"ds_read_b64_tr_b16 a[174:175], v18 offset:8704\n"
"buffer_load_dwordx4 v32, s[8:11], s76 offen offset:2048 lds\n"
"v_lshrrev_b32 v43, 16, v43\n"
"v_mul_u32_u24 v62, 20, v43\n"
"v_mfma_f32_32x32x16_bf16 v[128:143], a[188:191], a[84:87], v[128:143]\n"
"ds_read_b64_tr_b16 a[176:177], v19 offset:8192\n"
"ds_read_b64_tr_b16 a[178:179], v19 offset:8704\n"
"v_sub_u32 v61, v61, v62\n"
"v_add_u32 v62, s79, v61\n"
"v_cmp_gt_u32 vcc, 16, v61\n"
"v_and_b32 v62, 15, v62\n"
"v_mfma_f32_32x32x16_bf16 v[144:159], a[160:163], a[84:87], v[144:159]\n"
"s_waitcnt lgkmcnt(4)\n"
"ds_read_b64_tr_b16 a[180:181], v20 offset:8192\n"
"ds_read_b64_tr_b16 a[182:183], v20 offset:8704\n"
"buffer_load_dwordx4 v33, s[8:11], s76 offen offset:3072 lds\n"
"s_mul_i32 s66, s76, 1536\n"
"s_add_u32 m0, s57, 61440\n"
"v_mfma_f32_32x32x16_bf16 v[160:175], a[164:167], a[84:87], v[160:175]\n"
"ds_read_b64_tr_b16 a[184:185], v21 offset:8192\n"
"ds_read_b64_tr_b16 a[186:187], v21 offset:8704\n"
"v_mul_u32_u24 v43, 1280, v43\n"
"v_cndmask_b32 v61, v61, v62, vcc\n"
"s_sub_u32 s60, s78, 0\n"
"v_lshlrev_b32 v61, 4, v61\n"
"v_mfma_f32_32x32x16_bf16 v[176:191], a[88:91], a[168:171], v[176:191]\n"
"s_waitcnt lgkmcnt(4)\n"
"ds_read_b128 a[188:191], v13 offset:16384\n"
"buffer_load_dwordx4 v34, s[20:23], s66 offen offset:0 lds\n"
"v_add3_u32 v43, v43, v61, s60\n"
"s_lshl_b32 s4, s53, 2\n"
"s_add_u32 s4, s4, s54\n"
"v_mfma_f32_32x32x16_bf16 v[112:127], a[172:175], a[88:91], v[112:127]\n"
"ds_read_b64_tr_b16 a[160:161], v18 offset:12288\n"
"ds_read_b64_tr_b16 a[162:163], v18 offset:12800\n"
"s_lshl_b32 s4, s4, 2\n"
"s_add_u32 s4, s4, s52\n"
"s_mul_i32 s4, s4, 384\n"
"s_add_u32 s4, s4, 0x1780000\n"
"v_mfma_f32_32x32x16_bf16 v[128:143], a[176:179], a[88:91], v[128:143]\n"
"s_waitcnt lgkmcnt(3)\n"
"s_waitcnt vmcnt(20)\n"
"ds_read_b128 a[96:99], v26 offset:0\n"
"ds_read_b128 a[100:103], v27 offset:0\n"
"ds_read_b64_tr_b16 a[164:165], v19 offset:12288\n"
"ds_read_b64_tr_b16 a[166:167], v19 offset:12800\n"
"buffer_load_dwordx4 v35, s[20:23], s66 offen offset:1024 lds\n"
"v_mfma_f32_32x32x16_bf16 v[144:159], a[180:183], a[88:91], v[144:159]\n"
"ds_read_b128 a[104:107], v28 offset:0\n"
"ds_read_b128 a[108:111], v29 offset:0\n"
"ds_read_b64_tr_b16 a[168:169], v20 offset:12288\n"
"ds_read_b64_tr_b16 a[170:171], v20 offset:12800\n"
"v_cmp_gt_u32 vcc, 4, v2\n"
"v_mov_b32 v61, 0x70000000\n"
"v_mfma_f32_32x32x16_bf16 v[160:175], a[184:187], a[88:91], v[160:175]\n"
"s_waitcnt vmcnt(16)\n"
"s_barrier\n"
"s_waitcnt lgkmcnt(8)\n"
"ds_read_b64_tr_b16 a[172:173], v21 offset:12288\n"
"ds_read_b64_tr_b16 a[174:175], v21 offset:12800\n"
"buffer_load_dwordx4 v36, s[24:27], s66 offen offset:2048 lds\n"
"v_mfma_f32_32x32x16_bf16 v[176:191], a[92:95], a[188:191], v[176:191]\n"
"ds_read_b128 a[176:179], v10 offset:36864\n"
"v_lshlrev_b32 v1, 2, v2\n"
"v_cndmask_b32 v1, v61, v1, vcc\n"
"v_cmp_eq_u32 vcc, s55, v2\n"
"v_nop\n"
"v_nop\n"
"v_mfma_f32_32x32x16_bf16 v[112:127], a[160:163], a[92:95], v[112:127]\n"
"s_waitcnt lgkmcnt(3)\n"
"ds_read_b64_tr_b16 a[180:181], v18 offset:20480\n"
"ds_read_b64_tr_b16 a[182:183], v18 offset:20992\n"
"buffer_load_dwordx4 v37, s[24:27], s66 offen offset:3072 lds\n"
"s_add_u32 m0, s58, 77824\n"
"v_cndmask_b32 v4, v61, v1, vcc\n"
"v_mfma_f32_32x32x16_bf16 v[128:143], a[164:167], a[92:95], v[128:143]\n"
"ds_read_b64_tr_b16 a[184:185], v19 offset:20480\n"
"ds_read_b64_tr_b16 a[186:187], v19 offset:20992\n"
"v_lshrrev_b32 v62, 5, v2\n"
"v_bfe_u32 v64, v2, 4, 1\n"
"v_lshl_add_u32 v62, v64, 1, v62\n"
"v_lshlrev_b32 v62, 2, v62\n"
"v_mfma_f32_32x32x16_bf16 v[144:159], a[168:171], a[92:95], v[144:159]\n"
"s_waitcnt lgkmcnt(4)\n"
"ds_read_b64_tr_b16 a[188:189], v20 offset:20480\n"
"ds_read_b64_tr_b16 a[190:191], v20 offset:20992\n"
"buffer_load_dwordx4 v38, s[12:15], s76 offen lds\n"
"v_sub_u32 v62, s53, v62\n"
"v_and_b32 v64, 15, v2\n"
"v_mfma_f32_32x32x16_bf16 v[160:175], a[172:175], a[92:95], v[160:175]\n"
"ds_read_b64_tr_b16 a[160:161], v21 offset:20480\n"
"ds_read_b64_tr_b16 a[162:163], v21 offset:20992\n"
"v_lshlrev_b32 v64, 4, v64\n"
"s_movk_i32 s62, 10240\n"
"v_add_u32 v62, -1, v62\n"
"v_cmp_le_i32 vcc, 0, v62\n"
"v_mfma_f32_32x32x16_bf16 v[176:191], a[96:99], a[176:179], v[176:191]\n"
"s_waitcnt lgkmcnt(4)\n"
"ds_read_b128 a[164:167], v11 offset:36864\n"
"v_mad_u32_u24 v65, v62, s62, v64\n"
"v_nop\n"
"v_cndmask_b32 v65, v61, v65, vcc\n"
"v_cndmask_b32 v86, 0, v7, vcc\n"
"v_accvgpr_write_b32 a208, v65\n"
"v_mfma_f32_32x32x16_bf16 v[112:127], a[180:183], a[96:99], v[112:127]\n"
"ds_read_b64_tr_b16 a[168:169], v18 offset:24576\n"
"ds_read_b64_tr_b16 a[170:171], v18 offset:25088\n"
"v_accvgpr_write_b32 a212, v86\n"
"v_add_u32 v62, -1, v62\n"
"v_cmp_le_i32 vcc, 0, v62\n"
"v_mad_u32_u24 v65, v62, s62, v64\n"
"v_mfma_f32_32x32x16_bf16 v[128:143], a[184:187], a[96:99], v[128:143]\n"
"s_waitcnt lgkmcnt(3)\n"
"ds_read_b64_tr_b16 a[172:173], v19 offset:24576\n"
"ds_read_b64_tr_b16 a[174:175], v19 offset:25088\n"
"s_add_u32 m0, s57, 102400\n"
"s_nop 0\n"
"buffer_load_dwordx4 v30, s[8:11], s77 offen offset:0 lds\n"
"v_mfma_f32_32x32x16_bf16 v[144:159], a[188:191], a[96:99], v[144:159]\n"
"ds_read_b64_tr_b16 a[176:177], v20 offset:24576\n"
"ds_read_b64_tr_b16 a[178:179], v20 offset:25088\n"
"v_nop\n"
"v_cndmask_b32 v65, v61, v65, vcc\n"
"v_cndmask_b32 v86, 0, v7, vcc\n"
"v_accvgpr_write_b32 a209, v65\n"
"v_mfma_f32_32x32x16_bf16 v[160:175], a[160:163], a[96:99], v[160:175]\n"
"s_waitcnt lgkmcnt(4)\n"
"ds_read_b64_tr_b16 a[180:181], v21 offset:24576\n"
"ds_read_b64_tr_b16 a[182:183], v21 offset:25088\n"
"buffer_load_dwordx4 v31, s[8:11], s77 offen offset:1024 lds\n"
"v_accvgpr_write_b32 a213, v86\n"
"v_add_u32 v62, -1, v62\n"
"v_mfma_f32_32x32x16_bf16 v[176:191], a[100:103], a[164:167], v[176:191]\n"
"ds_read_b128 a[184:187], v12 offset:36864\n"
"v_cmp_le_i32 vcc, 0, v62\n"
"v_mad_u32_u24 v65, v62, s62, v64\n"
"v_nop\n"
"v_cndmask_b32 v65, v61, v65, vcc\n"
"v_cndmask_b32 v86, 0, v7, vcc\n"
"v_mfma_f32_32x32x16_bf16 v[112:127], a[168:171], a[100:103], v[112:127]\n"
"s_waitcnt lgkmcnt(3)\n"
"ds_read_b64_tr_b16 a[188:189], v18 offset:28672\n"
"ds_read_b64_tr_b16 a[190:191], v18 offset:29184\n"
"buffer_load_dwordx4 v32, s[8:11], s77 offen offset:2048 lds\n"
"v_accvgpr_write_b32 a210, v65\n"
"v_accvgpr_write_b32 a214, v86\n"
"v_mfma_f32_32x32x16_bf16 v[128:143], a[172:175], a[100:103], v[128:143]\n"
"ds_read_b64_tr_b16 a[160:161], v19 offset:28672\n"
"ds_read_b64_tr_b16 a[162:163], v19 offset:29184\n"
"v_add_u32 v62, -1, v62\n"
"v_cmp_le_i32 vcc, 0, v62\n"
"v_mad_u32_u24 v65, v62, s62, v64\n"
"v_nop\n"
"v_mfma_f32_32x32x16_bf16 v[144:159], a[176:179], a[100:103], v[144:159]\n"
"s_waitcnt lgkmcnt(4)\n"
"ds_read_b64_tr_b16 a[164:165], v20 offset:28672\n"
"ds_read_b64_tr_b16 a[166:167], v20 offset:29184\n"
"buffer_load_dwordx4 v33, s[8:11], s77 offen offset:3072 lds\n"
"s_mul_i32 s66, s77, 1536\n"
"s_add_u32 m0, s57, 81920\n"
"v_mfma_f32_32x32x16_bf16 v[160:175], a[180:183], a[100:103], v[160:175]\n"
"ds_read_b64_tr_b16 a[168:169], v21 offset:28672\n"
"ds_read_b64_tr_b16 a[170:171], v21 offset:29184\n"
"s_waitcnt vmcnt(13)\n"
"v_cmp_ne_u32 s[0:1], s56, v59\n"
"s_nop 0\n"
"s_cmp_lg_u64 s[0:1], 0\n"
"s_cbranch_scc1 LstA_slow%=\n"
"LstA_ok%=:\n"
"v_and_b32 v59, s100, v58\n"
"v_mfma_f32_32x32x16_bf16 v[176:191], a[104:107], a[184:187], v[176:191]\n"
"s_waitcnt lgkmcnt(4)\n"
"ds_read_b128 a[172:175], v13 offset:36864\n"
"buffer_load_dwordx4 v34, s[20:23], s66 offen offset:0 lds\n"
"v_lshlrev_b32 v58, 16, v58\n"
"v_mul_f32 v52, v59, v59\n"
"v_cndmask_b32 v65, v61, v65, vcc\n"
"v_mfma_f32_32x32x16_bf16 v[112:127], a[188:191], a[104:107], v[112:127]\n"
"ds_read_b64_tr_b16 a[176:177], v18 offset:32768\n"
"ds_read_b64_tr_b16 a[178:179], v18 offset:33280\n"
"v_rcp_f32 v5, v59\n"
"v_cvt_pk_bf16_f32 v54, -v58, -v58\n"
"v_mov_b32 v55, 0\n"
"v_mfma_f32_32x32x16_bf16 v[128:143], a[160:163], a[104:107], v[128:143]\n"
"s_waitcnt lgkmcnt(3)\n"
"ds_read_b128 a[112:115], v26 offset:16384\n"
"ds_read_b128 a[116:119], v27 offset:16384\n"
"ds_read_b64_tr_b16 a[180:181], v19 offset:32768\n"
"ds_read_b64_tr_b16 a[182:183], v19 offset:33280\n"
"buffer_load_dwordx4 v35, s[20:23], s66 offen offset:1024 lds\n"
"v_mfma_f32_32x32x16_bf16 v[144:159], a[164:167], a[104:107], v[144:159]\n"
"ds_read_b128 a[120:123], v28 offset:16384\n"
"ds_read_b128 a[124:127], v29 offset:16384\n"
"ds_read_b64_tr_b16 a[184:185], v20 offset:32768\n"
"ds_read_b64_tr_b16 a[186:187], v20 offset:33280\n"
"v_mov_b32 v56, v54\n"
"v_mov_b32 v57, 0\n"
"v_mfma_f32_32x32x16_bf16 v[160:175], a[168:171], a[104:107], v[160:175]\n"
"s_barrier\n"
"s_waitcnt lgkmcnt(8)\n"
"ds_read_b64_tr_b16 a[188:189], v21 offset:32768\n"
"ds_read_b64_tr_b16 a[190:191], v21 offset:33280\n"
"buffer_load_dwordx4 v36, s[24:27], s66 offen offset:2048 lds\n"
"v_mfma_f32_32x32x16_bf16 v[176:191], a[108:111], a[172:175], v[176:191]\n"
"ds_read_b128 a[160:163], v10 offset:57344\n"
"v_mov_b32 v59, 0.5\n"
"v_mul_f32 v6, v58, v5\n"
"v_mul_f32 v50, 0xc0135761, v5\n"
"v_mul_f32 v51, v5, v5\n"
"v_mul_f32 v51, v51, v50\n"
"v_mfma_f32_32x32x16_bf16 v[112:127], a[176:179], a[108:111], v[112:127]\n"
"s_waitcnt lgkmcnt(3)\n"
"ds_read_b64_tr_b16 a[164:165], v18 offset:40960\n"
"ds_read_b64_tr_b16 a[166:167], v18 offset:41472\n"
"buffer_load_dwordx4 v37, s[24:27], s66 offen offset:3072 lds\n"
"s_add_u32 m0, s58, 98304\n"
"v_mul_f32 v51, 0x3d372713, v51\n"
"v_mfma_f32_32x32x16_bf16 v[128:143], a[180:183], a[108:111], v[128:143]\n"
"ds_read_b64_tr_b16 a[168:169], v19 offset:40960\n"
"ds_read_b64_tr_b16 a[170:171], v19 offset:41472\n"
"v_cndmask_b32 v86, 0, v7, vcc\n"
"v_accvgpr_write_b32 a211, v65\n"
"v_accvgpr_write_b32 a215, v86\n"
"v_lshlrev_b32 v109, 16, v109\n"
"v_mfma_f32_32x32x16_bf16 v[144:159], a[184:187], a[108:111], v[144:159]\n"
"s_waitcnt lgkmcnt(4)\n"
"ds_read_b64_tr_b16 a[172:173], v20 offset:40960\n"
"ds_read_b64_tr_b16 a[174:175], v20 offset:41472\n"
"buffer_load_dwordx4 v38, s[12:15], s77 offen lds\n"
"s_mul_i32 s66, s68, 1536\n"
"s_add_u32 s66, s66, 128\n"
"s_add_u32 m0, s57, 0\n"
"v_mfma_f32_32x32x16_bf16 v[160:175], a[188:191], a[108:111], v[160:175]\n"
"ds_read_b64_tr_b16 a[176:177], v21 offset:40960\n"
"ds_read_b64_tr_b16 a[178:179], v21 offset:41472\n"
"v_lshlrev_b32 v107, 16, v107\n"
"v_lshlrev_b32 v108, 16, v108\n"
"v_mul_f32 v109, 0x3fb8aa3b, v109\n"
"v_mfma_f32_32x32x16_bf16 v[176:191], a[112:115], a[160:163], v[176:191]\n"
"s_waitcnt lgkmcnt(4)\n"
"ds_read_b128 a[180:183], v11 offset:57344\n"
"s_lshl_b32 s3, s54, 2\n"
"s_add_u32 s3, s3, s55\n"
"s_mul_i32 s3, s3, 10240\n"
"s_add_u32 s3, s3, 0x1680000\n"
"buffer_load_dwordx4 v34, s[20:23], s66 offen offset:0 lds\n"
"v_mfma_f32_32x32x16_bf16 v[112:127], a[164:167], a[112:115], v[112:127]\n"
"ds_read_b64_tr_b16 a[184:185], v18 offset:45056\n"
"ds_read_b64_tr_b16 a[186:187], v18 offset:45568\n"
"s_add_u32 s2, s3, 4096\n"
"buffer_load_dwordx4 a[224:227], v44, s[40:43], s3 offen offset:0 sc1\n"
"v_mfma_f32_32x32x16_bf16 v[128:143], a[168:171], a[112:115], v[128:143]\n"
"s_waitcnt lgkmcnt(3)\n"
"ds_read_b64_tr_b16 a[188:189], v19 offset:45056\n"
"ds_read_b64_tr_b16 a[190:191], v19 offset:45568\n"
"v_exp_f32 v109, v109\n"
"v_mul_f32 v110, v107, v107\n"
"v_fmac_f32 v110, v108, v108\n"
"v_mfma_f32_32x32x16_bf16 v[144:159], a[172:175], a[112:115], v[144:159]\n"
"ds_read_b64_tr_b16 a[160:161], v20 offset:45056\n"
"ds_read_b64_tr_b16 a[162:163], v20 offset:45568\n"
"buffer_load_dwordx4 a[228:231], v44, s[40:43], s3 offen offset:1024 sc1\n"
"v_cvt_pk_bf16_f32 v109, v109, v109\n"
"v_mfma_f32_32x32x16_bf16 v[160:175], a[176:179], a[112:115], v[160:175]\n"
"s_waitcnt lgkmcnt(4)\n"
"ds_read_b64_tr_b16 a[164:165], v21 offset:45056\n"
"ds_read_b64_tr_b16 a[166:167], v21 offset:45568\n"
"v_rcp_f32 v110, v110\n"
"v_lshlrev_b32 v109, 16, v109\n"
"v_mul_f32 v90, v109, v107\n"
"v_mfma_f32_32x32x16_bf16 v[176:191], a[116:119], a[180:183], v[176:191]\n"
"ds_read_b128 a[168:171], v12 offset:57344\n"
"buffer_load_dwordx4 a[232:235], v44, s[40:43], s3 offen offset:2048 sc1\n"
"v_mul_f32 v91, v109, v108\n"
"v_mul_f32 v90, 0x3fb8aa3b, v90\n"
"v_mul_f32 v91, 0.15915494, v91\n"
"v_mfma_f32_32x32x16_bf16 v[112:127], a[184:187], a[116:119], v[112:127]\n"
"s_waitcnt lgkmcnt(3)\n"
"ds_read_b64_tr_b16 a[172:173], v18 offset:49152\n"
"ds_read_b64_tr_b16 a[174:175], v18 offset:49664\n"
"v_exp_f32 v92, v90\n"
"v_fract_f32 v91, v91\n"
"v_mfma_f32_32x32x16_bf16 v[128:143], a[188:191], a[116:119], v[128:143]\n"
"ds_read_b64_tr_b16 a[176:177], v19 offset:49152\n"
"ds_read_b64_tr_b16 a[178:179], v19 offset:49664\n"
"buffer_load_dwordx4 a[236:239], v44, s[40:43], s3 offen offset:3072 sc1\n"
"v_cos_f32 v90, v91\n"
"v_mfma_f32_32x32x16_bf16 v[144:159], a[160:163], a[116:119], v[144:159]\n"
"s_waitcnt lgkmcnt(4)\n"
"ds_read_b64_tr_b16 a[180:181], v20 offset:49152\n"
"ds_read_b64_tr_b16 a[182:183], v20 offset:49664\n"
"buffer_load_dwordx4 a[240:243], v44, s[40:43], s2 offen offset:0 sc1\n"
"v_sin_f32 v105, v91\n"
"v_mfma_f32_32x32x16_bf16 v[160:175], a[164:167], a[116:119], v[160:175]\n"
"ds_read_b64_tr_b16 a[184:185], v21 offset:49152\n"
"ds_read_b64_tr_b16 a[186:187], v21 offset:49664\n"
"v_add_f32 v110, v110, v110\n"
"v_mul_f32 v90, v90, v92\n"
"v_mul_f32 v91, v105, v92\n"
"v_add_f32 v111, -1.0, v90\n"
"v_mfma_f32_32x32x16_bf16 v[176:191], a[120:123], a[168:171], v[176:191]\n"
"s_waitcnt lgkmcnt(4)\n"
"ds_read_b128 a[188:191], v13 offset:57344\n"
"buffer_load_dwordx4 v35, s[20:23], s66 offen offset:1024 lds\n"
"v_mul_f32 v94, v111, v107\n"
"v_fmac_f32 v94, v91, v108\n"
"v_mul_f32 v94, v94, v110\n"
"v_mfma_f32_32x32x16_bf16 v[112:127], a[172:175], a[120:123], v[112:127]\n"
"ds_read_b64_tr_b16 a[160:161], v18 offset:53248\n"
"ds_read_b64_tr_b16 a[162:163], v18 offset:53760\n"
"v_mul_f32 v95, v111, v108\n"
"v_fma_f32 v95, -v91, v107, v95\n"
"v_mul_f32 v95, v95, v110\n"
"v_mov_b32 v46, v90\n"
"v_mfma_f32_32x32x16_bf16 v[128:143], a[176:179], a[120:123], v[128:143]\n"
"s_waitcnt lgkmcnt(3)\n"
"s_waitcnt vmcnt(21)\n"
"ds_read_b128 a[128:131], v26 offset:32768\n"
"ds_read_b128 a[132:135], v27 offset:32768\n"
"ds_read_b64_tr_b16 a[164:165], v19 offset:53248\n"
"ds_read_b64_tr_b16 a[166:167], v19 offset:53760\n"
"buffer_load_dwordx4 v36, s[24:27], s66 offen offset:2048 lds\n"
"v_mfma_f32_32x32x16_bf16 v[144:159], a[180:183], a[120:123], v[144:159]\n"
"ds_read_b128 a[136:139], v28 offset:32768\n"
"ds_read_b128 a[140:143], v29 offset:32768\n"
"ds_read_b64_tr_b16 a[168:169], v20 offset:53248\n"
"ds_read_b64_tr_b16 a[170:171], v20 offset:53760\n"
"v_mov_b32 v47, v91\n"
"v_mov_b32 v48, v94\n"
"v_mfma_f32_32x32x16_bf16 v[160:175], a[184:187], a[120:123], v[160:175]\n"
"s_waitcnt vmcnt(17)\n"
"s_barrier\n"
"s_waitcnt lgkmcnt(8)\n"
"ds_read_b64_tr_b16 a[172:173], v21 offset:53248\n"
"ds_read_b64_tr_b16 a[174:175], v21 offset:53760\n"
"buffer_load_dwordx4 v37, s[24:27], s66 offen offset:3072 lds\n"
"s_mul_i32 s66, s69, 1536\n"
"v_mfma_f32_32x32x16_bf16 v[176:191], a[124:127], a[188:191], v[176:191]\n"
"ds_read_b128 a[176:179], v14 offset:16384\n"
"s_add_u32 s66, s66, 128\n"
"s_add_u32 m0, s57, 20480\n"
"v_mov_b32 v49, v95\n"
"v_mul_f32 v92, v90, v90\n"
"v_add_f32 v93, v90, v90\n"
"v_mfma_f32_32x32x16_bf16 v[112:127], a[160:163], a[124:127], v[112:127]\n"
"s_waitcnt lgkmcnt(3)\n"
"ds_read_b64_tr_b16 a[180:181], v22 offset:0\n"
"ds_read_b64_tr_b16 a[182:183], v22 offset:512\n"
"buffer_load_dwordx4 v34, s[20:23], s66 offen offset:0 lds\n"
"v_fma_f32 v92, -v91, v91, v92\n"
"v_mul_f32 v93, v93, v91\n"
"v_mfma_f32_32x32x16_bf16 v[128:143], a[164:167], a[124:127], v[128:143]\n"
"ds_read_b64_tr_b16 a[184:185], v23 offset:0\n"
"ds_read_b64_tr_b16 a[186:187], v23 offset:512\n"
"v_mul_f32 v98, v92, v92\n"
"v_add_f32 v99, v92, v92\n"
"v_fma_f32 v98, -v93, v93, v98\n"
"v_mul_f32 v99, v99, v93\n"
"v_mfma_f32_32x32x16_bf16 v[144:159], a[168:171], a[124:127], v[144:159]\n"
"s_waitcnt lgkmcnt(4)\n"
"ds_read_b64_tr_b16 a[188:189], v24 offset:0\n"
"ds_read_b64_tr_b16 a[190:191], v24 offset:512\n"
"buffer_load_dwordx4 v35, s[20:23], s66 offen offset:1024 lds\n"
"v_mul_f32 v66, v98, v98\n"
"v_add_f32 v67, v98, v98\n"
"v_mfma_f32_32x32x16_bf16 v[160:175], a[172:175], a[124:127], v[160:175]\n"
"ds_read_b64_tr_b16 a[160:161], v25 offset:0\n"
"ds_read_b64_tr_b16 a[162:163], v25 offset:512\n"
"v_fma_f32 v66, -v99, v99, v66\n"
"v_mul_f32 v67, v67, v99\n"
"v_mul_f32 v98, v66, v66\n"
"v_add_f32 v99, v66, v66\n"
"v_mfma_f32_32x32x16_bf16 v[176:191], a[128:131], a[176:179], v[176:191]\n"
"s_waitcnt lgkmcnt(4)\n"
"ds_read_b128 a[164:167], v15 offset:16384\n"
"buffer_load_dwordx4 v36, s[24:27], s66 offen offset:2048 lds\n"
"v_fma_f32 v98, -v67, v67, v98\n"
"v_mul_f32 v99, v99, v67\n"
"v_mul_f32 v68, v98, v98\n"
"v_mfma_f32_32x32x16_bf16 v[112:127], a[180:183], a[128:131], v[112:127]\n"
"ds_read_b64_tr_b16 a[168:169], v22 offset:4096\n"
"ds_read_b64_tr_b16 a[170:171], v22 offset:4608\n"
"v_add_f32 v69, v98, v98\n"
"v_fma_f32 v68, -v99, v99, v68\n"
"v_mul_f32 v69, v69, v99\n"
"v_mul_f32 v70, v68, v68\n"
"v_mfma_f32_32x32x16_bf16 v[128:143], a[184:187], a[128:131], v[128:143]\n"
"s_waitcnt lgkmcnt(3)\n"
"ds_read_b64_tr_b16 a[172:173], v23 offset:4096\n"
"ds_read_b64_tr_b16 a[174:175], v23 offset:4608\n"
"buffer_load_dwordx4 v37, s[24:27], s66 offen offset:3072 lds\n"
"v_add_f32 v71, v68, v68\n"
"v_fma_f32 v70, -v69, v69, v70\n"
"v_mfma_f32_32x32x16_bf16 v[144:159], a[188:191], a[128:131], v[144:159]\n"
"ds_read_b64_tr_b16 a[176:177], v24 offset:4096\n"
"ds_read_b64_tr_b16 a[178:179], v24 offset:4608\n"
"v_mul_f32 v71, v71, v69\n"
"v_mul_f32 v98, v70, v70\n"
"v_add_f32 v99, v70, v70\n"
"v_fma_f32 v98, -v71, v71, v98\n"
"v_mfma_f32_32x32x16_bf16 v[160:175], a[160:163], a[128:131], v[160:175]\n"
"s_waitcnt lgkmcnt(4)\n"
"ds_read_b64_tr_b16 a[180:181], v25 offset:4096\n"
"ds_read_b64_tr_b16 a[182:183], v25 offset:4608\n"
"v_mul_f32 v99, v99, v71\n"
"v_mul_f32 v104, v98, v98\n"
"v_add_f32 v105, v98, v98\n"
"v_fma_f32 v104, -v99, v99, v104\n"
"v_mfma_f32_32x32x16_bf16 v[176:191], a[132:135], a[164:167], v[176:191]\n"
"ds_read_b128 a[184:187], v16 offset:16384\n"
"v_mul_f32 v105, v105, v99\n"
"v_mul_f32 v100, v104, v104\n"
"v_add_f32 v101, v104, v104\n"
"v_fma_f32 v100, -v105, v105, v100\n"
"v_mul_f32 v101, v101, v105\n"
"v_mfma_f32_32x32x16_bf16 v[112:127], a[168:171], a[132:135], v[112:127]\n"
"s_waitcnt lgkmcnt(3)\n"
"ds_read_b64_tr_b16 a[188:189], v22 offset:8192\n"
"ds_read_b64_tr_b16 a[190:191], v22 offset:8704\n"
"v_mul_f32 v102, v100, v100\n"
"v_add_f32 v103, v100, v100\n"
"v_fma_f32 v102, -v101, v101, v102\n"
"v_mul_f32 v103, v103, v101\n"
"v_mfma_f32_32x32x16_bf16 v[128:143], a[172:175], a[132:135], v[128:143]\n"
"ds_read_b64_tr_b16 a[160:161], v23 offset:8192\n"
"ds_read_b64_tr_b16 a[162:163], v23 offset:8704\n"
"v_cmp_le_u32 vcc, 32, v2\n"
"v_mov_b32 v108, 1.0\n"
"v_and_b32 v107, 16, v2\n"
"v_cndmask_b32 v72, v108, v68, vcc\n"
"v_mfma_f32_32x32x16_bf16 v[144:159], a[176:179], a[132:135], v[144:159]\n"
"s_waitcnt lgkmcnt(4)\n"
"ds_read_b64_tr_b16 a[164:165], v24 offset:8192\n"
"ds_read_b64_tr_b16 a[166:167], v24 offset:8704\n"
"v_cndmask_b32 v73, 0, v69, vcc\n"
"v_cmp_ne_u32 vcc, 0, v107\n"
"v_nop\n"
"v_nop\n"
"v_mfma_f32_32x32x16_bf16 v[160:175], a[180:183], a[132:135], v[160:175]\n"
"ds_read_b64_tr_b16 a[168:169], v25 offset:8192\n"
"ds_read_b64_tr_b16 a[170:171], v25 offset:8704\n"
"v_cndmask_b32 v108, v108, v70, vcc\n"
"v_cndmask_b32 v109, 0, v71, vcc\n"
"v_mul_f32 v104, v72, v108\n"
"v_mul_f32 v105, v72, v109\n"
"v_mfma_f32_32x32x16_bf16 v[176:191], a[136:139], a[184:187], v[176:191]\n"
"s_waitcnt lgkmcnt(4)\n"
"ds_read_b128 a[172:175], v17 offset:16384\n"
"v_fma_f32 v104, -v73, v109, v104\n"
"v_fmac_f32 v105, v73, v108\n"
"v_cndmask_b32 v72, 0, v72, vcc\n"
"v_cndmask_b32 v73, 0, v73, vcc\n"
"v_accvgpr_write_b32 a200, v98\n"
"v_mfma_f32_32x32x16_bf16 v[112:127], a[188:191], a[136:139], v[112:127]\n"
"ds_read_b64_tr_b16 a[176:177], v22 offset:12288\n"
"ds_read_b64_tr_b16 a[178:179], v22 offset:12800\n"
"v_accvgpr_write_b32 a201, v99\n"
"v_accvgpr_write_b32 a202, v100\n"
"v_accvgpr_write_b32 a203, v101\n"
"v_mfma_f32_32x32x16_bf16 v[128:143], a[160:163], a[136:139], v[128:143]\n"
"s_waitcnt lgkmcnt(3)\n"
"s_waitcnt vmcnt(18)\n"
"ds_read_b128 a[144:147], v26 offset:0\n"
"ds_read_b128 a[148:151], v27 offset:0\n"
"ds_read_b64_tr_b16 a[180:181], v23 offset:12288\n"
"ds_read_b64_tr_b16 a[182:183], v23 offset:12800\n"
"v_mfma_f32_32x32x16_bf16 v[144:159], a[164:167], a[136:139], v[144:159]\n"
"ds_read_b128 a[152:155], v28 offset:0\n"
"ds_read_b128 a[156:159], v29 offset:0\n"
"ds_read_b64_tr_b16 a[184:185], v24 offset:12288\n"
"ds_read_b64_tr_b16 a[186:187], v24 offset:12800\n"
"v_accvgpr_write_b32 a204, v102\n"
"v_mfma_f32_32x32x16_bf16 v[160:175], a[168:171], a[136:139], v[160:175]\n"
"s_waitcnt vmcnt(13)\n"
"s_barrier\n"
"s_waitcnt lgkmcnt(8)\n"
"ds_read_b64_tr_b16 a[188:189], v25 offset:12288\n"
"ds_read_b64_tr_b16 a[190:191], v25 offset:12800\n"
"s_mul_i32 s66, s70, 1536\n"
"s_add_u32 s66, s66, 128\n"
"s_add_u32 m0, s57, 40960\n"
"v_mfma_f32_32x32x16_bf16 v[176:191], a[140:143], a[172:175], v[176:191]\n"
"ds_read_b128 a[160:163], v14 offset:36864\n"
"buffer_load_dwordx4 v34, s[20:23], s66 offen offset:0 lds\n"
"s_waitcnt vmcnt(12)\n"
"v_accvgpr_read_b32 v77, a225\n"
"v_accvgpr_read_b32 v78, a227\n"
"v_xor_b32 v77, s56, v77\n"
"v_mfma_f32_32x32x16_bf16 v[112:127], a[176:179], a[140:143], v[112:127]\n"
"s_waitcnt lgkmcnt(3)\n"
"ds_read_b64_tr_b16 a[164:165], v22 offset:20480\n"
"ds_read_b64_tr_b16 a[166:167], v22 offset:20992\n"
"v_xor_b32 v78, s56, v78\n"
"v_or_b32 v76, v77, v78\n"
"s_waitcnt vmcnt(11)\n"
"v_accvgpr_read_b32 v77, a229\n"
"v_accvgpr_read_b32 v78, a231\n"
"v_mfma_f32_32x32x16_bf16 v[128:143], a[180:183], a[140:143], v[128:143]\n"
"ds_read_b64_tr_b16 a[168:169], v23 offset:20480\n"
"ds_read_b64_tr_b16 a[170:171], v23 offset:20992\n"
"buffer_load_dwordx4 v35, s[20:23], s66 offen offset:1024 lds\n"
"v_xor_b32 v77, s56, v77\n"
"v_xor_b32 v78, s56, v78\n"
"v_mfma_f32_32x32x16_bf16 v[144:159], a[184:187], a[140:143], v[144:159]\n"
"s_waitcnt lgkmcnt(4)\n"
"ds_read_b64_tr_b16 a[172:173], v24 offset:20480\n"
"ds_read_b64_tr_b16 a[174:175], v24 offset:20992\n"
"v_or3_b32 v76, v76, v77, v78\n"
"s_waitcnt vmcnt(11)\n"
"v_accvgpr_read_b32 v77, a233\n"
"v_accvgpr_read_b32 v78, a235\n"
"v_mfma_f32_32x32x16_bf16 v[160:175], a[188:191], a[140:143], v[160:175]\n"
"ds_read_b64_tr_b16 a[176:177], v25 offset:20480\n"
"ds_read_b64_tr_b16 a[178:179], v25 offset:20992\n"
"buffer_load_dwordx4 v36, s[24:27], s66 offen offset:2048 lds\n"
"v_xor_b32 v77, s56, v77\n"
"v_xor_b32 v78, s56, v78\n"
"v_mfma_f32_32x32x16_bf16 v[176:191], a[144:147], a[160:163], v[176:191]\n"
"s_waitcnt lgkmcnt(4)\n"
"ds_read_b128 a[180:183], v15 offset:36864\n"
"v_or3_b32 v76, v76, v77, v78\n"
"s_waitcnt vmcnt(11)\n"
"v_accvgpr_read_b32 v77, a237\n"
"v_accvgpr_read_b32 v78, a239\n"
"v_xor_b32 v77, s56, v77\n"
"v_xor_b32 v78, s56, v78\n"
"v_mfma_f32_32x32x16_bf16 v[112:127], a[164:167], a[144:147], v[112:127]\n"
"ds_read_b64_tr_b16 a[184:185], v22 offset:24576\n"
"ds_read_b64_tr_b16 a[186:187], v22 offset:25088\n"
"buffer_load_dwordx4 v37, s[24:27], s66 offen offset:3072 lds\n"
"v_or3_b32 v76, v76, v77, v78\n"
"s_waitcnt vmcnt(11)\n"
"v_accvgpr_read_b32 v77, a241\n"
"v_mfma_f32_32x32x16_bf16 v[128:143], a[168:171], a[144:147], v[128:143]\n"
"s_waitcnt lgkmcnt(3)\n"
"ds_read_b64_tr_b16 a[188:189], v23 offset:24576\n"
"ds_read_b64_tr_b16 a[190:191], v23 offset:25088\n"
"v_accvgpr_read_b32 v78, a243\n"
"v_xor_b32 v77, s56, v77\n"
"v_xor_b32 v78, s56, v78\n"
"v_or3_b32 v76, v76, v77, v78\n"
"v_mfma_f32_32x32x16_bf16 v[144:159], a[172:175], a[144:147], v[144:159]\n"
"ds_read_b64_tr_b16 a[160:161], v24 offset:24576\n"
"ds_read_b64_tr_b16 a[162:163], v24 offset:25088\n"
"v_cmp_ne_u32 s[0:1], 0, v76\n"
"s_nop 0\n"
"s_cmp_lg_u64 s[0:1], 0\n"
"s_cbranch_scc1 Lcs0A_slow%=\n"
"Lcs0A_ok%=:\n"
"v_accvgpr_write_b32 a225, 0\n"
"v_mfma_f32_32x32x16_bf16 v[160:175], a[176:179], a[144:147], v[160:175]\n"
"s_waitcnt lgkmcnt(4)\n"
"ds_read_b64_tr_b16 a[164:165], v25 offset:24576\n"
"ds_read_b64_tr_b16 a[166:167], v25 offset:25088\n"
"v_accvgpr_write_b32 a227, 0\n"
"v_accvgpr_write_b32 a229, 0\n"
"v_accvgpr_write_b32 a231, 0\n"
"v_mfma_f32_32x32x16_bf16 v[176:191], a[148:151], a[180:183], v[176:191]\n"
"v_accvgpr_write_b32 a233, 0\n"
"v_accvgpr_write_b32 a235, 0\n"
"v_accvgpr_write_b32 a237, 0\n"
"v_accvgpr_write_b32 a239, 0\n"
"v_accvgpr_write_b32 a241, 0\n"
"v_mfma_f32_32x32x16_bf16 v[112:127], a[184:187], a[148:151], v[112:127]\n"
"s_waitcnt lgkmcnt(2)\n"
"v_accvgpr_write_b32 a243, 0\n"
"v_accvgpr_write_b32 a205, v103\n"
"v_accvgpr_write_b32 a206, v104\n"
"v_accvgpr_write_b32 a207, v105\n"
"v_mul_f32 v90, v46, v46\n"
"v_add_f32 v91, v46, v46\n"
"v_mfma_f32_32x32x16_bf16 v[128:143], a[188:191], a[148:151], v[128:143]\n"
"v_fma_f32 v90, -v47, v47, v90\n"
"v_mul_f32 v91, v91, v47\n"
"v_mfma_f32_32x32x16_bf16 v[144:159], a[160:163], a[148:151], v[144:159]\n"
"s_waitcnt lgkmcnt(0)\n"
"v_accvgpr_read_b32 v30, a192\n"
"v_accvgpr_read_b32 v31, a193\n"
"v_accvgpr_read_b32 v32, a194\n"
"v_accvgpr_read_b32 v33, a195\n"
"v_accvgpr_read_b32 v38, a196\n"
"v_mfma_f32_32x32x16_bf16 v[160:175], a[164:167], a[148:151], v[160:175]\n"
"v_accvgpr_read_b32 v39, a197\n"
"v_accvgpr_read_b32 v26, a198\n"
"v_add_u32 v27, 61440, v26\n"
"v_add_u32 v80, 61440, v27\n"
"s_and_b32 s78, s54, 1\n"
"s_lshl_b32 s78, s78, 6\n"
"v_mfma_f32_32x32x16_bf16 v[176:191], v[54:57], a[240:243], v[176:191]\n"
"s_and_b32 s79, s55, 1\n"
"s_lshl_b32 s79, s79, 5\n"
"s_or_b32 s78, s78, s79\n"
"v_xor_b32 v28, s78, v14\n"
"v_xor_b32 v29, s78, v15\n"
"v_mfma_f32_32x32x16_bf16 v[112:127], a[224:227], v[54:57], v[112:127]\n"
"ds_read_b128 a[168:171], v16 offset:36864\n"
"v_mfma_f32_32x32x16_bf16 v[128:143], a[228:231], v[54:57], v[128:143]\n"
"ds_read_b64_tr_b16 a[172:173], v22 offset:28672\n"
"ds_read_b64_tr_b16 a[174:175], v22 offset:29184\n"
"v_mfma_f32_32x32x16_bf16 v[144:159], a[232:235], v[54:57], v[144:159]\n"
"ds_read_b64_tr_b16 a[176:177], v23 offset:28672\n"
"ds_read_b64_tr_b16 a[178:179], v23 offset:29184\n"
"v_mfma_f32_32x32x16_bf16 v[160:175], a[236:239], v[54:57], v[160:175]\n"
"s_waitcnt lgkmcnt(4)\n"
"ds_read_b64_tr_b16 a[180:181], v24 offset:28672\n"
"ds_read_b64_tr_b16 a[182:183], v24 offset:29184\n"
"v_mfma_f32_32x32x2_f32 v[96:111], v5, v59, 0\n"
"ds_read_b64_tr_b16 a[184:185], v25 offset:28672\n"
"ds_read_b64_tr_b16 a[186:187], v25 offset:29184\n"
"v_mfma_f32_32x32x16_bf16 v[176:191], a[152:155], a[168:171], v[176:191]\n"
"s_waitcnt lgkmcnt(4)\n"
"ds_read_b128 a[188:191], v17 offset:36864\n"
"v_mfma_f32_32x32x16_bf16 v[112:127], a[172:175], a[152:155], v[112:127]\n"
"ds_read_b64_tr_b16 a[160:161], v22 offset:32768\n"
"ds_read_b64_tr_b16 a[162:163], v22 offset:33280\n"
"v_mfma_f32_32x32x16_bf16 v[128:143], a[176:179], a[152:155], v[128:143]\n"
"s_waitcnt lgkmcnt(3)\n"
"ds_read_b64_tr_b16 a[164:165], v23 offset:32768\n"
"ds_read_b64_tr_b16 a[166:167], v23 offset:33280\n"
"v_mfma_f32_32x32x16_bf16 v[144:159], a[180:183], a[152:155], v[144:159]\n"
"ds_read_b64_tr_b16 a[168:169], v24 offset:32768\n"
"ds_read_b64_tr_b16 a[170:171], v24 offset:33280\n"
"v_mfma_f32_32x32x16_bf16 v[160:175], a[184:187], a[152:155], v[160:175]\n"
"s_waitcnt vmcnt(8)\n"
"s_barrier\n"
"s_waitcnt lgkmcnt(4)\n"
"ds_read_b64_tr_b16 a[172:173], v25 offset:32768\n"
"ds_read_b64_tr_b16 a[174:175], v25 offset:33280\n"
"s_mul_i32 s66, s71, 1536\n"
"s_add_u32 s66, s66, 128\n"
"s_add_u32 m0, s57, 61440\n"
"v_mfma_f32_32x32x16_bf16 v[176:191], a[156:159], a[188:191], v[176:191]\n"
"ds_read_b64_tr_b16 a[176:177], v18 offset:0\n"
"ds_read_b64_tr_b16 a[178:179], v18 offset:512\n"
"buffer_load_dwordx4 v34, s[20:23], s66 offen offset:0 lds\n"
"v_mfma_f32_32x32x16_bf16 v[112:127], a[160:163], a[156:159], v[112:127]\n"
"s_waitcnt lgkmcnt(4)\n"
"ds_read_b64_tr_b16 a[180:181], v19 offset:0\n"
"ds_read_b64_tr_b16 a[182:183], v19 offset:512\n"
"v_mfma_f32_32x32x16_bf16 v[128:143], a[164:167], a[156:159], v[128:143]\n"
"ds_read_b64_tr_b16 a[184:185], v8 offset:0\n"
"ds_read_b64_tr_b16 a[186:187], v8 offset:512\n"
"buffer_load_dwordx4 v35, s[20:23], s66 offen offset:1024 lds\n"
"v_mfma_f32_32x32x16_bf16 v[144:159], a[168:171], a[156:159], v[144:159]\n"
"s_waitcnt lgkmcnt(4)\n"
"ds_read_b64_tr_b16 a[188:189], v20 offset:0\n"
"ds_read_b64_tr_b16 a[190:191], v20 offset:512\n"
"v_mfma_f32_32x32x16_bf16 v[160:175], a[172:175], a[156:159], v[160:175]\n"
"ds_read_b64_tr_b16 a[160:161], v21 offset:0\n"
"ds_read_b64_tr_b16 a[162:163], v21 offset:512\n"
"buffer_load_dwordx4 v36, s[24:27], s66 offen offset:2048 lds\n"
"v_mfma_f32_32x32x16_bf16 v[192:207], a[176:179], a[0:3], 0\n"
"s_waitcnt lgkmcnt(4)\n"
"ds_read_b64_tr_b16 a[164:165], v18 offset:4096\n"
"ds_read_b64_tr_b16 a[166:167], v18 offset:4608\n"
"v_pk_mul_f32 v[176:177], v[176:177], v[96:97]\n"
"v_pk_mul_f32 v[178:179], v[178:179], v[98:99]\n"
"v_pk_mul_f32 v[180:181], v[180:181], v[100:101]\n"
"v_pk_mul_f32 v[182:183], v[182:183], v[102:103]\n"
"v_pk_mul_f32 v[184:185], v[184:185], v[104:105]\n"
"v_pk_mul_f32 v[186:187], v[186:187], v[106:107]\n"
"v_pk_mul_f32 v[188:189], v[188:189], v[108:109]\n"
"v_pk_mul_f32 v[190:191], v[190:191], v[110:111]\n"
"v_permlane16_swap_b32 v176, v184\n"
"v_permlane16_swap_b32 v177, v185\n"
"v_permlane16_swap_b32 v178, v186\n"
"v_permlane16_swap_b32 v179, v187\n"
"v_permlane16_swap_b32 v180, v188\n"
"v_permlane16_swap_b32 v181, v189\n"
"v_permlane16_swap_b32 v182, v190\n"
"v_permlane16_swap_b32 v183, v191\n"
"v_pk_fma_f32 v[106:107], v[90:91], v[176:177], v[178:179] op_sel:[0,0,0] op_sel_hi:[0,1,1]\n"
"v_pk_fma_f32 v[108:109], v[90:91], v[184:185], v[186:187] op_sel:[0,0,0] op_sel_hi:[0,1,1]\n"
"v_pk_fma_f32 v[106:107], v[90:91], v[184:185], v[106:107] op_sel:[1,0,0] op_sel_hi:[1,1,1] neg_lo:[1,0,0] neg_hi:[1,0,0]\n"
"v_pk_fma_f32 v[108:109], v[90:91], v[176:177], v[108:109] op_sel:[1,0,0] op_sel_hi:[1,1,1]\n"
"v_pk_fma_f32 v[102:103], v[90:91], v[106:107], v[180:181] op_sel:[0,0,0] op_sel_hi:[0,1,1]\n"
"v_pk_fma_f32 v[104:105], v[90:91], v[108:109], v[188:189] op_sel:[0,0,0] op_sel_hi:[0,1,1]\n"
"v_pk_fma_f32 v[102:103], v[90:91], v[108:109], v[102:103] op_sel:[1,0,0] op_sel_hi:[1,1,1] neg_lo:[1,0,0] neg_hi:[1,0,0]\n"
"v_pk_fma_f32 v[104:105], v[90:91], v[106:107], v[104:105] op_sel:[1,0,0] op_sel_hi:[1,1,1]\n"
"v_pk_fma_f32 v[106:107], v[90:91], v[102:103], v[182:183] op_sel:[0,0,0] op_sel_hi:[0,1,1]\n"
"v_pk_fma_f32 v[108:109], v[90:91], v[104:105], v[190:191] op_sel:[0,0,0] op_sel_hi:[0,1,1]\n"
"v_pk_fma_f32 v[106:107], v[90:91], v[104:105], v[106:107] op_sel:[1,0,0] op_sel_hi:[1,1,1] neg_lo:[1,0,0] neg_hi:[1,0,0]\n"
"v_pk_fma_f32 v[108:109], v[90:91], v[102:103], v[108:109] op_sel:[1,0,0] op_sel_hi:[1,1,1]\n"
"v_fma_f32 v110, v46, v106, v107\n"
"v_fma_f32 v111, v46, v108, v109\n"
"v_fma_f32 v110, -v47, v108, v110\n"
"v_fmac_f32 v111, v47, v106\n"
"ds_write_b64 v74, v[110:111] offset:0\n"
"s_mul_i32 s60, s53, 10240\n"
"s_lshl_b32 s61, s63, 8\n"
"s_add_u32 s61, s61, 0x1720000\n"
"s_add_u32 s60, s60, s61\n"
"s_cmp_eq_u32 s52, 0\n"
"s_cselect_b32 s60, s60, 0x70000000\n"
"s_waitcnt lgkmcnt(0)\n"
"s_barrier\n"
"ds_read_b128 v[90:93], v75\n"
"ds_read_b128 v[94:97], v75 offset:16\n"
"v_cmp_gt_u32 vcc, 16, v2\n"
"v_mov_b32 v106, 0x70000000\n"
"v_mov_b32 v99, s56\n"
"v_mov_b32 v101, s56\n"
"v_cndmask_b32 v106, v106, v44, vcc\n"
"s_waitcnt lgkmcnt(1)\n"
"v_fmac_f32 v92, v90, v66\n"
"v_fmac_f32 v93, v90, v67\n"
"v_fma_f32 v92, -v91, v67, v92\n"
"v_fmac_f32 v93, v91, v66\n"
"s_waitcnt lgkmcnt(0)\n"
"v_fmac_f32 v94, v92, v66\n"
"v_fmac_f32 v95, v92, v67\n"
"v_fma_f32 v94, -v93, v67, v94\n"
"v_fmac_f32 v95, v93, v66\n"
"v_fmac_f32 v96, v94, v66\n"
"v_fmac_f32 v97, v94, v67\n"
"v_fma_f32 v96, -v95, v67, v96\n"
"v_fmac_f32 v97, v95, v66\n"
"v_mov_b32 v90, v96\n"
"v_mov_b32 v91, v97\n"
"s_nop 0\n"
"v_permlane32_swap_b32 v96, v90\n"
"v_permlane32_swap_b32 v97, v91\n"
"v_fmac_f32 v90, v68, v96\n"
"v_fmac_f32 v91, v68, v97\n"
"v_fma_f32 v90, -v69, v97, v90\n"
"v_fmac_f32 v91, v69, v96\n"
"v_mov_b32 v92, v90\n"
"v_mov_b32 v93, v91\n"
"s_nop 0\n"
"v_permlane16_swap_b32 v90, v92\n"
"v_permlane16_swap_b32 v91, v93\n"
"v_fmac_f32 v92, v70, v90\n"
"v_fmac_f32 v93, v70, v91\n"
"v_fma_f32 v92, -v71, v91, v92\n"
"v_fmac_f32 v93, v71, v90\n"
"v_mov_b32 v98, v92\n"
"v_mov_b32 v100, v93\n"
"buffer_store_dwordx4 v[98:101], v106, s[40:43], s60 offen sc1\n"
"v_mul_f32 v86, v53, v96\n"
"v_mul_f32 v87, v53, v97\n"
"v_fmac_f32 v86, v72, v90\n"
"v_fmac_f32 v87, v72, v91\n"
"v_fma_f32 v86, -v73, v91, v86\n"
"v_fmac_f32 v87, v73, v90\n"
"v_mfma_f32_32x32x16_bf16 v[208:223], a[180:183], a[0:3], 0\n"
"ds_read_b64_tr_b16 a[168:169], v19 offset:4096\n"
"ds_read_b64_tr_b16 a[170:171], v19 offset:4608\n"
"s_add_u32 s67, s64, 0\n"
"s_lshl_b32 s67, s67, 12\n"
"s_add_u32 s67, s67, s65\n"
"s_add_u32 s67, s67, 81920\n"
"v_mfma_f32_16x16x32_bf16 a[248:251], a[244:247], a[184:187], 0\n"
"ds_read_b64_tr_b16 a[172:173], v8 offset:4096\n"
"ds_read_b64_tr_b16 a[174:175], v8 offset:4608\n"
"v_mfma_f32_32x32x16_bf16 v[224:239], a[188:191], a[0:3], 0\n"
"ds_read_b64_tr_b16 a[176:177], v20 offset:4096\n"
"ds_read_b64_tr_b16 a[178:179], v20 offset:4608\n"
"buffer_load_dwordx4 v37, s[24:27], s66 offen offset:3072 lds\n"
"s_mul_i32 s66, s72, 1536\n"
"s_add_u32 s66, s66, 128\n"
"s_add_u32 m0, s57, 102400\n"
"v_mfma_f32_32x32x16_bf16 v[240:255], a[160:163], a[0:3], 0\n"
"s_waitcnt lgkmcnt(4)\n"
"ds_read_b64_tr_b16 a[180:181], v21 offset:4096\n"
"ds_read_b64_tr_b16 a[182:183], v21 offset:4608\n"
"v_accvgpr_read_b32 v58, a208\n"
"v_accvgpr_read_b32 v59, a209\n"
"v_accvgpr_read_b32 v60, a210\n"
"v_mfma_f32_32x32x16_bf16 v[192:207], a[164:167], a[4:7], v[192:207]\n"
"ds_read_b64_tr_b16 a[184:185], v18 offset:8192\n"
"ds_read_b64_tr_b16 a[186:187], v18 offset:8704\n"
"buffer_load_dwordx4 v34, s[20:23], s66 offen offset:0 lds\n"
"v_accvgpr_read_b32 v61, a211\n"
"v_accvgpr_read_b32 v62, a212\n"
"v_mfma_f32_32x32x16_bf16 v[208:223], a[168:171], a[4:7], v[208:223]\n"
"s_waitcnt lgkmcnt(4)\n"
"ds_read_b64_tr_b16 a[188:189], v19 offset:8192\n"
"ds_read_b64_tr_b16 a[190:191], v19 offset:8704\n"
"v_accvgpr_read_b32 v63, a213\n"
"v_accvgpr_read_b32 v64, a214\n"
"v_accvgpr_read_b32 v65, a215\n"
"v_mfma_f32_16x16x32_bf16 a[248:251], a[244:247], a[172:175], a[248:251]\n"
"ds_read_b64_tr_b16 a[160:161], v8 offset:8192\n"
"ds_read_b64_tr_b16 a[162:163], v8 offset:8704\n"
"buffer_load_dwordx4 v35, s[20:23], s66 offen offset:1024 lds\n"
"v_mfma_f32_32x32x16_bf16 v[224:239], a[176:179], a[4:7], v[224:239]\n"
"s_waitcnt lgkmcnt(4)\n"
"ds_read_b64_tr_b16 a[164:165], v20 offset:8192\n"
"ds_read_b64_tr_b16 a[166:167], v20 offset:8704\n"
"v_accvgpr_read_b32 v68, a200\n"
"v_accvgpr_read_b32 v69, a201\n"
"v_accvgpr_read_b32 v70, a202\n"
"v_mfma_f32_32x32x16_bf16 v[240:255], a[180:183], a[4:7], v[240:255]\n"
"ds_read_b64_tr_b16 a[168:169], v21 offset:8192\n"
"ds_read_b64_tr_b16 a[170:171], v21 offset:8704\n"
"buffer_load_dwordx4 v36, s[24:27], s66 offen offset:2048 lds\n"
"v_accvgpr_read_b32 v71, a203\n"
"v_accvgpr_read_b32 v72, a204\n"
"v_mfma_f32_32x32x16_bf16 v[192:207], a[184:187], a[8:11], v[192:207]\n"
"s_waitcnt lgkmcnt(4)\n"
"ds_read_b64_tr_b16 a[172:173], v18 offset:12288\n"
"ds_read_b64_tr_b16 a[174:175], v18 offset:12800\n"
"v_accvgpr_read_b32 v73, a205\n"
"v_accvgpr_read_b32 v110, a206\n"
"v_accvgpr_read_b32 v111, a207\n"
"v_mfma_f32_32x32x16_bf16 v[208:223], a[188:191], a[8:11], v[208:223]\n"
"ds_read_b64_tr_b16 a[176:177], v19 offset:12288\n"
"ds_read_b64_tr_b16 a[178:179], v19 offset:12800\n"
"buffer_load_dwordx4 v37, s[24:27], s66 offen offset:3072 lds\n"
"s_add_u32 s66, s64, 0\n"
"s_mul_i32 s66, s66, 81920\n"
"s_add_u32 m0, s59, 122880\n"
"v_mfma_f32_16x16x32_bf16 a[248:251], a[244:247], a[160:163], a[248:251]\n"
"s_waitcnt lgkmcnt(4)\n"
"ds_read_b64_tr_b16 a[180:181], v8 offset:12288\n"
"ds_read_b64_tr_b16 a[182:183], v8 offset:12800\n"
"v_mfma_f32_32x32x16_bf16 v[224:239], a[164:167], a[8:11], v[224:239]\n"
"ds_read_b64_tr_b16 a[184:185], v20 offset:12288\n"
"ds_read_b64_tr_b16 a[186:187], v20 offset:12800\n"
"buffer_load_dwordx4 v39, s[28:31], s66 offen offset:0 lds\n"
"v_sub_f32 v106, 0, v48\n"
"v_mul_f32 v53, v48, v176\n"
"v_mfma_f32_32x32x16_bf16 v[240:255], a[168:171], a[8:11], v[240:255]\n"
"s_waitcnt vmcnt(14)\n"
"s_barrier\n"
"s_waitcnt lgkmcnt(4)\n"
"ds_read_b64_tr_b16 a[188:189], v21 offset:12288\n"
"ds_read_b64_tr_b16 a[190:191], v21 offset:12800\n"
"v_fmac_f32 v53, v49, v184\n"
"v_mul_f32 v184, v106, v184\n"
"v_mfma_f32_32x32x16_bf16 v[192:207], a[172:175], a[12:15], v[192:207]\n"
"ds_read_b64_tr_b16 a[160:161], v18 offset:20480\n"
"ds_read_b64_tr_b16 a[162:163], v18 offset:20992\n"
"buffer_load_dwordx4 v40, s[28:31], s66 offen offset:1024 lds\n"
"v_fmac_f32 v184, v49, v176\n"
"v_mul_f32 v176, v48, v177\n"
"v_mfma_f32_32x32x16_bf16 v[208:223], a[176:179], a[12:15], v[208:223]\n"
"s_waitcnt lgkmcnt(4)\n"
"ds_read_b64_tr_b16 a[164:165], v19 offset:20480\n"
"ds_read_b64_tr_b16 a[166:167], v19 offset:20992\n"
"v_fmac_f32 v176, v49, v185\n"
"v_mul_f32 v185, v106, v185\n"
"v_fmac_f32 v185, v49, v177\n"
"v_mul_f32 v177, v48, v178\n"
"v_mfma_f32_16x16x32_bf16 a[248:251], a[244:247], a[180:183], a[248:251]\n"
"ds_read_b64_tr_b16 a[168:169], v8 offset:20480\n"
"ds_read_b64_tr_b16 a[170:171], v8 offset:20992\n"
"buffer_load_dwordx4 v41, s[28:31], s66 offen offset:2048 lds\n"
"v_mfma_f32_32x32x16_bf16 v[224:239], a[184:187], a[12:15], v[224:239]\n"
"s_waitcnt lgkmcnt(4)\n"
"ds_read_b64_tr_b16 a[172:173], v20 offset:20480\n"
"ds_read_b64_tr_b16 a[174:175], v20 offset:20992\n"
"v_fmac_f32 v177, v49, v186\n"
"v_mul_f32 v186, v106, v186\n"
"v_fmac_f32 v186, v49, v178\n"
"v_mul_f32 v178, v48, v179\n"
"v_mfma_f32_32x32x16_bf16 v[240:255], a[188:191], a[12:15], v[240:255]\n"
"ds_read_b64_tr_b16 a[176:177], v21 offset:20480\n"
"ds_read_b64_tr_b16 a[178:179], v21 offset:20992\n"
"v_fmac_f32 v178, v49, v187\n"
"v_mul_f32 v187, v106, v187\n"
"v_fmac_f32 v187, v49, v179\n"
"v_mul_f32 v179, v48, v180\n"
"v_mfma_f32_32x32x16_bf16 v[192:207], a[160:163], a[16:19], v[192:207]\n"
"s_waitcnt lgkmcnt(4)\n"
"ds_read_b64_tr_b16 a[180:181], v18 offset:24576\n"
"ds_read_b64_tr_b16 a[182:183], v18 offset:25088\n"
"buffer_load_dwordx4 v42, s[28:31], s66 offen offset:3072 lds\n"
"s_add_u32 m0, s59, 126976\n"
"v_fmac_f32 v179, v49, v188\n"
"v_mfma_f32_32x32x16_bf16 v[208:223], a[164:167], a[16:19], v[208:223]\n"
"ds_read_b64_tr_b16 a[184:185], v19 offset:24576\n"
"ds_read_b64_tr_b16 a[186:187], v19 offset:25088\n"
"v_mul_f32 v188, v106, v188\n"
"v_fmac_f32 v188, v49, v180\n"
"v_mul_f32 v180, v48, v181\n"
"v_fmac_f32 v180, v49, v189\n"
"v_mfma_f32_16x16x32_bf16 a[248:251], a[244:247], a[168:171], a[248:251]\n"
"s_waitcnt lgkmcnt(4)\n"
"ds_read_b64_tr_b16 a[188:189], v8 offset:24576\n"
"ds_read_b64_tr_b16 a[190:191], v8 offset:25088\n"
"v_mfma_f32_32x32x16_bf16 v[224:239], a[172:175], a[16:19], v[224:239]\n"
"ds_read_b64_tr_b16 a[160:161], v20 offset:24576\n"
"ds_read_b64_tr_b16 a[162:163], v20 offset:25088\n"
"buffer_load_dwordx4 v43, s[28:31], s66 offen lds\n"
"s_mul_i32 s66, s73, 1536\n"
"s_add_u32 s66, s66, 128\n"
"s_add_u32 m0, s57, 81920\n"
"v_mfma_f32_32x32x16_bf16 v[240:255], a[176:179], a[16:19], v[240:255]\n"
"s_waitcnt lgkmcnt(4)\n"
"ds_read_b64_tr_b16 a[164:165], v21 offset:24576\n"
"ds_read_b64_tr_b16 a[166:167], v21 offset:25088\n"
"v_mul_f32 v189, v106, v189\n"
"v_fmac_f32 v189, v49, v181\n"
"v_mul_f32 v181, v48, v182\n"
"v_fmac_f32 v181, v49, v190\n"
"v_mfma_f32_32x32x16_bf16 v[192:207], a[180:183], a[20:23], v[192:207]\n"
"ds_read_b64_tr_b16 a[168:169], v18 offset:28672\n"
"ds_read_b64_tr_b16 a[170:171], v18 offset:29184\n"
"buffer_load_dwordx4 v34, s[20:23], s66 offen offset:0 lds\n"
"v_mul_f32 v190, v106, v190\n"
"v_fmac_f32 v190, v49, v182\n"
"v_mfma_f32_32x32x16_bf16 v[208:223], a[184:187], a[20:23], v[208:223]\n"
"s_waitcnt lgkmcnt(4)\n"
"ds_read_b64_tr_b16 a[172:173], v19 offset:28672\n"
"ds_read_b64_tr_b16 a[174:175], v19 offset:29184\n"
"v_mul_f32 v182, v48, v183\n"
"v_fmac_f32 v182, v49, v191\n"
"v_mul_f32 v191, v106, v191\n"
"v_fmac_f32 v191, v49, v183\n"
"v_mfma_f32_16x16x32_bf16 a[248:251], a[244:247], a[188:191], a[248:251]\n"
"ds_read_b64_tr_b16 a[176:177], v8 offset:28672\n"
"ds_read_b64_tr_b16 a[178:179], v8 offset:29184\n"
"buffer_load_dwordx4 v35, s[20:23], s66 offen offset:1024 lds\n"
"v_mfma_f32_32x32x16_bf16 v[224:239], a[160:163], a[20:23], v[224:239]\n"
"s_waitcnt lgkmcnt(4)\n"
"ds_read_b64_tr_b16 a[180:181], v20 offset:28672\n"
"ds_read_b64_tr_b16 a[182:183], v20 offset:29184\n"
"v_mul_f32 v82, v112, v112\n"
"v_mul_f32 v83, v113, v113\n"
"v_fma_f32 v82, v51, v82, v50\n"
"v_fma_f32 v83, v51, v83, v50\n"
"v_mfma_f32_32x32x16_bf16 v[240:255], a[164:167], a[20:23], v[240:255]\n"
"ds_read_b64_tr_b16 a[184:185], v21 offset:28672\n"
"ds_read_b64_tr_b16 a[186:187], v21 offset:29184\n"
"buffer_load_dwordx4 v36, s[24:27], s66 offen offset:2048 lds\n"
"v_mul_f32 v82, v112, v82\n"
"v_mul_f32 v83, v113, v83\n"
"v_mfma_f32_32x32x16_bf16 v[192:207], a[168:171], a[24:27], v[192:207]\n"
"s_waitcnt lgkmcnt(4)\n"
"ds_read_b64_tr_b16 a[188:189], v18 offset:32768\n"
"ds_read_b64_tr_b16 a[190:191], v18 offset:33280\n"
"v_exp_f32 v82, v82\n"
"v_mul_f32 v120, v112, v120\n"
"v_mfma_f32_32x32x16_bf16 v[208:223], a[172:175], a[24:27], v[208:223]\n"
"ds_read_b64_tr_b16 a[160:161], v19 offset:32768\n"
"ds_read_b64_tr_b16 a[162:163], v19 offset:33280\n"
"buffer_load_dwordx4 v37, s[24:27], s66 offen offset:3072 lds\n"
"v_exp_f32 v83, v83\n"
"v_mfma_f32_16x16x32_bf16 a[248:251], a[244:247], a[176:179], a[248:251]\n"
"s_waitcnt lgkmcnt(4)\n"
"ds_read_b64_tr_b16 a[164:165], v8 offset:32768\n"
"ds_read_b64_tr_b16 a[166:167], v8 offset:33280\n"
"v_mfma_f32_32x32x16_bf16 v[224:239], a[180:183], a[24:27], v[224:239]\n"
"ds_read_b64_tr_b16 a[168:169], v20 offset:32768\n"
"ds_read_b64_tr_b16 a[170:171], v20 offset:33280\n"
"v_mul_f32 v121, v113, v121\n"
"v_fma_f32 v82, v82, v52, v52\n"
"v_fma_f32 v83, v83, v52, v52\n"
"v_mfma_f32_32x32x16_bf16 v[240:255], a[184:187], a[24:27], v[240:255]\n"
"s_waitcnt vmcnt(18)\n"
"s_barrier\n"
"s_waitcnt lgkmcnt(4)\n"
"ds_read_b64_tr_b16 a[172:173], v21 offset:32768\n"
"ds_read_b64_tr_b16 a[174:175], v21 offset:33280\n"
"s_mul_i32 s66, s74, 1536\n"
"s_add_u32 s66, s66, 128\n"
"s_add_u32 m0, s57, 0\n"
"v_mfma_f32_32x32x16_bf16 v[192:207], a[188:191], a[28:31], v[192:207]\n"
"ds_read_b64_tr_b16 a[176:177], v18 offset:40960\n"
"ds_read_b64_tr_b16 a[178:179], v18 offset:41472\n"
"buffer_load_dwordx4 v34, s[20:23], s66 offen offset:0 lds\n"
"v_rcp_f32 v82, v82\n"
"v_mfma_f32_32x32x16_bf16 v[208:223], a[160:163], a[28:31], v[208:223]\n"
"s_waitcnt lgkmcnt(4)\n"
"ds_read_b64_tr_b16 a[180:181], v19 offset:40960\n"
"ds_read_b64_tr_b16 a[182:183], v19 offset:41472\n"
"v_rcp_f32 v83, v83\n"
"v_mul_f32 v120, v120, v82\n"
"v_mul_f32 v121, v121, v83\n"
"v_mfma_f32_16x16x32_bf16 a[248:251], a[244:247], a[164:167], a[248:251]\n"
"ds_read_b64_tr_b16 a[184:185], v8 offset:40960\n"
"ds_read_b64_tr_b16 a[186:187], v8 offset:41472\n"
"buffer_load_dwordx4 v35, s[20:23], s66 offen offset:1024 lds\n"
"v_mfma_f32_32x32x16_bf16 v[224:239], a[168:171], a[28:31], v[224:239]\n"
"s_waitcnt lgkmcnt(4)\n"
"ds_read_b64_tr_b16 a[188:189], v20 offset:40960\n"
"ds_read_b64_tr_b16 a[190:191], v20 offset:41472\n"
"v_cvt_pk_bf16_f32 v112, v120, v121\n"
"v_accvgpr_write_b32 a208, v112\n"
"v_mul_f32 v84, v114, v114\n"
"v_mul_f32 v85, v115, v115\n"
"v_mfma_f32_32x32x16_bf16 v[240:255], a[172:175], a[28:31], v[240:255]\n"
"ds_read_b64_tr_b16 a[160:161], v21 offset:40960\n"
"ds_read_b64_tr_b16 a[162:163], v21 offset:41472\n"
"buffer_load_dwordx4 v[90:93], v58, s[40:43], s61 offen sc1\n"
"v_fma_f32 v84, v51, v84, v50\n"
"v_fma_f32 v85, v51, v85, v50\n"
"v_mfma_f32_32x32x16_bf16 v[192:207], a[176:179], a[32:35], v[192:207]\n"
"s_waitcnt lgkmcnt(4)\n"
"ds_read_b64_tr_b16 a[164:165], v18 offset:45056\n"
"ds_read_b64_tr_b16 a[166:167], v18 offset:45568\n"
"buffer_load_dwordx4 v[94:97], v59, s[40:43], s61 offen sc1\n"
"v_mul_f32 v84, v114, v84\n"
"v_mul_f32 v85, v115, v85\n"
"v_mfma_f32_32x32x16_bf16 v[208:223], a[180:183], a[32:35], v[208:223]\n"
"ds_read_b64_tr_b16 a[168:169], v19 offset:45056\n"
"ds_read_b64_tr_b16 a[170:171], v19 offset:45568\n"
"v_exp_f32 v84, v84\n"
"v_mul_f32 v122, v114, v122\n"
"v_mfma_f32_16x16x32_bf16 a[248:251], a[252:255], a[184:187], a[248:251]\n"
"s_waitcnt lgkmcnt(4)\n"
"ds_read_b64_tr_b16 a[172:173], v8 offset:45056\n"
"ds_read_b64_tr_b16 a[174:175], v8 offset:45568\n"
"buffer_load_dwordx4 v[98:101], v60, s[40:43], s61 offen sc1\n"
"v_mfma_f32_32x32x16_bf16 v[224:239], a[188:191], a[32:35], v[224:239]\n"
"ds_read_b64_tr_b16 a[176:177], v20 offset:45056\n"
"ds_read_b64_tr_b16 a[178:179], v20 offset:45568\n"
"v_exp_f32 v85, v85\n"
"v_mul_f32 v123, v115, v123\n"
"v_fma_f32 v84, v84, v52, v52\n"
"v_mfma_f32_32x32x16_bf16 v[240:255], a[160:163], a[32:35], v[240:255]\n"
"s_waitcnt lgkmcnt(4)\n"
"ds_read_b64_tr_b16 a[180:181], v21 offset:45056\n"
"ds_read_b64_tr_b16 a[182:183], v21 offset:45568\n"
"buffer_load_dwordx4 v[102:105], v61, s[40:43], s61 offen sc1\n"
"v_fma_f32 v85, v85, v52, v52\n"
"v_mfma_f32_32x32x16_bf16 v[192:207], a[164:167], a[36:39], v[192:207]\n"
"ds_read_b64_tr_b16 a[184:185], v18 offset:49152\n"
"ds_read_b64_tr_b16 a[186:187], v18 offset:49664\n"
"v_rcp_f32 v84, v84\n"
"v_rcp_f32 v85, v85\n"
"v_mfma_f32_32x32x16_bf16 v[208:223], a[168:171], a[36:39], v[208:223]\n"
"s_waitcnt lgkmcnt(4)\n"
"ds_read_b64_tr_b16 a[188:189], v19 offset:49152\n"
"ds_read_b64_tr_b16 a[190:191], v19 offset:49664\n"
"buffer_load_dwordx4 v36, s[24:27], s66 offen offset:2048 lds\n"
"v_mul_f32 v122, v122, v84\n"
"v_mul_f32 v123, v123, v85\n"
"v_mfma_f32_16x16x32_bf16 a[248:251], a[252:255], a[172:175], a[248:251]\n"
"ds_read_b64_tr_b16 a[160:161], v8 offset:49152\n"
"ds_read_b64_tr_b16 a[162:163], v8 offset:49664\n"
"v_mfma_f32_32x32x16_bf16 v[224:239], a[176:179], a[36:39], v[224:239]\n"
"s_waitcnt lgkmcnt(4)\n"
"ds_read_b64_tr_b16 a[164:165], v20 offset:49152\n"
"ds_read_b64_tr_b16 a[166:167], v20 offset:49664\n"
"buffer_load_dwordx4 v37, s[24:27], s66 offen offset:3072 lds\n"
"v_cvt_pk_bf16_f32 v113, v122, v123\n"
"v_accvgpr_write_b32 a209, v113\n"
"v_mfma_f32_32x32x16_bf16 v[240:255], a[180:183], a[36:39], v[240:255]\n"
"ds_read_b64_tr_b16 a[168:169], v21 offset:49152\n"
"ds_read_b64_tr_b16 a[170:171], v21 offset:49664\n"
"v_mul_f32 v82, v116, v116\n"
"v_mul_f32 v83, v117, v117\n"
"v_fma_f32 v82, v51, v82, v50\n"
"v_fma_f32 v83, v51, v83, v50\n"
"v_mfma_f32_32x32x16_bf16 v[192:207], a[184:187], a[40:43], v[192:207]\n"
"s_waitcnt lgkmcnt(4)\n"
"ds_read_b64_tr_b16 a[172:173], v18 offset:53248\n"
"ds_read_b64_tr_b16 a[174:175], v18 offset:53760\n"
"v_mul_f32 v82, v116, v82\n"
"v_mul_f32 v83, v117, v83\n"
"v_exp_f32 v82, v82\n"
"v_mfma_f32_32x32x16_bf16 v[208:223], a[188:191], a[40:43], v[208:223]\n"
"ds_read_b64_tr_b16 a[176:177], v19 offset:53248\n"
"ds_read_b64_tr_b16 a[178:179], v19 offset:53760\n"
"v_mul_f32 v124, v116, v124\n"
"v_exp_f32 v83, v83\n"
"v_mul_f32 v125, v117, v125\n"
"v_mfma_f32_16x16x32_bf16 a[248:251], a[252:255], a[160:163], a[248:251]\n"
"s_waitcnt lgkmcnt(4)\n"
"ds_read_b64_tr_b16 a[180:181], v8 offset:53248\n"
"ds_read_b64_tr_b16 a[182:183], v8 offset:53760\n"
"v_mfma_f32_32x32x16_bf16 v[224:239], a[164:167], a[40:43], v[224:239]\n"
"ds_read_b64_tr_b16 a[184:185], v20 offset:53248\n"
"ds_read_b64_tr_b16 a[186:187], v20 offset:53760\n"
"v_fma_f32 v82, v82, v52, v52\n"
"v_fma_f32 v83, v83, v52, v52\n"
"v_rcp_f32 v82, v82\n"
"v_mfma_f32_32x32x16_bf16 v[240:255], a[168:171], a[40:43], v[240:255]\n"
"s_waitcnt vmcnt(21)\n"
"s_barrier\n"
"s_waitcnt lgkmcnt(4)\n"
"ds_read_b64_tr_b16 a[188:189], v21 offset:53248\n"
"ds_read_b64_tr_b16 a[190:191], v21 offset:53760\n"
"s_mul_i32 s66, s75, 1536\n"
"s_add_u32 s66, s66, 128\n"
"s_add_u32 m0, s57, 20480\n"
"v_mfma_f32_32x32x16_bf16 v[192:207], a[172:175], a[44:47], v[192:207]\n"
"ds_read_b64_tr_b16 a[160:161], v22 offset:0\n"
"ds_read_b64_tr_b16 a[162:163], v22 offset:512\n"
"buffer_load_dwordx4 v34, s[20:23], s66 offen offset:0 lds\n"
"v_rcp_f32 v83, v83\n"
"v_mfma_f32_32x32x16_bf16 v[208:223], a[176:179], a[44:47], v[208:223]\n"
"s_waitcnt lgkmcnt(4)\n"
"ds_read_b64_tr_b16 a[164:165], v23 offset:0\n"
"ds_read_b64_tr_b16 a[166:167], v23 offset:512\n"
"v_mul_f32 v124, v124, v82\n"
"v_mul_f32 v125, v125, v83\n"
"v_cvt_pk_bf16_f32 v114, v124, v125\n"
"v_accvgpr_write_b32 a210, v114\n"
"v_mfma_f32_16x16x32_bf16 a[248:251], a[252:255], a[180:183], a[248:251]\n"
"ds_read_b64_tr_b16 a[168:169], v24 offset:0\n"
"ds_read_b64_tr_b16 a[170:171], v24 offset:512\n"
"buffer_load_dwordx4 v35, s[20:23], s66 offen offset:1024 lds\n"
"v_mfma_f32_32x32x16_bf16 v[224:239], a[184:187], a[44:47], v[224:239]\n"
"s_waitcnt lgkmcnt(4)\n"
"ds_read_b64_tr_b16 a[172:173], v25 offset:0\n"
"ds_read_b64_tr_b16 a[174:175], v25 offset:512\n"
"v_mul_f32 v84, v118, v118\n"
"v_mul_f32 v85, v119, v119\n"
"v_fma_f32 v84, v51, v84, v50\n"
"v_fma_f32 v85, v51, v85, v50\n"
"v_mfma_f32_32x32x16_bf16 v[240:255], a[188:191], a[44:47], v[240:255]\n"
"ds_read_b64_tr_b16 a[176:177], v22 offset:4096\n"
"ds_read_b64_tr_b16 a[178:179], v22 offset:4608\n"
"buffer_load_dwordx4 v36, s[24:27], s66 offen offset:2048 lds\n"
"v_accvgpr_read_b32 v76, a248\n"
"v_cmp_gt_u32 s[0:1], 32, v2\n"
"v_mfma_f32_32x32x16_bf16 v[192:207], a[160:163], a[48:51], v[192:207]\n"
"s_waitcnt lgkmcnt(4)\n"
"ds_read_b64_tr_b16 a[180:181], v23 offset:4096\n"
"ds_read_b64_tr_b16 a[182:183], v23 offset:4608\n"
"v_and_b32 v78, 31, v2\n"
"v_lshlrev_b32 v78, 4, v78\n"
"s_lshl_b32 s2, s52, 10\n"
"v_add_u32 v79, s2, v78\n"
"v_mfma_f32_32x32x16_bf16 v[208:223], a[164:167], a[48:51], v[208:223]\n"
"ds_read_b64_tr_b16 a[184:185], v24 offset:4096\n"
"ds_read_b64_tr_b16 a[186:187], v24 offset:4608\n"
"buffer_load_dwordx4 v37, s[24:27], s66 offen offset:3072 lds\n"
"v_mov_b32 v78, 0x70000000\n"
"v_cvt_pk_bf16_f32 v77, v76, v76\n"
"v_mfma_f32_32x32x16_bf16 v[224:239], a[168:171], a[48:51], v[224:239]\n"
"s_waitcnt lgkmcnt(4)\n"
"ds_read_b64_tr_b16 a[188:189], v25 offset:4096\n"
"ds_read_b64_tr_b16 a[190:191], v25 offset:4608\n"
"v_cndmask_b32 v78, v78, v79, s[0:1]\n"
"v_and_b32 v77, s100, v77\n"
"s_lshl_b32 s3, s54, 2\n"
"s_add_u32 s3, s3, s55\n"
"v_mfma_f32_32x32x16_bf16 v[240:255], a[172:175], a[48:51], v[240:255]\n"
"ds_read_b64_tr_b16 a[160:161], v22 offset:8192\n"
"ds_read_b64_tr_b16 a[162:163], v22 offset:8704\n"
"s_mul_i32 s3, s3, 10240\n"
"s_add_u32 s3, s3, 0x1681400\n"
"v_sub_f32 v76, v76, v77\n"
"v_cvt_pk_bf16_f32 v76, v77, v76\n"
"v_mfma_f32_32x32x16_bf16 v[192:207], a[176:179], a[52:55], v[192:207]\n"
"s_waitcnt lgkmcnt(4)\n"
"ds_read_b64_tr_b16 a[164:165], v23 offset:8192\n"
"ds_read_b64_tr_b16 a[166:167], v23 offset:8704\n"
"v_mov_b32 v77, s56\n"
"s_lshr_b32 s2, s101, 1\n"
"s_lshl_b32 s2, s2, 9\n"
"s_and_b32 s7, s101, 1\n"
"v_mfma_f32_32x32x16_bf16 v[208:223], a[180:183], a[52:55], v[208:223]\n"
"ds_read_b64_tr_b16 a[168:169], v24 offset:8192\n"
"ds_read_b64_tr_b16 a[170:171], v24 offset:8704\n"
"s_lshl_b32 s7, s7, 3\n"
"s_add_u32 s2, s2, s7\n"
"s_add_u32 s2, s2, s3\n"
"s_cmp_lt_u32 s101, 4\n"
"s_cselect_b32 s2, s2, 0x60000000\n"
"v_mfma_f32_32x32x16_bf16 v[224:239], a[184:187], a[52:55], v[224:239]\n"
"s_waitcnt lgkmcnt(4)\n"
"ds_read_b64_tr_b16 a[172:173], v25 offset:8192\n"
"ds_read_b64_tr_b16 a[174:175], v25 offset:8704\n"
"buffer_store_dwordx2 v[76:77], v78, s[40:43], s2 offen sc1\n"
"v_mul_f32 v84, v118, v84\n"
"v_mfma_f32_32x32x16_bf16 v[240:255], a[188:191], a[52:55], v[240:255]\n"
"ds_read_b64_tr_b16 a[176:177], v22 offset:12288\n"
"ds_read_b64_tr_b16 a[178:179], v22 offset:12800\n"
"v_mul_f32 v85, v119, v85\n"
"v_exp_f32 v84, v84\n"
"v_mul_f32 v126, v118, v126\n"
"v_mfma_f32_32x32x16_bf16 v[192:207], a[160:163], a[56:59], v[192:207]\n"
"s_waitcnt lgkmcnt(4)\n"
"ds_read_b64_tr_b16 a[180:181], v23 offset:12288\n"
"ds_read_b64_tr_b16 a[182:183], v23 offset:12800\n"
"v_exp_f32 v85, v85\n"
"v_mul_f32 v127, v119, v127\n"
"v_fma_f32 v84, v84, v52, v52\n"
"v_mfma_f32_32x32x16_bf16 v[208:223], a[164:167], a[56:59], v[208:223]\n"
"ds_read_b64_tr_b16 a[184:185], v24 offset:12288\n"
"ds_read_b64_tr_b16 a[186:187], v24 offset:12800\n"
"s_waitcnt vmcnt(10)\n"
"v_xor_b32 v91, v62, v91\n"
"v_xor_b32 v93, v62, v93\n"
"s_waitcnt vmcnt(9)\n"
"v_xor_b32 v95, v63, v95\n"
"v_xor_b32 v97, v63, v97\n"
"s_waitcnt vmcnt(8)\n"
"v_xor_b32 v99, v64, v99\n"
"v_xor_b32 v101, v64, v101\n"
"s_waitcnt vmcnt(7)\n"
"v_xor_b32 v103, v65, v103\n"
"v_xor_b32 v105, v65, v105\n"
"v_or3_b32 v91, v91, v93, v95\n"
"v_or3_b32 v97, v97, v99, v101\n"
"v_or3_b32 v91, v91, v103, v105\n"
"v_or_b32 v91, v91, v97\n"
"v_mfma_f32_32x32x16_bf16 v[224:239], a[168:171], a[56:59], v[224:239]\n"
"s_barrier\n"
"s_waitcnt lgkmcnt(4)\n"
"ds_read_b64_tr_b16 a[188:189], v25 offset:12288\n"
"ds_read_b64_tr_b16 a[190:191], v25 offset:12800\n"
"s_mul_i32 s66, s76, 1536\n"
"s_add_u32 s66, s66, 128\n"
"v_cmp_ne_u32 vcc, 0, v91\n"
"s_nop 0\n"
"s_cbranch_vccnz LaggA_slow%=\n"
"LaggA_ok%=:\n"
"v_mfma_f32_32x32x16_bf16 v[240:255], a[172:175], a[56:59], v[240:255]\n"
"ds_read_b64_tr_b16 a[160:161], v22 offset:40960\n"
"ds_read_b64_tr_b16 a[162:163], v22 offset:41472\n"
"ds_read_b64 v[60:61], v75\n"
"ds_read_b64 v[62:63], v75 offset:8\n"
"ds_read_b64 v[64:65], v75 offset:16\n"
"s_add_u32 m0, s57, 40960\n"
"s_nop 0\n"
"buffer_load_dwordx4 v34, s[20:23], s66 offen offset:0 lds\n"
"v_mov_b32 v91, v92\n"
"v_mov_b32 v95, v96\n"
"v_mov_b32 v99, v100\n"
"v_mov_b32 v103, v104\n"
"v_fmac_f32 v98, v102, v68\n"
"v_fmac_f32 v99, v102, v69\n"
"v_mfma_f32_32x32x16_bf16 v[192:207], a[176:179], a[60:63], v[192:207]\n"
"s_waitcnt lgkmcnt(7)\n"
"ds_read_b64_tr_b16 a[164:165], v23 offset:40960\n"
"ds_read_b64_tr_b16 a[166:167], v23 offset:41472\n"
"buffer_load_dwordx4 v35, s[20:23], s66 offen offset:1024 lds\n"
"v_fma_f32 v98, -v103, v69, v98\n"
"v_fmac_f32 v99, v103, v68\n"
"v_fmac_f32 v94, v98, v68\n"
"v_fmac_f32 v95, v98, v69\n"
"v_fma_f32 v94, -v99, v69, v94\n"
"v_fmac_f32 v95, v99, v68\n"
"v_fmac_f32 v90, v94, v68\n"
"v_fmac_f32 v91, v94, v69\n"
"v_fma_f32 v90, -v95, v69, v90\n"
"v_fmac_f32 v91, v95, v68\n"
"v_mfma_f32_32x32x16_bf16 v[208:223], a[180:183], a[60:63], v[208:223]\n"
"ds_read_b64_tr_b16 a[168:169], v24 offset:40960\n"
"ds_read_b64_tr_b16 a[170:171], v24 offset:41472\n"
"buffer_load_dwordx4 v36, s[24:27], s66 offen offset:2048 lds\n"
"v_mov_b32 v92, v90\n"
"v_mov_b32 v93, v91\n"
"s_nop 0\n"
"v_permlane32_swap_b32 v90, v92\n"
"v_permlane32_swap_b32 v91, v93\n"
"v_fmac_f32 v90, v70, v92\n"
"v_fmac_f32 v91, v70, v93\n"
"v_fma_f32 v90, -v71, v93, v90\n"
"v_fmac_f32 v91, v71, v92\n"
"v_mov_b32 v92, v90\n"
"v_mov_b32 v93, v91\n"
"v_mfma_f32_32x32x16_bf16 v[224:239], a[184:187], a[60:63], v[224:239]\n"
"s_waitcnt lgkmcnt(7)\n"
"ds_read_b64_tr_b16 a[172:173], v25 offset:40960\n"
"ds_read_b64_tr_b16 a[174:175], v25 offset:41472\n"
"buffer_load_dwordx4 v37, s[24:27], s66 offen offset:3072 lds\n"
"v_permlane16_swap_b32 v90, v92\n"
"v_permlane16_swap_b32 v91, v93\n"
"v_fmac_f32 v90, v72, v92\n"
"v_fmac_f32 v91, v72, v93\n"
"v_fma_f32 v90, -v73, v93, v90\n"
"v_fmac_f32 v91, v73, v92\n"
"v_mov_b32 v58, v86\n"
"v_mov_b32 v59, v87\n"
"v_fmac_f32 v58, v110, v90\n"
"v_fmac_f32 v59, v110, v91\n"
"v_mfma_f32_32x32x16_bf16 v[240:255], a[188:191], a[60:63], v[240:255]\n"
"ds_read_b64_tr_b16 a[176:177], v22 offset:45056\n"
"ds_read_b64_tr_b16 a[178:179], v22 offset:45568\n"
"v_fma_f32 v58, -v111, v91, v58\n"
"v_fmac_f32 v59, v111, v90\n"
"s_waitcnt lgkmcnt(10)\n"
"v_fmac_f32 v60, v58, v66\n"
"v_fmac_f32 v61, v58, v67\n"
"v_fma_f32 v60, -v59, v67, v60\n"
"v_fmac_f32 v61, v59, v66\n"
"s_waitcnt lgkmcnt(9)\n"
"v_fmac_f32 v62, v60, v66\n"
"v_fmac_f32 v63, v60, v67\n"
"v_fma_f32 v62, -v61, v67, v62\n"
"v_fmac_f32 v63, v61, v66\n"
"s_waitcnt lgkmcnt(8)\n"
"v_fmac_f32 v64, v62, v66\n"
"v_fmac_f32 v65, v62, v67\n"
"v_mfma_f32_32x32x16_bf16 v[192:207], a[160:163], a[64:67], v[192:207]\n"
"s_waitcnt lgkmcnt(4)\n"
"ds_read_b64_tr_b16 a[180:181], v23 offset:45056\n"
"ds_read_b64_tr_b16 a[182:183], v23 offset:45568\n"
"v_fma_f32 v64, -v63, v67, v64\n"
"v_fmac_f32 v65, v63, v66\n"
"v_lshrrev_b32 v89, 5, v2\n"
"v_mul_u32_u24 v88, 1280, v88\n"
"v_mfma_f32_32x32x16_bf16 v[208:223], a[164:167], a[64:67], v[208:223]\n"
"ds_read_b64_tr_b16 a[184:185], v24 offset:45056\n"
"ds_read_b64_tr_b16 a[186:187], v24 offset:45568\n"
"ds_write_b128 v75, v[58:61] offset:2304\n"
"ds_write_b128 v75, v[62:65] offset:2320\n"
"s_lshl_b32 s60, s63, 11\n"
"s_add_u32 s78, s60, s65\n"
"s_add_u32 s79, s78, 4096\n"
"ds_read_b64 v[90:91], v74 offset:2304\n"
"v_mfma_f32_32x32x16_bf16 v[224:239], a[168:171], a[64:67], v[224:239]\n"
"s_waitcnt lgkmcnt(7)\n"
"ds_read_b64_tr_b16 a[188:189], v25 offset:45056\n"
"ds_read_b64_tr_b16 a[190:191], v25 offset:45568\n"
"v_lshl_add_u32 v88, v89, 4, v88\n"
"s_lshl_b32 s12, s53, 2\n"
"s_add_u32 s12, s12, s52\n"
"s_mul_i32 s12, s12, 40960\n"
"v_mfma_f32_32x32x16_bf16 v[240:255], a[172:175], a[64:67], v[240:255]\n"
"ds_read_b64_tr_b16 a[160:161], v22 offset:49152\n"
"ds_read_b64_tr_b16 a[162:163], v22 offset:49664\n"
"s_mul_i32 s13, s55, 320\n"
"s_add_u32 s12, s12, s13\n"
"s_add_u32 s13, s12, 256\n"
"s_lshl_b32 s14, s54, 6\n"
"v_mfma_f32_32x32x16_bf16 v[192:207], a[176:179], a[68:71], v[192:207]\n"
"s_waitcnt lgkmcnt(7)\n"
"ds_read_b64_tr_b16 a[164:165], v23 offset:49152\n"
"ds_read_b64_tr_b16 a[166:167], v23 offset:49664\n"
"s_add_u32 s12, s12, s14\n"
"s_cmp_eq_u32 s52, s54\n"
"s_cselect_b32 s13, s13, 0x70000000\n"
"s_waitcnt lgkmcnt(6)\n"
"v_mul_f32 v98, v48, v90\n"
"v_mul_f32 v99, v49, v90\n"
"v_sub_f32 v100, 0, v47\n"
"v_fmac_f32 v98, v49, v91\n"
"v_fma_f32 v99, -v48, v91, v99\n"
"v_fmac_f32 v53, v46, v98\n"
"v_fmac_f32 v184, v46, v99\n"
"v_fmac_f32 v53, v47, v99\n"
"v_fmac_f32 v184, v100, v98\n"
"v_mfma_f32_32x32x16_bf16 v[208:223], a[180:183], a[68:71], v[208:223]\n"
"ds_read_b64_tr_b16 a[168:169], v24 offset:49152\n"
"ds_read_b64_tr_b16 a[170:171], v24 offset:49664\n"
"buffer_load_dwordx4 a[240:243], v88, s[8:11], s12 offen offset:0\n"
"v_fmac_f32 v176, v46, v53\n"
"v_fmac_f32 v185, v46, v184\n"
"v_fmac_f32 v176, v47, v184\n"
"v_fmac_f32 v185, v100, v53\n"
"v_fmac_f32 v177, v46, v176\n"
"v_fmac_f32 v186, v46, v185\n"
"v_fmac_f32 v177, v47, v185\n"
"v_fmac_f32 v186, v100, v176\n"
"v_fmac_f32 v178, v46, v177\n"
"v_fmac_f32 v187, v46, v186\n"
"v_mfma_f32_32x32x16_bf16 v[224:239], a[184:187], a[68:71], v[224:239]\n"
"s_waitcnt lgkmcnt(4)\n"
"ds_read_b64_tr_b16 a[172:173], v25 offset:49152\n"
"ds_read_b64_tr_b16 a[174:175], v25 offset:49664\n"
"v_fmac_f32 v178, v47, v186\n"
"v_fmac_f32 v187, v100, v177\n"
"v_fmac_f32 v179, v46, v178\n"
"v_fmac_f32 v188, v46, v187\n"
"v_fmac_f32 v179, v47, v187\n"
"v_fmac_f32 v188, v100, v178\n"
"v_fmac_f32 v180, v46, v179\n"
"v_fmac_f32 v189, v46, v188\n"
"v_fmac_f32 v180, v47, v188\n"
"v_fmac_f32 v189, v100, v179\n"
"v_fmac_f32 v181, v46, v180\n"
"v_fmac_f32 v190, v46, v189\n"
"v_mfma_f32_32x32x16_bf16 v[240:255], a[188:191], a[68:71], v[240:255]\n"
"ds_read_b64_tr_b16 a[176:177], v22 offset:53248\n"
"ds_read_b64_tr_b16 a[178:179], v22 offset:53760\n"
"v_fmac_f32 v181, v47, v189\n"
"v_fmac_f32 v190, v100, v180\n"
"v_fmac_f32 v182, v46, v181\n"
"v_fmac_f32 v191, v46, v190\n"
"v_fmac_f32 v182, v47, v190\n"
"v_fmac_f32 v191, v100, v181\n"
"v_cvt_pk_bf16_f32 v104, v53, v176\n"
"v_cvt_pk_bf16_f32 v105, v177, v178\n"
"v_cvt_pk_bf16_f32 v106, v179, v180\n"
"v_cvt_pk_bf16_f32 v107, v181, v182\n"
"v_cvt_pk_bf16_f32 v108, v184, v185\n"
"v_cvt_pk_bf16_f32 v109, v186, v187\n"
"v_mfma_f32_32x32x16_bf16 v[192:207], a[160:163], a[72:75], v[192:207]\n"
"s_waitcnt lgkmcnt(4)\n"
"ds_read_b64_tr_b16 a[180:181], v23 offset:53248\n"
"ds_read_b64_tr_b16 a[182:183], v23 offset:53760\n"
"buffer_load_dwordx4 a[244:247], v88, s[8:11], s12 offen offset:32\n"
"v_cvt_pk_bf16_f32 v110, v188, v189\n"
"v_cvt_pk_bf16_f32 v111, v190, v191\n"
"v_mfma_f32_32x32x16_bf16 v[208:223], a[164:167], a[72:75], v[208:223]\n"
"ds_read_b64_tr_b16 a[184:185], v24 offset:53248\n"
"ds_read_b64_tr_b16 a[186:187], v24 offset:53760\n"
"ds_write_b128 v45, v[104:107]\n"
"ds_read_b64_tr_b16 a[192:193], v81\n"
"ds_read_b64_tr_b16 a[194:195], v81 offset:320\n"
"ds_write_b128 v45, v[108:111]\n"
"ds_read_b64_tr_b16 a[196:197], v81\n"
"ds_read_b64_tr_b16 a[198:199], v81 offset:320\n"
"v_mfma_f32_32x32x16_bf16 v[224:239], a[168:171], a[72:75], v[224:239]\n"
"s_barrier\n"
"s_waitcnt lgkmcnt(10)\n"
"ds_read_b64_tr_b16 a[188:189], v25 offset:53248\n"
"ds_read_b64_tr_b16 a[190:191], v25 offset:53760\n"
"s_mul_i32 s66, s77, 1536\n"
"s_add_u32 s66, s66, 128\n"
"s_add_u32 m0, s57, 61440\n"
"v_mfma_f32_32x32x16_bf16 v[240:255], a[172:175], a[72:75], v[240:255]\n"
"ds_read_b64_tr_b16 a[160:161], v22 offset:20480\n"
"ds_read_b64_tr_b16 a[162:163], v22 offset:20992\n"
"v_fma_f32 v85, v85, v52, v52\n"
"v_rcp_f32 v84, v84\n"
"v_mfma_f32_32x32x16_bf16 v[192:207], a[176:179], a[76:79], v[192:207]\n"
"s_waitcnt lgkmcnt(10)\n"
"ds_read_b64_tr_b16 a[164:165], v23 offset:20480\n"
"ds_read_b64_tr_b16 a[166:167], v23 offset:20992\n"
"v_rcp_f32 v85, v85\n"
"v_mul_f32 v126, v126, v84\n"
"v_mul_f32 v127, v127, v85\n"
"v_mfma_f32_32x32x16_bf16 v[208:223], a[180:183], a[76:79], v[208:223]\n"
"ds_read_b64_tr_b16 a[168:169], v24 offset:20480\n"
"ds_read_b64_tr_b16 a[170:171], v24 offset:20992\n"
"v_cvt_pk_bf16_f32 v115, v126, v127\n"
"v_accvgpr_write_b32 a211, v115\n"
"v_mul_f32 v82, v128, v128\n"
"v_mul_f32 v83, v129, v129\n"
"v_mfma_f32_32x32x16_bf16 v[224:239], a[184:187], a[76:79], v[224:239]\n"
"s_waitcnt lgkmcnt(4)\n"
"ds_read_b64_tr_b16 a[172:173], v25 offset:20480\n"
"ds_read_b64_tr_b16 a[174:175], v25 offset:20992\n"
"v_fma_f32 v82, v51, v82, v50\n"
"v_fma_f32 v83, v51, v83, v50\n"
"v_mul_f32 v82, v128, v82\n"
"v_mul_f32 v83, v129, v83\n"
"v_mfma_f32_32x32x16_bf16 v[240:255], a[188:191], a[76:79], v[240:255]\n"
"ds_read_b64_tr_b16 a[176:177], v22 offset:24576\n"
"ds_read_b64_tr_b16 a[178:179], v22 offset:25088\n"
"buffer_store_dwordx4 a[208:211], v44, s[40:43], s67 offen offset:0 sc1\n"
"v_mfma_f32_32x32x16_bf16 v[192:207], a[160:163], a[80:83], v[192:207]\n"
"s_waitcnt lgkmcnt(4)\n"
"ds_read_b64_tr_b16 a[180:181], v23 offset:24576\n"
"ds_read_b64_tr_b16 a[182:183], v23 offset:25088\n"
"s_lshl_b32 s3, s54, 2\n"
"s_add_u32 s3, s3, s55\n"
"s_mul_i32 s3, s3, 10240\n"
"s_add_u32 s3, s3, 0x1681400\n"
"v_mfma_f32_32x32x16_bf16 v[208:223], a[164:167], a[80:83], v[208:223]\n"
"ds_read_b64_tr_b16 a[184:185], v24 offset:24576\n"
"ds_read_b64_tr_b16 a[186:187], v24 offset:25088\n"
"s_add_u32 s2, s3, 4096\n"
"v_exp_f32 v82, v82\n"
"v_mul_f32 v136, v128, v136\n"
"v_mfma_f32_32x32x16_bf16 v[224:239], a[168:171], a[80:83], v[224:239]\n"
"s_waitcnt lgkmcnt(4)\n"
"ds_read_b64_tr_b16 a[188:189], v25 offset:24576\n"
"ds_read_b64_tr_b16 a[190:191], v25 offset:25088\n"
"buffer_store_dwordx4 a[192:195], v44, s[40:43], s78 offen offset:0 sc1\n"
"v_mfma_f32_32x32x16_bf16 v[240:255], a[172:175], a[80:83], v[240:255]\n"
"ds_read_b64_tr_b16 a[160:161], v22 offset:28672\n"
"ds_read_b64_tr_b16 a[162:163], v22 offset:29184\n"
"v_exp_f32 v83, v83\n"
"v_mul_f32 v137, v129, v137\n"
"v_fma_f32 v82, v82, v52, v52\n"
"v_mfma_f32_32x32x16_bf16 v[192:207], a[176:179], a[84:87], v[192:207]\n"
"s_waitcnt lgkmcnt(4)\n"
"ds_read_b64_tr_b16 a[164:165], v23 offset:28672\n"
"ds_read_b64_tr_b16 a[166:167], v23 offset:29184\n"
"v_fma_f32 v83, v83, v52, v52\n"
"v_rcp_f32 v82, v82\n"
"v_mfma_f32_32x32x16_bf16 v[208:223], a[180:183], a[84:87], v[208:223]\n"
"ds_read_b64_tr_b16 a[168:169], v24 offset:28672\n"
"ds_read_b64_tr_b16 a[170:171], v24 offset:29184\n"
"v_rcp_f32 v83, v83\n"
"v_mul_f32 v136, v136, v82\n"
"v_mul_f32 v137, v137, v83\n"
"v_mfma_f32_32x32x16_bf16 v[224:239], a[184:187], a[84:87], v[224:239]\n"
"s_waitcnt lgkmcnt(4)\n"
"ds_read_b64_tr_b16 a[172:173], v25 offset:28672\n"
"ds_read_b64_tr_b16 a[174:175], v25 offset:29184\n"
"buffer_store_dwordx4 a[196:199], v44, s[40:43], s78 offen offset:1024 sc1\n"
"v_cvt_pk_bf16_f32 v116, v136, v137\n"
"v_mfma_f32_32x32x16_bf16 v[240:255], a[188:191], a[84:87], v[240:255]\n"
"ds_read_b64_tr_b16 a[176:177], v22 offset:32768\n"
"ds_read_b64_tr_b16 a[178:179], v22 offset:33280\n"
"v_accvgpr_write_b32 a212, v116\n"
"v_mul_f32 v84, v130, v130\n"
"v_mul_f32 v85, v131, v131\n"
"v_fma_f32 v84, v51, v84, v50\n"
"v_mfma_f32_32x32x16_bf16 v[192:207], a[160:163], a[88:91], v[192:207]\n"
"s_waitcnt lgkmcnt(4)\n"
"ds_read_b64_tr_b16 a[180:181], v23 offset:32768\n"
"ds_read_b64_tr_b16 a[182:183], v23 offset:33280\n"
"v_fma_f32 v85, v51, v85, v50\n"
"v_mul_f32 v84, v130, v84\n"
"v_mul_f32 v85, v131, v85\n"
"v_mfma_f32_32x32x16_bf16 v[208:223], a[164:167], a[88:91], v[208:223]\n"
"ds_read_b64_tr_b16 a[184:185], v24 offset:32768\n"
"ds_read_b64_tr_b16 a[186:187], v24 offset:33280\n"
"v_exp_f32 v84, v84\n"
"v_mul_f32 v138, v130, v138\n"
"v_mfma_f32_32x32x16_bf16 v[224:239], a[168:171], a[88:91], v[224:239]\n"
"s_waitcnt vmcnt(14)\n"
"s_barrier\n"
"s_waitcnt lgkmcnt(4)\n"
"ds_read_b64_tr_b16 a[188:189], v25 offset:32768\n"
"ds_read_b64_tr_b16 a[190:191], v25 offset:33280\n"
"buffer_load_dwordx4 a[224:227], v44, s[40:43], s3 offen offset:0 sc1\n"
"v_mfma_f32_32x32x16_bf16 v[240:255], a[172:175], a[88:91], v[240:255]\n"
"ds_read_b64_tr_b16 a[160:161], v18 offset:0\n"
"ds_read_b64_tr_b16 a[162:163], v18 offset:512\n"
"buffer_load_dwordx4 a[228:231], v44, s[40:43], s3 offen offset:1024 sc1\n"
"v_exp_f32 v85, v85\n"
"v_mfma_f32_32x32x16_bf16 v[192:207], a[176:179], a[92:95], v[192:207]\n"
"s_waitcnt lgkmcnt(4)\n"
"ds_read_b64_tr_b16 a[164:165], v19 offset:0\n"
"ds_read_b64_tr_b16 a[166:167], v19 offset:512\n"
"v_mul_f32 v139, v131, v139\n"
"v_fma_f32 v84, v84, v52, v52\n"
"v_fma_f32 v85, v85, v52, v52\n"
"v_mfma_f32_32x32x16_bf16 v[208:223], a[180:183], a[92:95], v[208:223]\n"
"ds_read_b64_tr_b16 a[168:169], v20 offset:0\n"
"ds_read_b64_tr_b16 a[170:171], v20 offset:512\n"
"buffer_load_dwordx4 a[232:235], v44, s[40:43], s3 offen offset:2048 sc1\n"
"v_rcp_f32 v84, v84\n"
"v_mfma_f32_32x32x16_bf16 v[224:239], a[184:187], a[92:95], v[224:239]\n"
"s_waitcnt lgkmcnt(4)\n"
"ds_read_b64_tr_b16 a[172:173], v21 offset:0\n"
"ds_read_b64_tr_b16 a[174:175], v21 offset:512\n"
"buffer_load_dwordx4 v34, s[20:23], s66 offen offset:0 lds\n"
"v_rcp_f32 v85, v85\n"
"v_mfma_f32_32x32x16_bf16 v[240:255], a[188:191], a[92:95], v[240:255]\n"
"ds_read_b64_tr_b16 a[176:177], v18 offset:4096\n"
"ds_read_b64_tr_b16 a[178:179], v18 offset:4608\n"
"v_mul_f32 v138, v138, v84\n"
"v_mul_f32 v139, v139, v85\n"
"v_cvt_pk_bf16_f32 v117, v138, v139\n"
"v_accvgpr_write_b32 a213, v117\n"
"v_mfma_f32_32x32x16_bf16 v[192:207], a[160:163], a[96:99], v[192:207]\n"
"s_waitcnt lgkmcnt(4)\n"
"ds_read_b64_tr_b16 a[180:181], v19 offset:4096\n"
"ds_read_b64_tr_b16 a[182:183], v19 offset:4608\n"
"buffer_load_dwordx4 v35, s[20:23], s66 offen offset:1024 lds\n"
"v_mul_f32 v82, v132, v132\n"
"v_mul_f32 v83, v133, v133\n"
"v_mfma_f32_32x32x16_bf16 v[208:223], a[164:167], a[96:99], v[208:223]\n"
"ds_read_b64_tr_b16 a[184:185], v20 offset:4096\n"
"ds_read_b64_tr_b16 a[186:187], v20 offset:4608\n"
"v_fma_f32 v82, v51, v82, v50\n"
"v_fma_f32 v83, v51, v83, v50\n"
"v_mul_f32 v82, v132, v82\n"
"v_mul_f32 v83, v133, v83\n"
"v_mfma_f32_32x32x16_bf16 v[224:239], a[168:171], a[96:99], v[224:239]\n"
"s_waitcnt lgkmcnt(4)\n"
"ds_read_b64_tr_b16 a[188:189], v21 offset:4096\n"
"ds_read_b64_tr_b16 a[190:191], v21 offset:4608\n"
"buffer_load_dwordx4 v36, s[24:27], s66 offen offset:2048 lds\n"
"v_exp_f32 v82, v82\n"
"v_mfma_f32_32x32x16_bf16 v[240:255], a[172:175], a[96:99], v[240:255]\n"
"ds_read_b64_tr_b16 a[160:161], v18 offset:8192\n"
"ds_read_b64_tr_b16 a[162:163], v18 offset:8704\n"
"v_mul_f32 v140, v132, v140\n"
"v_exp_f32 v83, v83\n"
"v_mul_f32 v141, v133, v141\n"
"v_mfma_f32_32x32x16_bf16 v[192:207], a[176:179], a[100:103], v[192:207]\n"
"s_waitcnt lgkmcnt(4)\n"
"ds_read_b64_tr_b16 a[164:165], v19 offset:8192\n"
"ds_read_b64_tr_b16 a[166:167], v19 offset:8704\n"
"buffer_load_dwordx4 v37, s[24:27], s66 offen offset:3072 lds\n"
"s_add_u32 s66, s64, 1\n"
"s_mul_i32 s66, s66, 81920\n"
"s_add_u32 m0, s59, 102400\n"
"v_mfma_f32_32x32x16_bf16 v[208:223], a[180:183], a[100:103], v[208:223]\n"
"ds_read_b64_tr_b16 a[168:169], v20 offset:8192\n"
"ds_read_b64_tr_b16 a[170:171], v20 offset:8704\n"
"v_fma_f32 v82, v82, v52, v52\n"
"v_fma_f32 v83, v83, v52, v52\n"
"v_rcp_f32 v82, v82\n"
"v_mfma_f32_32x32x16_bf16 v[224:239], a[184:187], a[100:103], v[224:239]\n"
"s_waitcnt lgkmcnt(4)\n"
"ds_read_b64_tr_b16 a[172:173], v21 offset:8192\n"
"ds_read_b64_tr_b16 a[174:175], v21 offset:8704\n"
"v_rcp_f32 v83, v83\n"
"v_mul_f32 v140, v140, v82\n"
"v_mul_f32 v141, v141, v83\n"
"v_mfma_f32_32x32x16_bf16 v[240:255], a[188:191], a[100:103], v[240:255]\n"
"ds_read_b64_tr_b16 a[176:177], v18 offset:12288\n"
"ds_read_b64_tr_b16 a[178:179], v18 offset:12800\n"
"buffer_load_dwordx4 a[236:239], v44, s[40:43], s3 offen offset:3072 sc1\n"
"v_cvt_pk_bf16_f32 v118, v140, v141\n"
"v_accvgpr_write_b32 a214, v118\n"
"v_mfma_f32_32x32x16_bf16 v[192:207], a[160:163], a[104:107], v[192:207]\n"
"s_waitcnt lgkmcnt(4)\n"
"ds_read_b64_tr_b16 a[180:181], v19 offset:12288\n"
"ds_read_b64_tr_b16 a[182:183], v19 offset:12800\n"
"buffer_load_dwordx4 a[248:251], v88, s[8:11], s13 offen offset:0\n"
"v_mul_f32 v84, v134, v134\n"
"v_mul_f32 v85, v135, v135\n"
"v_mfma_f32_32x32x16_bf16 v[208:223], a[164:167], a[104:107], v[208:223]\n"
"ds_read_b64_tr_b16 a[184:185], v20 offset:12288\n"
"ds_read_b64_tr_b16 a[186:187], v20 offset:12800\n"
"v_fma_f32 v84, v51, v84, v50\n"
"v_fma_f32 v85, v51, v85, v50\n"
"v_mul_f32 v84, v134, v84\n"
"v_mul_f32 v85, v135, v85\n"
"v_mfma_f32_32x32x16_bf16 v[224:239], a[168:171], a[104:107], v[224:239]\n"
"s_waitcnt vmcnt(19)\n"
"s_barrier\n"
"s_waitcnt lgkmcnt(4)\n"
"ds_read_b64_tr_b16 a[188:189], v21 offset:12288\n"
"ds_read_b64_tr_b16 a[190:191], v21 offset:12800\n"
"v_exp_f32 v84, v84\n"
"v_mfma_f32_32x32x16_bf16 v[240:255], a[172:175], a[104:107], v[240:255]\n"
"ds_read_b64_tr_b16 a[160:161], v18 offset:20480\n"
"ds_read_b64_tr_b16 a[162:163], v18 offset:20992\n"
"v_mul_f32 v142, v134, v142\n"
"v_exp_f32 v85, v85\n"
"v_mul_f32 v143, v135, v143\n"
"v_mfma_f32_32x32x16_bf16 v[192:207], a[176:179], a[108:111], v[192:207]\n"
"s_waitcnt lgkmcnt(4)\n"
"ds_read_b64_tr_b16 a[164:165], v19 offset:20480\n"
"ds_read_b64_tr_b16 a[166:167], v19 offset:20992\n"
"buffer_load_dwordx4 a[252:255], v88, s[8:11], s13 offen offset:32\n"
"v_fma_f32 v84, v84, v52, v52\n"
"v_fma_f32 v85, v85, v52, v52\n"
"v_mfma_f32_32x32x16_bf16 v[208:223], a[180:183], a[108:111], v[208:223]\n"
"ds_read_b64_tr_b16 a[168:169], v20 offset:20480\n"
"ds_read_b64_tr_b16 a[170:171], v20 offset:20992\n"
"v_rcp_f32 v84, v84\n"
"v_rcp_f32 v85, v85\n"
"v_mfma_f32_32x32x16_bf16 v[224:239], a[184:187], a[108:111], v[224:239]\n"
"s_waitcnt lgkmcnt(4)\n"
"ds_read_b64_tr_b16 a[172:173], v21 offset:20480\n"
"ds_read_b64_tr_b16 a[174:175], v21 offset:20992\n"
"v_mul_f32 v142, v142, v84\n"
"v_mul_f32 v143, v143, v85\n"
"v_cvt_pk_bf16_f32 v119, v142, v143\n"
"v_accvgpr_write_b32 a215, v119\n"
"v_mfma_f32_32x32x16_bf16 v[240:255], a[188:191], a[108:111], v[240:255]\n"
"ds_read_b64_tr_b16 a[176:177], v18 offset:24576\n"
"ds_read_b64_tr_b16 a[178:179], v18 offset:25088\n"
"buffer_store_dwordx4 a[212:215], v44, s[40:43], s67 offen offset:1024 sc1\n"
"v_mul_f32 v82, v144, v144\n"
"v_mfma_f32_32x32x16_bf16 v[192:207], a[160:163], a[112:115], v[192:207]\n"
"s_waitcnt lgkmcnt(4)\n"
"ds_read_b64_tr_b16 a[180:181], v19 offset:24576\n"
"ds_read_b64_tr_b16 a[182:183], v19 offset:25088\n"
"v_mul_f32 v83, v145, v145\n"
"v_fma_f32 v82, v51, v82, v50\n"
"v_fma_f32 v83, v51, v83, v50\n"
"v_mul_f32 v82, v144, v82\n"
"v_mfma_f32_32x32x16_bf16 v[208:223], a[164:167], a[112:115], v[208:223]\n"
"ds_read_b64_tr_b16 a[184:185], v20 offset:24576\n"
"ds_read_b64_tr_b16 a[186:187], v20 offset:25088\n"
"v_mul_f32 v83, v145, v83\n"
"v_exp_f32 v82, v82\n"
"v_mul_f32 v152, v144, v152\n"
"v_mfma_f32_32x32x16_bf16 v[224:239], a[168:171], a[112:115], v[224:239]\n"
"s_waitcnt lgkmcnt(4)\n"
"ds_read_b64_tr_b16 a[188:189], v21 offset:24576\n"
"ds_read_b64_tr_b16 a[190:191], v21 offset:25088\n"
"v_exp_f32 v83, v83\n"
"v_mul_f32 v153, v145, v153\n"
"v_fma_f32 v82, v82, v52, v52\n"
"v_mfma_f32_32x32x16_bf16 v[240:255], a[172:175], a[112:115], v[240:255]\n"
"ds_read_b64_tr_b16 a[160:161], v18 offset:28672\n"
"ds_read_b64_tr_b16 a[162:163], v18 offset:29184\n"
"buffer_load_dwordx4 v39, s[28:31], s66 offen offset:0 lds\n"
"v_fma_f32 v83, v83, v52, v52\n"
"v_mfma_f32_32x32x16_bf16 v[192:207], a[176:179], a[116:119], v[192:207]\n"
"s_waitcnt lgkmcnt(4)\n"
"ds_read_b64_tr_b16 a[164:165], v19 offset:28672\n"
"ds_read_b64_tr_b16 a[166:167], v19 offset:29184\n"
"v_rcp_f32 v82, v82\n"
"v_rcp_f32 v83, v83\n"
"v_mfma_f32_32x32x16_bf16 v[208:223], a[180:183], a[116:119], v[208:223]\n"
"ds_read_b64_tr_b16 a[168:169], v20 offset:28672\n"
"ds_read_b64_tr_b16 a[170:171], v20 offset:29184\n"
"buffer_load_dwordx4 v40, s[28:31], s66 offen offset:1024 lds\n"
"v_mul_f32 v152, v152, v82\n"
"v_mul_f32 v153, v153, v83\n"
"v_mfma_f32_32x32x16_bf16 v[224:239], a[184:187], a[116:119], v[224:239]\n"
"s_waitcnt lgkmcnt(4)\n"
"ds_read_b64_tr_b16 a[172:173], v21 offset:28672\n"
"ds_read_b64_tr_b16 a[174:175], v21 offset:29184\n"
"v_cvt_pk_bf16_f32 v120, v152, v153\n"
"v_accvgpr_write_b32 a216, v120\n"
"v_mul_f32 v84, v146, v146\n"
"v_mul_f32 v85, v147, v147\n"
"v_mfma_f32_32x32x16_bf16 v[240:255], a[188:191], a[116:119], v[240:255]\n"
"ds_read_b64_tr_b16 a[176:177], v18 offset:32768\n"
"ds_read_b64_tr_b16 a[178:179], v18 offset:33280\n"
"buffer_load_dwordx4 v41, s[28:31], s66 offen offset:2048 lds\n"
"v_fma_f32 v84, v51, v84, v50\n"
"v_fma_f32 v85, v51, v85, v50\n"
"v_mfma_f32_32x32x16_bf16 v[192:207], a[160:163], a[120:123], v[192:207]\n"
"s_waitcnt lgkmcnt(4)\n"
"ds_read_b64_tr_b16 a[180:181], v19 offset:32768\n"
"ds_read_b64_tr_b16 a[182:183], v19 offset:33280\n"
"v_mul_f32 v84, v146, v84\n"
"v_mul_f32 v85, v147, v85\n"
"v_exp_f32 v84, v84\n"
"v_mfma_f32_32x32x16_bf16 v[208:223], a[164:167], a[120:123], v[208:223]\n"
"ds_read_b64_tr_b16 a[184:185], v20 offset:32768\n"
"ds_read_b64_tr_b16 a[186:187], v20 offset:33280\n"
"buffer_load_dwordx4 v42, s[28:31], s66 offen offset:3072 lds\n"
"s_add_u32 m0, s59, 106496\n"
"v_mul_f32 v154, v146, v154\n"
"v_mfma_f32_32x32x16_bf16 v[224:239], a[168:171], a[120:123], v[224:239]\n"
"s_waitcnt vmcnt(20)\n"
"s_barrier\n"
"s_waitcnt lgkmcnt(4)\n"
"ds_read_b64_tr_b16 a[188:189], v21 offset:32768\n"
"ds_read_b64_tr_b16 a[190:191], v21 offset:33280\n"
"v_exp_f32 v85, v85\n"
"v_mfma_f32_32x32x16_bf16 v[240:255], a[172:175], a[120:123], v[240:255]\n"
"ds_read_b64_tr_b16 a[160:161], v18 offset:40960\n"
"ds_read_b64_tr_b16 a[162:163], v18 offset:41472\n"
"v_mul_f32 v155, v147, v155\n"
"v_fma_f32 v84, v84, v52, v52\n"
"v_fma_f32 v85, v85, v52, v52\n"
"v_mfma_f32_32x32x16_bf16 v[192:207], a[176:179], a[124:127], v[192:207]\n"
"s_waitcnt lgkmcnt(4)\n"
"ds_read_b64_tr_b16 a[164:165], v19 offset:40960\n"
"ds_read_b64_tr_b16 a[166:167], v19 offset:41472\n"
"buffer_load_dwordx4 v43, s[28:31], s66 offen lds\n"
"s_lshl_b32 s66, s6, 5\n"
"s_and_b32 s66, s66, 0xffffff80\n"
"s_add_u32 m0, s59, 81920\n"
"v_mfma_f32_32x32x16_bf16 v[208:223], a[180:183], a[124:127], v[208:223]\n"
"ds_read_b64_tr_b16 a[168:169], v20 offset:40960\n"
"ds_read_b64_tr_b16 a[170:171], v20 offset:41472\n"
"v_rcp_f32 v84, v84\n"
"v_rcp_f32 v85, v85\n"
"v_mfma_f32_32x32x16_bf16 v[224:239], a[184:187], a[124:127], v[224:239]\n"
"s_waitcnt lgkmcnt(4)\n"
"ds_read_b64_tr_b16 a[172:173], v21 offset:40960\n"
"ds_read_b64_tr_b16 a[174:175], v21 offset:41472\n"
"buffer_load_dwordx4 v30, s[32:35], s66 offen offset:0 lds\n"
"v_mul_f32 v154, v154, v84\n"
"v_mul_f32 v155, v155, v85\n"
"v_mfma_f32_32x32x16_bf16 v[240:255], a[188:191], a[124:127], v[240:255]\n"
"ds_read_b64_tr_b16 a[176:177], v18 offset:45056\n"
"ds_read_b64_tr_b16 a[178:179], v18 offset:45568\n"
"v_cvt_pk_bf16_f32 v121, v154, v155\n"
"v_accvgpr_write_b32 a217, v121\n"
"v_mul_f32 v82, v148, v148\n"
"v_mul_f32 v83, v149, v149\n"
"v_mfma_f32_32x32x16_bf16 v[192:207], a[160:163], a[128:131], v[192:207]\n"
"s_waitcnt lgkmcnt(4)\n"
"ds_read_b64_tr_b16 a[180:181], v19 offset:45056\n"
"ds_read_b64_tr_b16 a[182:183], v19 offset:45568\n"
"buffer_load_dwordx4 v31, s[32:35], s66 offen offset:1024 lds\n"
"v_fma_f32 v82, v51, v82, v50\n"
"v_fma_f32 v83, v51, v83, v50\n"
"v_mfma_f32_32x32x16_bf16 v[208:223], a[164:167], a[128:131], v[208:223]\n"
"ds_read_b64_tr_b16 a[184:185], v20 offset:45056\n"
"ds_read_b64_tr_b16 a[186:187], v20 offset:45568\n"
"s_waitcnt vmcnt(18)\n"
"v_mul_f32 v82, v148, v82\n"
"v_mul_f32 v83, v149, v83\n"
"v_exp_f32 v82, v82\n"
"v_mfma_f32_32x32x16_bf16 v[224:239], a[168:171], a[128:131], v[224:239]\n"
"s_waitcnt lgkmcnt(4)\n"
"ds_read_b64_tr_b16 a[188:189], v21 offset:45056\n"
"ds_read_b64_tr_b16 a[190:191], v21 offset:45568\n"
"buffer_store_dword v7, v4, s[40:43], s4 offen offset:128 sc1\n"
"v_mul_f32 v156, v148, v156\n"
"v_mfma_f32_32x32x16_bf16 v[240:255], a[172:175], a[128:131], v[240:255]\n"
"ds_read_b64_tr_b16 a[160:161], v18 offset:49152\n"
"ds_read_b64_tr_b16 a[162:163], v18 offset:49664\n"
"buffer_load_dwordx4 v32, s[32:35], s66 offen offset:2048 lds\n"
"s_waitcnt vmcnt(19)\n"
"v_accvgpr_read_b32 v77, a225\n"
"v_accvgpr_read_b32 v78, a227\n"
"v_mfma_f32_32x32x16_bf16 v[192:207], a[176:179], a[132:135], v[192:207]\n"
"s_waitcnt lgkmcnt(4)\n"
"ds_read_b64_tr_b16 a[164:165], v19 offset:49152\n"
"ds_read_b64_tr_b16 a[166:167], v19 offset:49664\n"
"v_xor_b32 v77, s56, v77\n"
"v_xor_b32 v78, s56, v78\n"
"v_or_b32 v76, v77, v78\n"
"s_waitcnt vmcnt(18)\n"
"v_accvgpr_read_b32 v77, a229\n"
"v_mfma_f32_32x32x16_bf16 v[208:223], a[180:183], a[132:135], v[208:223]\n"
"ds_read_b64_tr_b16 a[168:169], v20 offset:49152\n"
"ds_read_b64_tr_b16 a[170:171], v20 offset:49664\n"
"buffer_load_dwordx4 v33, s[32:35], s66 offen offset:3072 lds\n"
"s_add_u32 m0, s59, 86016\n"
"v_accvgpr_read_b32 v78, a231\n"
"v_mfma_f32_32x32x16_bf16 v[224:239], a[184:187], a[132:135], v[224:239]\n"
"s_waitcnt lgkmcnt(4)\n"
"ds_read_b64_tr_b16 a[172:173], v21 offset:49152\n"
"ds_read_b64_tr_b16 a[174:175], v21 offset:49664\n"
"v_xor_b32 v77, s56, v77\n"
"v_xor_b32 v78, s56, v78\n"
"v_or3_b32 v76, v76, v77, v78\n"
"s_waitcnt vmcnt(18)\n"
"v_accvgpr_read_b32 v77, a233\n"
"v_mfma_f32_32x32x16_bf16 v[240:255], a[188:191], a[132:135], v[240:255]\n"
"ds_read_b64_tr_b16 a[176:177], v18 offset:53248\n"
"ds_read_b64_tr_b16 a[178:179], v18 offset:53760\n"
"buffer_load_dwordx4 v38, s[32:35], s66 offen lds\n"
"s_lshl_b32 s66, s6, 5\n"
"s_and_b32 s66, s66, 0xffffff80\n"
"s_add_u32 m0, s59, 0\n"
"v_mfma_f32_32x32x16_bf16 v[192:207], a[160:163], a[136:139], v[192:207]\n"
"s_waitcnt lgkmcnt(4)\n"
"ds_read_b64_tr_b16 a[180:181], v19 offset:53248\n"
"ds_read_b64_tr_b16 a[182:183], v19 offset:53760\n"
"v_accvgpr_read_b32 v78, a235\n"
"v_xor_b32 v77, s56, v77\n"
"v_xor_b32 v78, s56, v78\n"
"v_or3_b32 v76, v76, v77, v78\n"
"v_mfma_f32_32x32x16_bf16 v[208:223], a[164:167], a[136:139], v[208:223]\n"
"ds_read_b64_tr_b16 a[184:185], v20 offset:53248\n"
"ds_read_b64_tr_b16 a[186:187], v20 offset:53760\n"
"buffer_load_dwordx4 v30, s[36:39], s66 offen offset:0 lds\n"
"s_waitcnt vmcnt(15)\n"
"v_accvgpr_read_b32 v77, a237\n"
"v_accvgpr_read_b32 v78, a239\n"
"v_mfma_f32_32x32x16_bf16 v[224:239], a[168:171], a[136:139], v[224:239]\n"
"s_barrier\n"
"s_waitcnt lgkmcnt(4)\n"
"ds_read_b64_tr_b16 a[188:189], v21 offset:53248\n"
"ds_read_b64_tr_b16 a[190:191], v21 offset:53760\n"
"v_xor_b32 v77, s56, v77\n"
"v_xor_b32 v78, s56, v78\n"
"v_mfma_f32_32x32x16_bf16 v[240:255], a[172:175], a[136:139], v[240:255]\n"
"ds_read_b64_tr_b16 a[160:161], v22 offset:0\n"
"ds_read_b64_tr_b16 a[162:163], v22 offset:512\n"
"buffer_load_dwordx4 v31, s[36:39], s66 offen offset:1024 lds\n"
"v_or3_b32 v76, v76, v77, v78\n"
"v_cmp_ne_u32 s[0:1], 0, v76\n"
"v_mfma_f32_32x32x16_bf16 v[192:207], a[176:179], a[140:143], v[192:207]\n"
"s_waitcnt lgkmcnt(4)\n"
"ds_read_b64_tr_b16 a[164:165], v23 offset:0\n"
"ds_read_b64_tr_b16 a[166:167], v23 offset:512\n"
"s_cmp_lg_u64 s[0:1], 0\n"
"s_cbranch_scc1 Lcs1A_slow%=\n"
"Lcs1A_ok%=:\n"
"v_accvgpr_write_b32 a225, 0\n"
"v_accvgpr_write_b32 a227, 0\n"
"v_mfma_f32_32x32x16_bf16 v[208:223], a[180:183], a[140:143], v[208:223]\n"
"ds_read_b64_tr_b16 a[168:169], v24 offset:0\n"
"ds_read_b64_tr_b16 a[170:171], v24 offset:512\n"
"buffer_load_dwordx4 v32, s[36:39], s66 offen offset:2048 lds\n"
"v_accvgpr_write_b32 a229, 0\n"
"v_accvgpr_write_b32 a231, 0\n"
"v_mfma_f32_32x32x16_bf16 v[224:239], a[184:187], a[140:143], v[224:239]\n"
"s_waitcnt lgkmcnt(4)\n"
"ds_read_b64_tr_b16 a[172:173], v25 offset:0\n"
"ds_read_b64_tr_b16 a[174:175], v25 offset:512\n"
"v_accvgpr_write_b32 a233, 0\n"
"v_accvgpr_write_b32 a235, 0\n"
"v_accvgpr_write_b32 a237, 0\n"
"v_mfma_f32_32x32x16_bf16 v[240:255], a[188:191], a[140:143], v[240:255]\n"
"ds_read_b64_tr_b16 a[176:177], v22 offset:4096\n"
"ds_read_b64_tr_b16 a[178:179], v22 offset:4608\n"
"buffer_load_dwordx4 v33, s[36:39], s66 offen offset:3072 lds\n"
"s_add_u32 m0, s59, 4096\n"
"v_accvgpr_write_b32 a239, 0\n"
"v_mfma_f32_32x32x16_bf16 v[192:207], a[160:163], a[144:147], v[192:207]\n"
"s_waitcnt lgkmcnt(4)\n"
"ds_read_b64_tr_b16 a[180:181], v23 offset:4096\n"
"ds_read_b64_tr_b16 a[182:183], v23 offset:4608\n"
"v_exp_f32 v83, v83\n"
"v_mul_f32 v157, v149, v157\n"
"v_fma_f32 v82, v82, v52, v52\n"
"v_mfma_f32_32x32x16_bf16 v[208:223], a[164:167], a[144:147], v[208:223]\n"
"buffer_load_dwordx4 v38, s[36:39], s66 offen lds\n"
"s_add_u32 s66, s5, 0\n"
"s_lshl_b32 s66, s66, 5\n"
"s_add_u32 m0, s59, 20480\n"
"v_fma_f32 v83, v83, v52, v52\n"
"v_mfma_f32_32x32x16_bf16 v[224:239], a[168:171], a[144:147], v[224:239]\n"
"s_waitcnt lgkmcnt(2)\n"
"v_rcp_f32 v82, v82\n"
"v_rcp_f32 v83, v83\n"
"v_mul_f32 v156, v156, v82\n"
"v_mul_f32 v157, v157, v83\n"
"v_mfma_f32_32x32x16_bf16 v[240:255], a[172:175], a[144:147], v[240:255]\n"
"buffer_load_dwordx4 v30, s[32:35], s66 offen offset:0 lds\n"
"v_cvt_pk_bf16_f32 v122, v156, v157\n"
"v_accvgpr_write_b32 a218, v122\n"
"v_mul_f32 v84, v150, v150\n"
"v_mul_f32 v85, v151, v151\n"
"v_mfma_f32_32x32x16_bf16 v[192:207], a[176:179], a[148:151], v[192:207]\n"
"s_waitcnt lgkmcnt(0)\n"
"v_fma_f32 v84, v51, v84, v50\n"
"v_fma_f32 v85, v51, v85, v50\n"
"v_mul_f32 v84, v150, v84\n"
"v_mul_f32 v85, v151, v85\n"
"v_exp_f32 v84, v84\n"
"v_mfma_f32_32x32x16_bf16 v[208:223], a[180:183], a[148:151], v[208:223]\n"
"ds_read_b64_tr_b16 a[184:185], v24 offset:4096\n"
"ds_read_b64_tr_b16 a[186:187], v24 offset:4608\n"
"buffer_load_dwordx4 v31, s[32:35], s66 offen offset:1024 lds\n"
"v_mul_f32 v158, v150, v158\n"
"v_mfma_f32_32x32x16_bf16 v[192:207], a[224:227], v[54:57], v[192:207]\n"
"ds_read_b64_tr_b16 a[188:189], v25 offset:4096\n"
"ds_read_b64_tr_b16 a[190:191], v25 offset:4608\n"
"v_exp_f32 v85, v85\n"
"v_mul_f32 v159, v151, v159\n"
"v_fma_f32 v84, v84, v52, v52\n"
"v_mfma_f32_32x32x16_bf16 v[208:223], a[228:231], v[54:57], v[208:223]\n"
"ds_read_b64_tr_b16 a[160:161], v22 offset:8192\n"
"ds_read_b64_tr_b16 a[162:163], v22 offset:8704\n"
"buffer_load_dwordx4 v32, s[32:35], s66 offen offset:2048 lds\n"
"v_fma_f32 v85, v85, v52, v52\n"
"v_mfma_f32_32x32x16_bf16 v[224:239], a[232:235], v[54:57], v[224:239]\n"
"s_waitcnt lgkmcnt(4)\n"
"ds_read_b64_tr_b16 a[164:165], v23 offset:8192\n"
"ds_read_b64_tr_b16 a[166:167], v23 offset:8704\n"
"v_rcp_f32 v84, v84\n"
"v_rcp_f32 v85, v85\n"
"v_mfma_f32_32x32x16_bf16 v[240:255], a[236:239], v[54:57], v[240:255]\n"
"ds_read_b64_tr_b16 a[168:169], v24 offset:8192\n"
"ds_read_b64_tr_b16 a[170:171], v24 offset:8704\n"
"buffer_load_dwordx4 v33, s[32:35], s66 offen offset:3072 lds\n"
"s_add_u32 m0, s59, 24576\n"
"v_mul_f32 v158, v158, v84\n"
"v_mfma_f32_32x32x16_bf16 v[224:239], a[184:187], a[148:151], v[224:239]\n"
"s_waitcnt lgkmcnt(4)\n"
"ds_read_b64_tr_b16 a[172:173], v25 offset:8192\n"
"ds_read_b64_tr_b16 a[174:175], v25 offset:8704\n"
"v_mul_f32 v159, v159, v85\n"
"v_cvt_pk_bf16_f32 v123, v158, v159\n"
"v_accvgpr_write_b32 a219, v123\n"
"v_mul_f32 v82, v160, v160\n"
"v_mfma_f32_32x32x16_bf16 v[240:255], a[188:191], a[148:151], v[240:255]\n"
"ds_read_b64_tr_b16 a[176:177], v22 offset:12288\n"
"ds_read_b64_tr_b16 a[178:179], v22 offset:12800\n"
"buffer_store_dwordx4 a[216:219], v44, s[40:43], s67 offen offset:2048 sc1\n"
"v_mul_f32 v83, v161, v161\n"
"v_mfma_f32_32x32x16_bf16 v[192:207], a[160:163], a[152:155], v[192:207]\n"
"s_waitcnt lgkmcnt(4)\n"
"ds_read_b64_tr_b16 a[180:181], v23 offset:12288\n"
"ds_read_b64_tr_b16 a[182:183], v23 offset:12800\n"
"v_fma_f32 v82, v51, v82, v50\n"
"v_fma_f32 v83, v51, v83, v50\n"
"v_mul_f32 v82, v160, v82\n"
"v_mul_f32 v83, v161, v83\n"
"v_mfma_f32_32x32x16_bf16 v[208:223], a[164:167], a[152:155], v[208:223]\n"
"ds_read_b64_tr_b16 a[184:185], v24 offset:12288\n"
"ds_read_b64_tr_b16 a[186:187], v24 offset:12800\n"
"v_exp_f32 v82, v82\n"
"v_mul_f32 v168, v160, v168\n"
"v_mfma_f32_32x32x16_bf16 v[224:239], a[168:171], a[152:155], v[224:239]\n"
"s_barrier\n"
"s_waitcnt lgkmcnt(4)\n"
"ds_read_b64_tr_b16 a[188:189], v25 offset:12288\n"
"ds_read_b64_tr_b16 a[190:191], v25 offset:12800\n"
"buffer_load_dwordx4 v38, s[32:35], s66 offen lds\n"
"s_add_u32 s66, s5, 0\n"
"v_mfma_f32_32x32x16_bf16 v[240:255], a[172:175], a[152:155], v[240:255]\n"
"ds_read_b64_tr_b16 a[160:161], v80 offset:0\n"
"ds_read_b64_tr_b16 a[162:163], v80 offset:1280\n"
"s_lshl_b32 s66, s66, 5\n"
"s_add_u32 m0, s59, 40960\n"
"v_exp_f32 v83, v83\n"
"v_mfma_f32_32x32x16_bf16 v[192:207], a[176:179], a[156:159], v[192:207]\n"
"s_waitcnt lgkmcnt(4)\n"
"ds_read_b64_tr_b16 a[164:165], v80 offset:64\n"
"ds_read_b64_tr_b16 a[166:167], v80 offset:1344\n"
"buffer_load_dwordx4 v30, s[36:39], s66 offen offset:0 lds\n"
"v_mul_f32 v169, v161, v169\n"
"v_fma_f32 v82, v82, v52, v52\n"
"v_mfma_f32_32x32x16_bf16 v[208:223], a[180:183], a[156:159], v[208:223]\n"
"ds_read_b64_tr_b16 a[168:169], v80 offset:128\n"
"ds_read_b64_tr_b16 a[170:171], v80 offset:1408\n"
"v_fma_f32 v83, v83, v52, v52\n"
"v_rcp_f32 v82, v82\n"
"v_mfma_f32_32x32x16_bf16 v[224:239], a[184:187], a[156:159], v[224:239]\n"
"s_waitcnt lgkmcnt(4)\n"
"ds_read_b64_tr_b16 a[172:173], v80 offset:192\n"
"ds_read_b64_tr_b16 a[174:175], v80 offset:1472\n"
"buffer_load_dwordx4 v31, s[36:39], s66 offen offset:1024 lds\n"
"v_rcp_f32 v83, v83\n"
"v_mfma_f32_32x32x16_bf16 v[240:255], a[188:191], a[156:159], v[240:255]\n"
"ds_read_b64_tr_b16 a[176:177], v80 offset:256\n"
"ds_read_b64_tr_b16 a[178:179], v80 offset:1536\n"
"v_mul_f32 v168, v168, v82\n"
"v_mul_f32 v169, v169, v83\n"
"v_cvt_pk_bf16_f32 v124, v168, v169\n"
"v_accvgpr_write_b32 a220, v124\n"
"v_mul_f32 v84, v162, v162\n"
"v_mul_f32 v85, v163, v163\n"
"v_fma_f32 v84, v51, v84, v50\n"
"v_fma_f32 v85, v51, v85, v50\n"
"v_mul_f32 v84, v162, v84\n"
"v_mul_f32 v85, v163, v85\n"
"v_exp_f32 v84, v84\n"
"v_mul_f32 v170, v162, v170\n"
"v_exp_f32 v85, v85\n"
"v_mul_f32 v171, v163, v171\n"
"v_fma_f32 v84, v84, v52, v52\n"
"v_fma_f32 v85, v85, v52, v52\n"
"v_rcp_f32 v84, v84\n"
"v_rcp_f32 v85, v85\n"
"v_mul_f32 v170, v170, v84\n"
"v_mul_f32 v171, v171, v85\n"
"v_cvt_pk_bf16_f32 v125, v170, v171\n"
"v_accvgpr_write_b32 a221, v125\n"
"v_mul_f32 v82, v164, v164\n"
"v_mul_f32 v83, v165, v165\n"
"v_fma_f32 v82, v51, v82, v50\n"
"v_fma_f32 v83, v51, v83, v50\n"
"v_mul_f32 v82, v164, v82\n"
"v_mul_f32 v83, v165, v83\n"
"v_exp_f32 v82, v82\n"
"v_mul_f32 v172, v164, v172\n"
"v_exp_f32 v83, v83\n"
"v_mul_f32 v173, v165, v173\n"
"v_fma_f32 v82, v82, v52, v52\n"
"v_fma_f32 v83, v83, v52, v52\n"
"v_rcp_f32 v82, v82\n"
"v_rcp_f32 v83, v83\n"
"v_mul_f32 v172, v172, v82\n"
"v_mul_f32 v173, v173, v83\n"
"v_cvt_pk_bf16_f32 v126, v172, v173\n"
"v_accvgpr_write_b32 a222, v126\n"
"v_mul_f32 v84, v166, v166\n"
"v_mul_f32 v85, v167, v167\n"
"v_fma_f32 v84, v51, v84, v50\n"
"v_fma_f32 v85, v51, v85, v50\n"
"v_mul_f32 v84, v166, v84\n"
"v_mul_f32 v85, v167, v85\n"
"v_exp_f32 v84, v84\n"
"v_mul_f32 v174, v166, v174\n"
"v_exp_f32 v85, v85\n"
"v_mul_f32 v175, v167, v175\n"
"v_fma_f32 v84, v84, v52, v52\n"
"v_fma_f32 v85, v85, v52, v52\n"
"v_rcp_f32 v84, v84\n"
"v_rcp_f32 v85, v85\n"
"v_mul_f32 v174, v174, v84\n"
"v_mul_f32 v175, v175, v85\n"
"v_cvt_pk_bf16_f32 v127, v174, v175\n"
"v_accvgpr_write_b32 a223, v127\n"
"v_mfma_f32_32x32x16_bf16 v[112:127], a[208:211], a[160:163], 0\n"
"s_waitcnt lgkmcnt(4)\n"
"ds_read_b64_tr_b16 a[180:181], v80 offset:5120\n"
"ds_read_b64_tr_b16 a[182:183], v80 offset:6400\n"
"buffer_store_dwordx4 a[220:223], v44, s[40:43], s67 offen offset:3072 sc1\n"
"s_add_u32 s67, s64, 1\n"
"s_lshl_b32 s67, s67, 12\n"
"v_mul_f32 v82, v192, v192\n"
"v_mul_f32 v83, v193, v193\n"
"v_fma_f32 v82, v51, v82, v50\n"
"v_fma_f32 v83, v51, v83, v50\n"
"v_mul_f32 v82, v192, v82\n"
"v_mul_f32 v83, v193, v83\n"
"v_exp_f32 v82, v82\n"
"v_mul_f32 v200, v192, v200\n"
"v_exp_f32 v83, v83\n"
"v_mul_f32 v201, v193, v201\n"
"v_fma_f32 v82, v82, v52, v52\n"
"v_fma_f32 v83, v83, v52, v52\n"
"v_rcp_f32 v82, v82\n"
"v_rcp_f32 v83, v83\n"
"v_mul_f32 v200, v200, v82\n"
"v_mfma_f32_32x32x16_bf16 v[128:143], a[208:211], a[164:167], 0\n"
"ds_read_b64_tr_b16 a[184:185], v80 offset:5184\n"
"ds_read_b64_tr_b16 a[186:187], v80 offset:6464\n"
"s_add_u32 s67, s67, s65\n"
"s_add_u32 s67, s67, 81920\n"
"buffer_load_dwordx4 v32, s[36:39], s66 offen offset:2048 lds\n"
"v_mul_f32 v201, v201, v83\n"
"v_cvt_pk_bf16_f32 v192, v200, v201\n"
"v_mul_f32 v84, v194, v194\n"
"v_mul_f32 v85, v195, v195\n"
"v_fma_f32 v84, v51, v84, v50\n"
"v_fma_f32 v85, v51, v85, v50\n"
"v_mul_f32 v84, v194, v84\n"
"v_mul_f32 v85, v195, v85\n"
"v_exp_f32 v84, v84\n"
"v_mul_f32 v202, v194, v202\n"
"v_exp_f32 v85, v85\n"
"v_mul_f32 v203, v195, v203\n"
"v_fma_f32 v84, v84, v52, v52\n"
"v_fma_f32 v85, v85, v52, v52\n"
"v_rcp_f32 v84, v84\n"
"v_rcp_f32 v85, v85\n"
"v_mfma_f32_32x32x16_bf16 v[144:159], a[208:211], a[168:171], 0\n"
"s_waitcnt lgkmcnt(4)\n"
"ds_read_b64_tr_b16 a[188:189], v80 offset:5248\n"
"ds_read_b64_tr_b16 a[190:191], v80 offset:6528\n"
"buffer_load_dwordx4 v33, s[36:39], s66 offen offset:3072 lds\n"
"s_add_u32 m0, s59, 45056\n"
"v_mul_f32 v202, v202, v84\n"
"v_mul_f32 v203, v203, v85\n"
"v_cvt_pk_bf16_f32 v193, v202, v203\n"
"v_mul_f32 v82, v196, v196\n"
"v_mul_f32 v83, v197, v197\n"
"v_fma_f32 v82, v51, v82, v50\n"
"v_fma_f32 v83, v51, v83, v50\n"
"v_mul_f32 v82, v196, v82\n"
"v_mul_f32 v83, v197, v83\n"
"v_exp_f32 v82, v82\n"
"v_mul_f32 v204, v196, v204\n"
"v_exp_f32 v83, v83\n"
"v_mul_f32 v205, v197, v205\n"
"v_fma_f32 v82, v82, v52, v52\n"
"v_fma_f32 v83, v83, v52, v52\n"
"v_rcp_f32 v82, v82\n"
"v_rcp_f32 v83, v83\n"
"v_mfma_f32_32x32x16_bf16 v[160:175], a[208:211], a[172:175], 0\n"
"ds_read_b64_tr_b16 a[160:161], v80 offset:5312\n"
"ds_read_b64_tr_b16 a[162:163], v80 offset:6592\n"
"buffer_load_dwordx4 v38, s[36:39], s66 offen lds\n"
"v_mul_f32 v204, v204, v82\n"
"v_mul_f32 v205, v205, v83\n"
"v_cvt_pk_bf16_f32 v194, v204, v205\n"
"v_mul_f32 v84, v198, v198\n"
"v_mul_f32 v85, v199, v199\n"
"v_fma_f32 v84, v51, v84, v50\n"
"v_fma_f32 v85, v51, v85, v50\n"
"v_mul_f32 v84, v198, v84\n"
"v_mul_f32 v85, v199, v85\n"
"v_exp_f32 v84, v84\n"
"v_mul_f32 v206, v198, v206\n"
"v_exp_f32 v85, v85\n"
"v_mul_f32 v207, v199, v207\n"
"v_fma_f32 v84, v84, v52, v52\n"
"v_fma_f32 v85, v85, v52, v52\n"
"v_rcp_f32 v84, v84\n"
"v_rcp_f32 v85, v85\n"
"v_mul_f32 v206, v206, v84\n"
"v_mfma_f32_32x32x16_bf16 v[176:191], a[208:211], a[176:179], 0\n"
"s_waitcnt lgkmcnt(4)\n"
"ds_read_b64_tr_b16 a[164:165], v80 offset:5376\n"
"ds_read_b64_tr_b16 a[166:167], v80 offset:6656\n"
"v_mul_f32 v207, v207, v85\n"
"v_cvt_pk_bf16_f32 v195, v206, v207\n"
"v_mul_f32 v82, v208, v208\n"
"v_mul_f32 v83, v209, v209\n"
"v_fma_f32 v82, v51, v82, v50\n"
"v_fma_f32 v83, v51, v83, v50\n"
"v_mul_f32 v82, v208, v82\n"
"v_mul_f32 v83, v209, v83\n"
"v_exp_f32 v82, v82\n"
"v_mul_f32 v216, v208, v216\n"
"v_exp_f32 v83, v83\n"
"v_mul_f32 v217, v209, v217\n"
"v_fma_f32 v82, v82, v52, v52\n"
"v_fma_f32 v83, v83, v52, v52\n"
"v_rcp_f32 v82, v82\n"
"v_rcp_f32 v83, v83\n"
"v_mul_f32 v216, v216, v82\n"
"v_mul_f32 v217, v217, v83\n"
"v_cvt_pk_bf16_f32 v196, v216, v217\n"
"v_mul_f32 v84, v210, v210\n"
"v_mfma_f32_32x32x16_bf16 v[112:127], a[212:215], a[180:183], v[112:127]\n"
"ds_read_b64_tr_b16 a[168:169], v80 offset:10240\n"
"ds_read_b64_tr_b16 a[170:171], v80 offset:11520\n"
"buffer_store_dwordx4 v[192:195], v44, s[40:43], s67 offen offset:0 sc1\n"
"v_mul_f32 v85, v211, v211\n"
"v_fma_f32 v84, v51, v84, v50\n"
"v_fma_f32 v85, v51, v85, v50\n"
"v_mul_f32 v84, v210, v84\n"
"v_mul_f32 v85, v211, v85\n"
"v_exp_f32 v84, v84\n"
"v_mul_f32 v218, v210, v218\n"
"v_exp_f32 v85, v85\n"
"v_mul_f32 v219, v211, v219\n"
"v_fma_f32 v84, v84, v52, v52\n"
"v_fma_f32 v85, v85, v52, v52\n"
"v_rcp_f32 v84, v84\n"
"v_rcp_f32 v85, v85\n"
"v_mul_f32 v218, v218, v84\n"
"v_mul_f32 v219, v219, v85\n"
"v_cvt_pk_bf16_f32 v197, v218, v219\n"
"v_mul_f32 v82, v212, v212\n"
"v_mfma_f32_32x32x16_bf16 v[128:143], a[212:215], a[184:187], v[128:143]\n"
"s_waitcnt lgkmcnt(4)\n"
"ds_read_b64_tr_b16 a[172:173], v80 offset:10304\n"
"ds_read_b64_tr_b16 a[174:175], v80 offset:11584\n"
"s_lshl_b32 s7, 1, s55\n"
"s_andn2_b32 s7, 15, s7\n"
"v_mov_b32 v3, 0\n"
"v_mul_f32 v83, v213, v213\n"
"v_fma_f32 v82, v51, v82, v50\n"
"v_fma_f32 v83, v51, v83, v50\n"
"v_mul_f32 v82, v212, v82\n"
"v_mul_f32 v83, v213, v83\n"
"v_exp_f32 v82, v82\n"
"v_mul_f32 v220, v212, v220\n"
"v_exp_f32 v83, v83\n"
"v_mul_f32 v221, v213, v221\n"
"v_fma_f32 v82, v82, v52, v52\n"
"v_fma_f32 v83, v83, v52, v52\n"
"v_rcp_f32 v82, v82\n"
"v_rcp_f32 v83, v83\n"
"v_mul_f32 v220, v220, v82\n"
"v_mul_f32 v221, v221, v83\n"
"v_cvt_pk_bf16_f32 v198, v220, v221\n"
"v_mul_f32 v84, v214, v214\n"
"v_mfma_f32_32x32x16_bf16 v[144:159], a[212:215], a[188:191], v[144:159]\n"
"ds_read_b64_tr_b16 a[176:177], v80 offset:10368\n"
"ds_read_b64_tr_b16 a[178:179], v80 offset:11648\n"
"buffer_atomic_add v3, v1, s[40:43], s4 offen offset:128 sc0 sc1\n"
"v_mul_f32 v85, v215, v215\n"
"v_fma_f32 v84, v51, v84, v50\n"
"v_fma_f32 v85, v51, v85, v50\n"
"v_mul_f32 v84, v214, v84\n"
"v_mul_f32 v85, v215, v85\n"
"v_exp_f32 v84, v84\n"
"v_mul_f32 v222, v214, v222\n"
"v_exp_f32 v85, v85\n"
"v_mul_f32 v223, v215, v223\n"
"v_fma_f32 v84, v84, v52, v52\n"
"v_fma_f32 v85, v85, v52, v52\n"
"v_rcp_f32 v84, v84\n"
"v_rcp_f32 v85, v85\n"
"v_mul_f32 v222, v222, v84\n"
"v_mul_f32 v223, v223, v85\n"
"v_cvt_pk_bf16_f32 v199, v222, v223\n"
"v_mul_f32 v82, v224, v224\n"
"v_mul_f32 v83, v225, v225\n"
"v_mfma_f32_32x32x16_bf16 v[160:175], a[212:215], a[160:163], v[160:175]\n"
"s_waitcnt lgkmcnt(4)\n"
"ds_read_b64_tr_b16 a[180:181], v80 offset:10432\n"
"ds_read_b64_tr_b16 a[182:183], v80 offset:11712\n"
"buffer_store_dwordx4 v[196:199], v44, s[40:43], s67 offen offset:1024 sc1\n"
"v_fma_f32 v82, v51, v82, v50\n"
"v_fma_f32 v83, v51, v83, v50\n"
"v_mul_f32 v82, v224, v82\n"
"v_mul_f32 v83, v225, v83\n"
"v_exp_f32 v82, v82\n"
"v_mul_f32 v232, v224, v232\n"
"v_exp_f32 v83, v83\n"
"v_mul_f32 v233, v225, v233\n"
"v_fma_f32 v82, v82, v52, v52\n"
"v_fma_f32 v83, v83, v52, v52\n"
"v_rcp_f32 v82, v82\n"
"v_rcp_f32 v83, v83\n"
"v_mul_f32 v232, v232, v82\n"
"v_mul_f32 v233, v233, v83\n"
"v_cvt_pk_bf16_f32 v200, v232, v233\n"
"v_mul_f32 v84, v226, v226\n"
"v_mul_f32 v85, v227, v227\n"
"v_mfma_f32_32x32x16_bf16 v[176:191], a[212:215], a[164:167], v[176:191]\n"
"ds_read_b64_tr_b16 a[184:185], v80 offset:10496\n"
"ds_read_b64_tr_b16 a[186:187], v80 offset:11776\n"
"v_fma_f32 v84, v51, v84, v50\n"
"v_fma_f32 v85, v51, v85, v50\n"
"v_mul_f32 v84, v226, v84\n"
"v_mul_f32 v85, v227, v85\n"
"v_exp_f32 v84, v84\n"
"v_mul_f32 v234, v226, v234\n"
"v_exp_f32 v85, v85\n"
"v_mul_f32 v235, v227, v235\n"
"v_fma_f32 v84, v84, v52, v52\n"
"v_fma_f32 v85, v85, v52, v52\n"
"v_rcp_f32 v84, v84\n"
"v_rcp_f32 v85, v85\n"
"v_mul_f32 v234, v234, v84\n"
"v_mul_f32 v235, v235, v85\n"
"v_cvt_pk_bf16_f32 v201, v234, v235\n"
"v_mul_f32 v82, v228, v228\n"
"v_mul_f32 v83, v229, v229\n"
"v_fma_f32 v82, v51, v82, v50\n"
"v_fma_f32 v83, v51, v83, v50\n"
"v_mul_f32 v82, v228, v82\n"
"v_mfma_f32_32x32x16_bf16 v[112:127], a[216:219], a[168:171], v[112:127]\n"
"s_waitcnt lgkmcnt(4)\n"
"ds_read_b64_tr_b16 a[188:189], v80 offset:15360\n"
"ds_read_b64_tr_b16 a[190:191], v80 offset:16640\n"
"v_mul_f32 v83, v229, v83\n"
"v_exp_f32 v82, v82\n"
"v_mul_f32 v236, v228, v236\n"
"v_exp_f32 v83, v83\n"
"v_mul_f32 v237, v229, v237\n"
"v_fma_f32 v82, v82, v52, v52\n"
"v_fma_f32 v83, v83, v52, v52\n"
"v_rcp_f32 v82, v82\n"
"v_rcp_f32 v83, v83\n"
"v_mul_f32 v236, v236, v82\n"
"v_mul_f32 v237, v237, v83\n"
"v_cvt_pk_bf16_f32 v202, v236, v237\n"
"v_mul_f32 v84, v230, v230\n"
"v_mul_f32 v85, v231, v231\n"
"v_fma_f32 v84, v51, v84, v50\n"
"v_fma_f32 v85, v51, v85, v50\n"
"v_mul_f32 v84, v230, v84\n"
"v_mul_f32 v85, v231, v85\n"
"v_exp_f32 v84, v84\n"
"v_mfma_f32_32x32x16_bf16 v[128:143], a[216:219], a[172:175], v[128:143]\n"
"ds_read_b64_tr_b16 a[160:161], v80 offset:15424\n"
"ds_read_b64_tr_b16 a[162:163], v80 offset:16704\n"
"v_mul_f32 v238, v230, v238\n"
"v_exp_f32 v85, v85\n"
"v_mul_f32 v239, v231, v239\n"
"v_fma_f32 v84, v84, v52, v52\n"
"v_fma_f32 v85, v85, v52, v52\n"
"v_rcp_f32 v84, v84\n"
"v_rcp_f32 v85, v85\n"
"v_mul_f32 v238, v238, v84\n"
"v_mul_f32 v239, v239, v85\n"
"v_cvt_pk_bf16_f32 v203, v238, v239\n"
"v_mul_f32 v82, v240, v240\n"
"v_mul_f32 v83, v241, v241\n"
"v_fma_f32 v82, v51, v82, v50\n"
"v_fma_f32 v83, v51, v83, v50\n"
"v_mul_f32 v82, v240, v82\n"
"v_mul_f32 v83, v241, v83\n"
"v_exp_f32 v82, v82\n"
"v_mul_f32 v248, v240, v248\n"
"v_exp_f32 v83, v83\n"
"v_mfma_f32_32x32x16_bf16 v[144:159], a[216:219], a[176:179], v[144:159]\n"
"s_waitcnt lgkmcnt(4)\n"
"ds_read_b64_tr_b16 a[164:165], v80 offset:15488\n"
"ds_read_b64_tr_b16 a[166:167], v80 offset:16768\n"
"buffer_store_dwordx4 v[200:203], v44, s[40:43], s67 offen offset:2048 sc1\n"
"s_waitcnt vmcnt(2)\n"
"v_cmp_ne_u32 s[0:1], s56, v3\n"
"s_nop 0\n"
"s_and_b32 s0, s0, s7\n"
"s_cbranch_scc1 Lh1A_slow%=\n"
"Lh1A_ok%=:\n"
"v_mul_f32 v249, v241, v249\n"
"v_fma_f32 v82, v82, v52, v52\n"
"v_fma_f32 v83, v83, v52, v52\n"
"v_rcp_f32 v82, v82\n"
"v_rcp_f32 v83, v83\n"
"v_mul_f32 v248, v248, v82\n"
"v_mul_f32 v249, v249, v83\n"
"v_cvt_pk_bf16_f32 v204, v248, v249\n"
"v_mul_f32 v84, v242, v242\n"
"v_mul_f32 v85, v243, v243\n"
"v_fma_f32 v84, v51, v84, v50\n"
"v_fma_f32 v85, v51, v85, v50\n"
"v_mul_f32 v84, v242, v84\n"
"v_mul_f32 v85, v243, v85\n"
"v_exp_f32 v84, v84\n"
"v_mfma_f32_32x32x16_bf16 v[160:175], a[216:219], a[180:183], v[160:175]\n"
"ds_read_b64_tr_b16 a[168:169], v80 offset:15552\n"
"ds_read_b64_tr_b16 a[170:171], v80 offset:16832\n"
"buffer_store_dword v7, v4, s[40:43], s4 offen offset:0 sc1\n"
"s_xor_b32 s2, s63, 1\n"
"s_sub_u32 s2, s2, 1\n"
"v_mul_f32 v250, v242, v250\n"
"v_exp_f32 v85, v85\n"
"v_mul_f32 v251, v243, v251\n"
"v_fma_f32 v84, v84, v52, v52\n"
"v_fma_f32 v85, v85, v52, v52\n"
"v_rcp_f32 v84, v84\n"
"v_rcp_f32 v85, v85\n"
"v_mul_f32 v250, v250, v84\n"
"v_mul_f32 v251, v251, v85\n"
"v_cvt_pk_bf16_f32 v205, v250, v251\n"
"v_mul_f32 v82, v244, v244\n"
"v_mul_f32 v83, v245, v245\n"
"v_fma_f32 v82, v51, v82, v50\n"
"v_fma_f32 v83, v51, v83, v50\n"
"v_mul_f32 v82, v244, v82\n"
"v_mul_f32 v83, v245, v83\n"
"v_mfma_f32_32x32x16_bf16 v[176:191], a[216:219], a[184:187], v[176:191]\n"
"s_barrier\n"
"s_waitcnt lgkmcnt(4)\n"
"ds_read_b64_tr_b16 a[172:173], v80 offset:15616\n"
"ds_read_b64_tr_b16 a[174:175], v80 offset:16896\n"
"s_lshl_b32 s2, s2, 11\n"
"s_add_u32 s2, s2, s65\n"
"s_add_u32 s3, s2, 4096\n"
"v_exp_f32 v82, v82\n"
"v_mul_f32 v252, v244, v252\n"
"v_exp_f32 v83, v83\n"
"v_mul_f32 v253, v245, v253\n"
"v_fma_f32 v82, v82, v52, v52\n"
"v_fma_f32 v83, v83, v52, v52\n"
"v_rcp_f32 v82, v82\n"
"v_rcp_f32 v83, v83\n"
"v_mul_f32 v252, v252, v82\n"
"v_mul_f32 v253, v253, v83\n"
"v_cvt_pk_bf16_f32 v206, v252, v253\n"
"v_mul_f32 v84, v246, v246\n"
"v_mul_f32 v85, v247, v247\n"
"v_fma_f32 v84, v51, v84, v50\n"
"v_fma_f32 v85, v51, v85, v50\n"
"v_mfma_f32_32x32x16_bf16 v[112:127], a[220:223], a[188:191], v[112:127]\n"
"ds_read_b64_tr_b16 a[176:177], v27 offset:40960\n"
"ds_read_b64_tr_b16 a[178:179], v27 offset:42240\n"
"buffer_load_dwordx4 a[0:3], v44, s[40:43], s2 offen offset:2048 sc1\n"
"s_add_u32 s66, s5, 4\n"
"s_lshl_b32 s66, s66, 5\n"
"s_add_u32 m0, s59, 61440\n"
"v_mul_f32 v84, v246, v84\n"
"v_mul_f32 v85, v247, v85\n"
"v_exp_f32 v84, v84\n"
"v_mul_f32 v254, v246, v254\n"
"v_exp_f32 v85, v85\n"
"v_mul_f32 v255, v247, v255\n"
"v_fma_f32 v84, v84, v52, v52\n"
"v_fma_f32 v85, v85, v52, v52\n"
"v_rcp_f32 v84, v84\n"
"v_rcp_f32 v85, v85\n"
"v_mul_f32 v254, v254, v84\n"
"v_mul_f32 v255, v255, v85\n"
"v_cvt_pk_bf16_f32 v207, v254, v255\n"
"v_mfma_f32_32x32x16_bf16 v[128:143], a[220:223], a[160:163], v[128:143]\n"
"s_waitcnt lgkmcnt(4)\n"
"ds_read_b64_tr_b16 a[180:181], v27 offset:41024\n"
"ds_read_b64_tr_b16 a[182:183], v27 offset:42304\n"
"buffer_store_dwordx4 v[204:207], v44, s[40:43], s67 offen offset:3072 sc1\n"
"s_xor_b32 s2, s63, 1\n"
"s_sub_u32 s2, s2, 1\n"
"v_mfma_f32_32x32x16_bf16 v[144:159], a[220:223], a[164:167], v[144:159]\n"
"ds_read_b64_tr_b16 a[184:185], v27 offset:41088\n"
"ds_read_b64_tr_b16 a[186:187], v27 offset:42368\n"
"s_lshl_b32 s2, s2, 11\n"
"s_add_u32 s2, s2, s65\n"
"s_add_u32 s3, s2, 4096\n"
"v_mfma_f32_32x32x16_bf16 v[160:175], a[220:223], a[168:171], v[160:175]\n"
"s_waitcnt lgkmcnt(4)\n"
"ds_read_b64_tr_b16 a[188:189], v27 offset:41152\n"
"ds_read_b64_tr_b16 a[190:191], v27 offset:42432\n"
"v_mfma_f32_32x32x16_bf16 v[176:191], a[220:223], a[172:175], v[176:191]\n"
"ds_read_b64_tr_b16 a[160:161], v27 offset:41216\n"
"ds_read_b64_tr_b16 a[162:163], v27 offset:42496\n"
"buffer_load_dwordx4 a[4:7], v44, s[40:43], s2 offen offset:3072 sc1\n"
"s_add_u32 s2, s5, 0\n"
"s_lshl_b32 s2, s2, 11\n"
"s_add_u32 s2, s2, s65\n"
"v_mfma_f32_32x32x16_bf16 v[112:127], v[192:195], a[176:179], v[112:127]\n"
"s_waitcnt lgkmcnt(4)\n"
"ds_read_b64_tr_b16 a[164:165], v27 offset:46080\n"
"ds_read_b64_tr_b16 a[166:167], v27 offset:47360\n"
"s_add_u32 s3, s2, 4096\n"
"v_mfma_f32_32x32x16_bf16 v[128:143], v[192:195], a[180:183], v[128:143]\n"
"ds_read_b64_tr_b16 a[168:169], v27 offset:46144\n"
"ds_read_b64_tr_b16 a[170:171], v27 offset:47424\n"
"buffer_load_dwordx4 a[8:11], v44, s[40:43], s2 offen offset:0 sc1\n"
"v_mfma_f32_32x32x16_bf16 v[144:159], v[192:195], a[184:187], v[144:159]\n"
"s_waitcnt lgkmcnt(4)\n"
"ds_read_b64_tr_b16 a[172:173], v27 offset:46208\n"
"ds_read_b64_tr_b16 a[174:175], v27 offset:47488\n"
"v_mfma_f32_32x32x16_bf16 v[160:175], v[192:195], a[188:191], v[160:175]\n"
"ds_read_b64_tr_b16 a[176:177], v27 offset:46272\n"
"ds_read_b64_tr_b16 a[178:179], v27 offset:47552\n"
"buffer_load_dwordx4 a[12:15], v44, s[40:43], s2 offen offset:2048 sc1\n"
"v_mfma_f32_32x32x16_bf16 v[176:191], v[192:195], a[160:163], v[176:191]\n"
"s_waitcnt lgkmcnt(4)\n"
"ds_read_b64_tr_b16 a[180:181], v27 offset:46336\n"
"ds_read_b64_tr_b16 a[182:183], v27 offset:47616\n"
"v_mfma_f32_32x32x16_bf16 v[112:127], v[196:199], a[164:167], v[112:127]\n"
"ds_read_b64_tr_b16 a[184:185], v27 offset:51200\n"
"ds_read_b64_tr_b16 a[186:187], v27 offset:52480\n"
"buffer_load_dwordx4 a[16:19], v44, s[40:43], s3 offen offset:0 sc1\n"
"v_mfma_f32_32x32x16_bf16 v[128:143], v[196:199], a[168:171], v[128:143]\n"
"s_waitcnt lgkmcnt(4)\n"
"ds_read_b64_tr_b16 a[188:189], v27 offset:51264\n"
"ds_read_b64_tr_b16 a[190:191], v27 offset:52544\n"
"buffer_load_dwordx4 a[20:23], v44, s[40:43], s3 offen offset:2048 sc1\n"
"s_add_u32 s2, s5, 0\n"
"s_lshl_b32 s2, s2, 11\n"
"s_add_u32 s2, s2, s65\n"
"v_mfma_f32_32x32x16_bf16 v[144:159], v[196:199], a[172:175], v[144:159]\n"
"ds_read_b64_tr_b16 a[160:161], v27 offset:51328\n"
"ds_read_b64_tr_b16 a[162:163], v27 offset:52608\n"
"s_add_u32 s3, s2, 4096\n"
"v_mfma_f32_32x32x16_bf16 v[160:175], v[196:199], a[176:179], v[160:175]\n"
"s_waitcnt lgkmcnt(4)\n"
"ds_read_b64_tr_b16 a[164:165], v27 offset:51392\n"
"ds_read_b64_tr_b16 a[166:167], v27 offset:52672\n"
"buffer_load_dwordx4 a[24:27], v44, s[40:43], s2 offen offset:1024 sc1\n"
"v_mfma_f32_32x32x16_bf16 v[176:191], v[196:199], a[180:183], v[176:191]\n"
"ds_read_b64_tr_b16 a[168:169], v27 offset:51456\n"
"ds_read_b64_tr_b16 a[170:171], v27 offset:52736\n"
"v_mfma_f32_32x32x16_bf16 v[112:127], v[200:203], a[184:187], v[112:127]\n"
"s_waitcnt lgkmcnt(4)\n"
"ds_read_b64_tr_b16 a[172:173], v27 offset:56320\n"
"ds_read_b64_tr_b16 a[174:175], v27 offset:57600\n"
"buffer_load_dwordx4 a[28:31], v44, s[40:43], s2 offen offset:3072 sc1\n"
"v_mfma_f32_32x32x16_bf16 v[128:143], v[200:203], a[188:191], v[128:143]\n"
"ds_read_b64_tr_b16 a[176:177], v27 offset:56384\n"
"ds_read_b64_tr_b16 a[178:179], v27 offset:57664\n"
"v_mfma_f32_32x32x16_bf16 v[144:159], v[200:203], a[160:163], v[144:159]\n"
"s_waitcnt lgkmcnt(4)\n"
"ds_read_b64_tr_b16 a[180:181], v27 offset:56448\n"
"ds_read_b64_tr_b16 a[182:183], v27 offset:57728\n"
"buffer_load_dwordx4 a[32:35], v44, s[40:43], s3 offen offset:1024 sc1\n"
"v_mfma_f32_32x32x16_bf16 v[160:175], v[200:203], a[164:167], v[160:175]\n"
"ds_read_b64_tr_b16 a[184:185], v27 offset:56512\n"
"ds_read_b64_tr_b16 a[186:187], v27 offset:57792\n"
"v_mfma_f32_32x32x16_bf16 v[176:191], v[200:203], a[168:171], v[176:191]\n"
"s_barrier\n"
"s_waitcnt lgkmcnt(4)\n"
"ds_read_b64_tr_b16 a[188:189], v27 offset:56576\n"
"ds_read_b64_tr_b16 a[190:191], v27 offset:57856\n"
"buffer_load_dwordx4 a[36:39], v44, s[40:43], s3 offen offset:3072 sc1\n"
"s_add_u32 s2, s5, 4\n"
"v_mfma_f32_32x32x16_bf16 v[112:127], v[204:207], a[172:175], v[112:127]\n"
"ds_read_b128 a[160:163], v28 offset:20480\n"
"s_lshl_b32 s2, s2, 11\n"
"s_add_u32 s2, s2, s65\n"
"s_add_u32 s3, s2, 4096\n"
"buffer_load_dwordx4 a[40:43], v44, s[40:43], s2 offen offset:0 sc1\n"
"v_mfma_f32_32x32x16_bf16 v[128:143], v[204:207], a[176:179], v[128:143]\n"
"s_waitcnt lgkmcnt(3)\n"
"ds_read_b128 a[164:167], v28 offset:24576\n"
"v_mfma_f32_32x32x16_bf16 v[144:159], v[204:207], a[180:183], v[144:159]\n"
"ds_read_b128 a[168:171], v28 offset:28672\n"
"buffer_load_dwordx4 a[44:47], v44, s[40:43], s2 offen offset:2048 sc1\n"
"v_mfma_f32_32x32x16_bf16 v[160:175], v[204:207], a[184:187], v[160:175]\n"
"s_waitcnt lgkmcnt(2)\n"
"ds_read_b128 a[172:175], v28 offset:32768\n"
"v_mfma_f32_32x32x16_bf16 v[176:191], v[204:207], a[188:191], v[176:191]\n"
"ds_read_b128 a[176:179], v28 offset:36864\n"
"v_subrev_u32 v28, 61440, v28\n"
"buffer_load_dwordx4 a[48:51], v44, s[40:43], s3 offen offset:0 sc1\n"
"v_mfma_f32_32x32x16_bf16 v[112:127], a[192:195], a[160:163], v[112:127]\n"
"s_waitcnt lgkmcnt(2)\n"
"ds_read_b128 a[180:183], v29 offset:20480\n"
"s_lshl_b32 s60, s53, 4\n"
"s_add_u32 s60, s60, s55\n"
"s_mul_i32 s60, s60, 40960\n"
"s_add_u32 s60, s60, 0xc00000\n"
"s_mul_i32 s61, s54, 10240\n"
"v_mfma_f32_32x32x16_bf16 v[128:143], a[192:195], a[164:167], v[128:143]\n"
"ds_read_b128 a[184:187], v29 offset:24576\n"
"buffer_load_dwordx4 a[52:55], v44, s[40:43], s3 offen offset:2048 sc1\n"
"s_add_u32 s61, s61, s60\n"
"s_lshl_b32 s67, s52, 11\n"
"s_mul_i32 s62, s54, 163840\n"
"s_add_u32 s2, s5, 4\n"
"v_mfma_f32_32x32x16_bf16 v[144:159], a[192:195], a[168:171], v[144:159]\n"
"s_waitcnt lgkmcnt(2)\n"
"ds_read_b128 a[188:191], v29 offset:28672\n"
"s_lshl_b32 s2, s2, 11\n"
"s_add_u32 s2, s2, s65\n"
"s_add_u32 s3, s2, 4096\n"
"s_add_u32 s60, s60, s62\n"
"s_add_u32 s62, s54, 1\n"
"v_mfma_f32_32x32x16_bf16 v[160:175], a[192:195], a[172:175], v[160:175]\n"
"ds_read_b128 a[160:163], v29 offset:32768\n"
"buffer_load_dwordx4 a[56:59], v44, s[40:43], s2 offen offset:1024 sc1\n"
"s_and_b32 s62, s62, 3\n"
"s_mul_i32 s12, s62, 163840\n"
"s_add_u32 s12, s12, s61\n"
"v_mfma_f32_32x32x16_bf16 v[176:191], a[192:195], a[176:179], v[176:191]\n"
"s_barrier\n"
"s_waitcnt vmcnt(15) lgkmcnt(2)\n"
"ds_read_b128 a[164:167], v29 offset:36864\n"
"v_subrev_u32 v29, 61440, v29\n"
"s_add_u32 s12, s12, s67\n"
"s_mul_i32 s62, s62, 10240\n"
"v_mfma_f32_32x32x16_bf16 v[112:127], a[0:3], a[180:183], v[112:127]\n"
"ds_read_b128 a[168:171], v28 offset:0\n"
"buffer_load_dwordx4 a[60:63], v44, s[40:43], s2 offen offset:3072 sc1\n"
"s_add_u32 s62, s62, s60\n"
"s_add_u32 s16, s62, s67\n"
"s_add_u32 s62, s62, 8192\n"
"v_mfma_f32_32x32x16_bf16 v[128:143], a[0:3], a[184:187], v[128:143]\n"
"s_waitcnt lgkmcnt(2)\n"
"ds_read_b128 a[172:175], v28 offset:4096\n"
"s_cmp_eq_u32 s52, s54\n"
"s_cselect_b32 s19, s62, 0x70000000\n"
"s_add_u32 s62, s54, 2\n"
"s_and_b32 s62, s62, 3\n"
"s_mul_i32 s13, s62, 163840\n"
"v_mfma_f32_32x32x16_bf16 v[144:159], a[0:3], a[188:191], v[144:159]\n"
"ds_read_b128 a[176:179], v28 offset:8192\n"
"buffer_load_dwordx4 a[64:67], v44, s[40:43], s3 offen offset:1024 sc1\n"
"s_add_u32 s13, s13, s61\n"
"s_add_u32 s13, s13, s67\n"
"s_mul_i32 s62, s62, 10240\n"
"v_mfma_f32_32x32x16_bf16 v[160:175], a[0:3], a[160:163], v[160:175]\n"
"s_waitcnt lgkmcnt(2)\n"
"ds_read_b128 a[180:183], v28 offset:12288\n"
"buffer_load_dwordx4 a[68:71], v44, s[40:43], s3 offen offset:3072 sc1\n"
"s_add_u32 s62, s62, s60\n"
"s_add_u32 s17, s62, s67\n"
"s_add_u32 s62, s62, 8192\n"
"v_mfma_f32_32x32x16_bf16 v[176:191], a[0:3], a[164:167], v[176:191]\n"
"ds_read_b128 a[184:187], v28 offset:16384\n"
"s_cmp_eq_u32 s52, s54\n"
"s_cselect_b32 s20, s62, 0x70000000\n"
"s_add_u32 s62, s54, 3\n"
"s_and_b32 s62, s62, 3\n"
"s_mul_i32 s14, s62, 163840\n"
"v_mfma_f32_32x32x16_bf16 v[112:127], a[196:199], a[168:171], v[112:127]\n"
"s_waitcnt lgkmcnt(2)\n"
"ds_read_b128 a[188:191], v29 offset:0\n"
"buffer_load_dwordx4 v30, s[32:35], s66 offen offset:0 lds\n"
"s_add_u32 s14, s14, s61\n"
"s_add_u32 s14, s14, s67\n"
"s_mul_i32 s62, s62, 10240\n"
"v_mfma_f32_32x32x16_bf16 v[128:143], a[196:199], a[172:175], v[128:143]\n"
"ds_read_b128 a[160:163], v29 offset:4096\n"
"s_add_u32 s62, s62, s60\n"
"s_add_u32 s18, s62, s67\n"
"s_add_u32 s62, s62, 8192\n"
"s_cmp_eq_u32 s52, s54\n"
"s_cselect_b32 s21, s62, 0x70000000\n"
"v_mfma_f32_32x32x16_bf16 v[144:159], a[196:199], a[176:179], v[144:159]\n"
"s_waitcnt lgkmcnt(2)\n"
"ds_read_b128 a[164:167], v29 offset:8192\n"
"buffer_load_dwordx4 v31, s[32:35], s66 offen offset:1024 lds\n"
"s_lshl_b32 s7, 1, s55\n"
"s_andn2_b32 s7, 12, s7\n"
"v_mov_b32 v3, 0\n"
"v_mfma_f32_32x32x16_bf16 v[160:175], a[196:199], a[180:183], v[160:175]\n"
"ds_read_b128 a[168:171], v29 offset:12288\n"
"s_mul_i32 s62, s52, 163840\n"
"s_add_u32 s62, s62, s61\n"
"s_add_u32 s62, s62, 8192\n"
"s_cmp_eq_u32 s52, s54\n"
"s_cselect_b32 s15, 0x70000000, s62\n"
"v_mfma_f32_32x32x16_bf16 v[176:191], a[196:199], a[184:187], v[176:191]\n"
"s_barrier\n"
"s_waitcnt vmcnt(18) lgkmcnt(2)\n"
"ds_read_b128 a[172:175], v29 offset:16384\n"
"buffer_load_dwordx4 v32, s[32:35], s66 offen offset:2048 lds\n"
"s_lshl_b32 s24, s53, 2\n"
"v_mfma_f32_32x32x16_bf16 v[112:127], a[4:7], a[188:191], v[112:127]\n"
"ds_read_b128 a[176:179], v10 offset:20480\n"
"s_add_u32 s24, s24, s55\n"
"s_lshl_b32 s24, s24, 2\n"
"s_add_u32 s24, s24, s52\n"
"s_lshl_b32 s24, s24, 7\n"
"s_add_u32 s24, s24, 0x1798000\n"
"v_mfma_f32_32x32x16_bf16 v[128:143], a[4:7], a[160:163], v[128:143]\n"
"s_waitcnt lgkmcnt(2)\n"
"ds_read_b128 a[180:183], v10 offset:24576\n"
"buffer_load_dwordx4 v33, s[32:35], s66 offen offset:3072 lds\n"
"s_add_u32 m0, s59, 65536\n"
"s_mov_b32 s26, 0x3f80\n"
"s_mov_b32 s27, 0x3f800000\n"
"v_mfma_f32_32x32x16_bf16 v[144:159], a[4:7], a[164:167], v[144:159]\n"
"ds_read_b128 a[184:187], v10 offset:28672\n"
"v_and_b32 v92, 31, v2\n"
"v_lshrrev_b32 v93, 5, v2\n"
"v_lshlrev_b32 v96, 1, v92\n"
"s_lshl_b32 s60, s53, 2\n"
"s_add_u32 s60, s60, s52\n"
"v_mfma_f32_32x32x16_bf16 v[160:175], a[4:7], a[168:171], v[160:175]\n"
"s_waitcnt vmcnt(19) lgkmcnt(2)\n"
"ds_read_b128 a[188:191], v10 offset:32768\n"
"buffer_store_dword v7, v4, s[40:43], s4 offen offset:256 sc1\n"
"buffer_load_dwordx4 v38, s[32:35], s66 offen lds\n"
"v_mfma_f32_32x32x16_bf16 v[176:191], a[4:7], a[172:175], v[176:191]\n"
"ds_read_b128 a[160:163], v10 offset:36864\n"
"s_mul_i32 s60, s60, 40960\n"
"s_mul_i32 s61, s55, 320\n"
"s_lshl_b32 s62, s54, 6\n"
"s_add_u32 s62, s62, s61\n"
"s_add_u32 s61, s61, 256\n"
"s_add_u32 s66, s5, 4\n"
"v_mfma_f32_32x32x16_bf16 v[112:127], a[8:11], a[176:179], v[112:127]\n"
"s_waitcnt lgkmcnt(2)\n"
"ds_read_b128 a[164:167], v11 offset:20480\n"
"s_add_u32 s22, s60, s62\n"
"s_add_u32 s23, s60, s61\n"
"s_cmp_eq_u32 s52, s54\n"
"s_cselect_b32 s61, s61, 0x70000000\n"
"s_lshl_b32 s66, s66, 5\n"
"s_add_u32 m0, s59, 102400\n"
"v_mfma_f32_32x32x16_bf16 v[128:143], a[8:11], a[180:183], v[128:143]\n"
"ds_read_b128 a[168:171], v11 offset:24576\n"
"buffer_load_ushort v90, v96, s[48:51], s62 offen\n"
"buffer_load_ushort v91, v96, s[48:51], s61 offen\n"
"s_lshl_b32 s22, s22, 1\n"
"v_mfma_f32_32x32x16_bf16 v[144:159], a[8:11], a[184:187], v[144:159]\n"
"s_waitcnt lgkmcnt(2)\n"
"ds_read_b128 a[172:175], v11 offset:28672\n"
"buffer_atomic_add v3, v1, s[40:43], s4 offen offset:0 sc0 sc1\n"
"s_lshl_b32 s23, s23, 1\n"
"v_mul_u32_u24 v95, 20480, v93\n"
"v_lshl_add_u32 v95, v92, 2, v95\n"
"v_mfma_f32_32x32x16_bf16 v[160:175], a[8:11], a[188:191], v[160:175]\n"
"ds_read_b128 a[176:179], v11 offset:32768\n"
"buffer_load_dwordx4 v30, s[36:39], s66 offen offset:0 lds\n"
"v_lshlrev_b32 v93, 3, v93\n"
"v_sub_u32 v93, v92, v93\n"
"v_ashrrev_i32 v93, 1, v93\n"
"v_mfma_f32_32x32x16_bf16 v[176:191], a[8:11], a[160:163], v[176:191]\n"
"s_waitcnt vmcnt(24) lgkmcnt(2)\n"
"ds_read_b128 a[180:183], v11 offset:36864\n"
"v_and_b32 v92, 1, v92\n"
"v_lshlrev_b32 v92, 4, v92\n"
"v_lshlrev_b32 v94, v92, s26\n"
"v_accvgpr_read_b32 v97, a240\n"
"v_lshlrev_b32 v96, 16, v97\n"
"v_mfma_f32_32x32x16_bf16 v[112:127], a[12:15], a[164:167], v[112:127]\n"
"ds_read_b128 a[184:187], v12 offset:20480\n"
"buffer_load_dwordx4 v31, s[36:39], s66 offen offset:1024 lds\n"
"v_and_b32 v97, s100, v97\n"
"v_fma_f32 v96, v96, v5, -v6\n"
"v_fma_f32 v97, v97, v5, -v6\n"
"v_mfma_f32_32x32x16_bf16 v[128:143], a[12:15], a[168:171], v[128:143]\n"
"s_waitcnt lgkmcnt(2)\n"
"ds_read_b128 a[188:191], v12 offset:24576\n"
"v_cvt_pk_bf16_f32 v224, v96, v97\n"
"v_accvgpr_read_b32 v99, a241\n"
"v_lshlrev_b32 v98, 16, v99\n"
"v_and_b32 v99, s100, v99\n"
"v_fma_f32 v98, v98, v5, -v6\n"
"v_mfma_f32_32x32x16_bf16 v[144:159], a[12:15], a[172:175], v[144:159]\n"
"ds_read_b128 a[160:163], v12 offset:28672\n"
"buffer_load_dwordx4 v32, s[36:39], s66 offen offset:2048 lds\n"
"v_fma_f32 v99, v99, v5, -v6\n"
"v_cvt_pk_bf16_f32 v225, v98, v99\n"
"v_accvgpr_read_b32 v97, a242\n"
"v_mfma_f32_32x32x16_bf16 v[160:175], a[12:15], a[176:179], v[160:175]\n"
"s_waitcnt vmcnt(25) lgkmcnt(2)\n"
"ds_read_b128 a[164:167], v12 offset:32768\n"
"v_lshlrev_b32 v96, 16, v97\n"
"v_and_b32 v97, s100, v97\n"
"v_fma_f32 v96, v96, v5, -v6\n"
"v_fma_f32 v97, v97, v5, -v6\n"
"v_cvt_pk_bf16_f32 v226, v96, v97\n"
"v_mfma_f32_32x32x16_bf16 v[176:191], a[12:15], a[180:183], v[176:191]\n"
"ds_read_b128 a[168:171], v12 offset:36864\n"
"buffer_load_dwordx4 v33, s[36:39], s66 offen offset:3072 lds\n"
"s_add_u32 m0, s59, 106496\n"
"v_accvgpr_read_b32 v99, a243\n"
"v_lshlrev_b32 v98, 16, v99\n"
"v_mfma_f32_32x32x16_bf16 v[112:127], a[16:19], a[184:187], v[112:127]\n"
"s_waitcnt lgkmcnt(2)\n"
"ds_read_b128 a[172:175], v13 offset:20480\n"
"v_and_b32 v99, s100, v99\n"
"v_fma_f32 v98, v98, v5, -v6\n"
"v_fma_f32 v99, v99, v5, -v6\n"
"v_cvt_pk_bf16_f32 v227, v98, v99\n"
"v_mfma_f32_32x32x16_bf16 v[128:143], a[16:19], a[188:191], v[128:143]\n"
"ds_read_b128 a[176:179], v13 offset:24576\n"
"buffer_load_dwordx4 v38, s[36:39], s66 offen lds\n"
"s_and_b32 s66, s55, 1\n"
"s_mul_i32 s66, s66, -4\n"
"s_add_u32 s66, s66, 2\n"
"s_add_u32 s66, s66, s64\n"
"v_mfma_f32_32x32x16_bf16 v[144:159], a[16:19], a[160:163], v[144:159]\n"
"s_waitcnt lgkmcnt(2)\n"
"ds_read_b128 a[180:183], v13 offset:28672\n"
"s_mul_i32 s66, s66, 81920\n"
"s_add_u32 m0, s59, 81920\n"
"v_accvgpr_read_b32 v97, a244\n"
"v_lshlrev_b32 v96, 16, v97\n"
"v_and_b32 v97, s100, v97\n"
"v_mfma_f32_32x32x16_bf16 v[160:175], a[16:19], a[164:167], v[160:175]\n"
"ds_read_b128 a[184:187], v13 offset:32768\n"
"buffer_load_dwordx4 v39, s[28:31], s66 offen offset:0 lds\n"
"v_fma_f32 v96, v96, v5, -v6\n"
"v_fma_f32 v97, v97, v5, -v6\n"
"v_cvt_pk_bf16_f32 v228, v96, v97\n"
"v_mfma_f32_32x32x16_bf16 v[176:191], a[16:19], a[168:171], v[176:191]\n"
"s_barrier\n"
"s_waitcnt vmcnt(27) lgkmcnt(2)\n"
"ds_read_b128 a[188:191], v13 offset:36864\n"
"v_accvgpr_read_b32 v99, a245\n"
"v_lshlrev_b32 v98, 16, v99\n"
"v_and_b32 v99, s100, v99\n"
"v_mfma_f32_32x32x16_bf16 v[112:127], a[20:23], a[172:175], v[112:127]\n"
"ds_read_b128 a[160:163], v10 offset:40960\n"
"buffer_load_dwordx4 v40, s[28:31], s66 offen offset:1024 lds\n"
"v_fma_f32 v98, v98, v5, -v6\n"
"v_fma_f32 v99, v99, v5, -v6\n"
"v_cvt_pk_bf16_f32 v229, v98, v99\n"
"v_mfma_f32_32x32x16_bf16 v[128:143], a[20:23], a[176:179], v[128:143]\n"
"s_waitcnt lgkmcnt(2)\n"
"ds_read_b128 a[164:167], v10 offset:45056\n"
"v_accvgpr_read_b32 v97, a246\n"
"v_lshlrev_b32 v96, 16, v97\n"
"v_and_b32 v97, s100, v97\n"
"v_fma_f32 v96, v96, v5, -v6\n"
"v_fma_f32 v97, v97, v5, -v6\n"
"v_mfma_f32_32x32x16_bf16 v[144:159], a[20:23], a[180:183], v[144:159]\n"
"ds_read_b128 a[168:171], v10 offset:49152\n"
"buffer_load_dwordx4 v41, s[28:31], s66 offen offset:2048 lds\n"
"s_waitcnt vmcnt(8)\n"
"v_cmp_ne_u32 s[0:1], s56, v3\n"
"s_nop 0\n"
"s_and_b32 s0, s0, s7\n"
"s_cbranch_scc1 Lh0A_slow%=\n"
"Lh0A_ok%=:\n"
"s_and_b32 s2, s55, 1\n"
"v_mfma_f32_32x32x16_bf16 v[160:175], a[20:23], a[184:187], v[160:175]\n"
"s_waitcnt lgkmcnt(2)\n"
"ds_read_b128 a[172:175], v10 offset:53248\n"
"s_mul_i32 s2, s2, -4\n"
"s_add_u32 s2, s2, 2\n"
"s_add_u32 s2, s2, s64\n"
"s_lshl_b32 s2, s2, 12\n"
"s_add_u32 s2, s2, 81920\n"
"s_add_u32 s2, s2, s65\n"
"v_mfma_f32_32x32x16_bf16 v[176:191], a[20:23], a[188:191], v[176:191]\n"
"ds_read_b128 a[176:179], v10 offset:57344\n"
"s_add_u32 s3, s2, 4096\n"
"buffer_load_dwordx4 a[72:75], v44, s[40:43], s2 offen offset:0 sc1\n"
"v_cvt_pk_bf16_f32 v230, v96, v97\n"
"v_accvgpr_read_b32 v99, a247\n"
"v_mfma_f32_32x32x16_bf16 v[112:127], a[24:27], a[160:163], v[112:127]\n"
"s_waitcnt lgkmcnt(2)\n"
"ds_read_b128 a[180:183], v11 offset:40960\n"
"v_lshlrev_b32 v98, 16, v99\n"
"v_and_b32 v99, s100, v99\n"
"v_fma_f32 v98, v98, v5, -v6\n"
"v_fma_f32 v99, v99, v5, -v6\n"
"v_cvt_pk_bf16_f32 v231, v98, v99\n"
"v_mfma_f32_32x32x16_bf16 v[128:143], a[24:27], a[164:167], v[128:143]\n"
"ds_read_b128 a[184:187], v11 offset:45056\n"
"buffer_load_dwordx4 a[76:79], v44, s[40:43], s2 offen offset:1024 sc1\n"
"v_accvgpr_read_b32 v97, a248\n"
"v_lshlrev_b32 v96, 16, v97\n"
"v_and_b32 v97, s100, v97\n"
"v_mfma_f32_32x32x16_bf16 v[144:159], a[24:27], a[168:171], v[144:159]\n"
"s_waitcnt lgkmcnt(2)\n"
"ds_read_b128 a[188:191], v11 offset:49152\n"
"v_fma_f32 v96, v96, v5, -v6\n"
"v_fma_f32 v97, v97, v5, -v6\n"
"v_cvt_pk_bf16_f32 v232, v96, v97\n"
"v_accvgpr_read_b32 v99, a249\n"
"v_mfma_f32_32x32x16_bf16 v[160:175], a[24:27], a[172:175], v[160:175]\n"
"ds_read_b128 a[160:163], v11 offset:53248\n"
"buffer_load_dwordx4 a[80:83], v44, s[40:43], s2 offen offset:2048 sc1\n"
"v_lshlrev_b32 v98, 16, v99\n"
"v_and_b32 v99, s100, v99\n"
"v_fma_f32 v98, v98, v5, -v6\n"
"v_mfma_f32_32x32x16_bf16 v[176:191], a[24:27], a[176:179], v[176:191]\n"
"s_waitcnt lgkmcnt(2)\n"
"ds_read_b128 a[164:167], v11 offset:57344\n"
"v_fma_f32 v99, v99, v5, -v6\n"
"v_cvt_pk_bf16_f32 v233, v98, v99\n"
"v_accvgpr_read_b32 v97, a250\n"
"v_lshlrev_b32 v96, 16, v97\n"
"v_and_b32 v97, s100, v97\n"
"v_mfma_f32_32x32x16_bf16 v[112:127], a[28:31], a[180:183], v[112:127]\n"
"ds_read_b128 a[168:171], v12 offset:40960\n"
"buffer_load_dwordx4 a[84:87], v44, s[40:43], s2 offen offset:3072 sc1\n"
"s_lshl_b32 s7, 1, s55\n"
"s_andn2_b32 s7, 15, s7\n"
"v_mov_b32 v3, 0\n"
"v_mfma_f32_32x32x16_bf16 v[128:143], a[28:31], a[184:187], v[128:143]\n"
"s_waitcnt lgkmcnt(2)\n"
"ds_read_b128 a[172:175], v12 offset:45056\n"
"buffer_atomic_add v3, v1, s[40:43], s4 offen offset:256 sc0 sc1\n"
"v_fma_f32 v96, v96, v5, -v6\n"
"v_fma_f32 v97, v97, v5, -v6\n"
"v_cvt_pk_bf16_f32 v234, v96, v97\n"
"v_mfma_f32_32x32x16_bf16 v[144:159], a[28:31], a[188:191], v[144:159]\n"
"ds_read_b128 a[176:179], v12 offset:49152\n"
"buffer_load_dwordx4 v42, s[28:31], s66 offen offset:3072 lds\n"
"s_add_u32 m0, s59, 86016\n"
"v_accvgpr_read_b32 v99, a251\n"
"v_lshlrev_b32 v98, 16, v99\n"
"v_mfma_f32_32x32x16_bf16 v[160:175], a[28:31], a[160:163], v[160:175]\n"
"s_waitcnt lgkmcnt(2)\n"
"ds_read_b128 a[180:183], v12 offset:53248\n"
"v_and_b32 v99, s100, v99\n"
"v_fma_f32 v98, v98, v5, -v6\n"
"v_fma_f32 v99, v99, v5, -v6\n"
"v_cvt_pk_bf16_f32 v235, v98, v99\n"
"v_mfma_f32_32x32x16_bf16 v[176:191], a[28:31], a[164:167], v[176:191]\n"
"ds_read_b128 a[184:187], v12 offset:57344\n"
"buffer_load_dwordx4 v43, s[28:31], s66 offen lds\n"
"s_mul_i32 s66, s54, 6\n"
"s_add_u32 s66, s66, 4\n"
"s_mul_i32 s66, s66, 81920\n"
"s_add_u32 m0, s59, 122880\n"
"v_mfma_f32_32x32x16_bf16 v[112:127], a[32:35], a[168:171], v[112:127]\n"
"s_waitcnt lgkmcnt(2)\n"
"ds_read_b128 a[188:191], v13 offset:40960\n"
"v_accvgpr_read_b32 v97, a252\n"
"v_lshlrev_b32 v96, 16, v97\n"
"v_and_b32 v97, s100, v97\n"
"v_fma_f32 v96, v96, v5, -v6\n"
"v_fma_f32 v97, v97, v5, -v6\n"
"v_mfma_f32_32x32x16_bf16 v[128:143], a[32:35], a[172:175], v[128:143]\n"
"ds_read_b128 a[160:163], v13 offset:45056\n"
"v_cvt_pk_bf16_f32 v236, v96, v97\n"
"v_accvgpr_read_b32 v99, a253\n"
"v_lshlrev_b32 v98, 16, v99\n"
"v_and_b32 v99, s100, v99\n"
"v_fma_f32 v98, v98, v5, -v6\n"
"v_mfma_f32_32x32x16_bf16 v[144:159], a[32:35], a[176:179], v[144:159]\n"
"s_waitcnt lgkmcnt(2)\n"
"ds_read_b128 a[164:167], v13 offset:49152\n"
"buffer_load_dwordx4 v39, s[28:31], s66 offen offset:0 lds\n"
"v_fma_f32 v99, v99, v5, -v6\n"
"v_cvt_pk_bf16_f32 v237, v98, v99\n"
"v_accvgpr_read_b32 v97, a254\n"
"v_mfma_f32_32x32x16_bf16 v[160:175], a[32:35], a[180:183], v[160:175]\n"
"ds_read_b128 a[168:171], v13 offset:53248\n"
"v_lshlrev_b32 v96, 16, v97\n"
"v_and_b32 v97, s100, v97\n"
"v_fma_f32 v96, v96, v5, -v6\n"
"v_fma_f32 v97, v97, v5, -v6\n"
"v_cvt_pk_bf16_f32 v238, v96, v97\n"
"v_mfma_f32_32x32x16_bf16 v[176:191], a[32:35], a[184:187], v[176:191]\n"
"s_barrier\n"
"s_waitcnt lgkmcnt(2)\n"
"ds_read_b128 a[172:175], v13 offset:57344\n"
"buffer_load_dwordx4 v40, s[28:31], s66 offen offset:1024 lds\n"
"v_accvgpr_read_b32 v99, a255\n"
"v_mfma_f32_32x32x16_bf16 v[112:127], a[36:39], a[188:191], v[112:127]\n"
"ds_read_b128 a[176:179], v14 offset:4096\n"
"v_lshlrev_b32 v98, 16, v99\n"
"v_and_b32 v99, s100, v99\n"
"v_fma_f32 v98, v98, v5, -v6\n"
"v_fma_f32 v99, v99, v5, -v6\n"
"v_cvt_pk_bf16_f32 v239, v98, v99\n"
"v_mfma_f32_32x32x16_bf16 v[128:143], a[36:39], a[160:163], v[128:143]\n"
"s_waitcnt lgkmcnt(2)\n"
"ds_read_b128 a[180:183], v14 offset:8192\n"
"buffer_load_dwordx4 v41, s[28:31], s66 offen offset:2048 lds\n"
"v_cmp_eq_u32 vcc, 0, v93\n"
"v_lshlrev_b32 v90, v92, v90\n"
"v_lshlrev_b32 v91, v92, v91\n"
"v_mfma_f32_32x32x16_bf16 v[144:159], a[36:39], a[164:167], v[144:159]\n"
"ds_read_b128 a[184:187], v14 offset:12288\n"
"v_cmp_eq_u32 s[78:79], 1, v93\n"
"v_cndmask_b32 v50, 0, v94, vcc\n"
"v_cndmask_b32 v58, 0, v90, vcc\n"
"v_cndmask_b32 v66, 0, v91, vcc\n"
"v_cmp_eq_u32 vcc, 2, v93\n"
"v_mfma_f32_32x32x16_bf16 v[160:175], a[36:39], a[168:171], v[160:175]\n"
"s_waitcnt lgkmcnt(2)\n"
"ds_read_b128 a[188:191], v14 offset:16384\n"
"buffer_load_dwordx4 v42, s[28:31], s66 offen offset:3072 lds\n"
"s_add_u32 m0, s59, 126976\n"
"v_cndmask_b32 v51, 0, v94, s[78:79]\n"
"v_cndmask_b32 v59, 0, v90, s[78:79]\n"
"v_mfma_f32_32x32x16_bf16 v[176:191], a[36:39], a[172:175], v[176:191]\n"
"ds_read_b128 a[160:163], v15 offset:4096\n"
"v_cndmask_b32 v67, 0, v91, s[78:79]\n"
"v_cmp_eq_u32 s[78:79], 3, v93\n"
"v_cndmask_b32 v52, 0, v94, vcc\n"
"v_cndmask_b32 v60, 0, v90, vcc\n"
"v_cndmask_b32 v68, 0, v91, vcc\n"
"v_mfma_f32_32x32x16_bf16 v[128:143], a[40:43], a[176:179], v[128:143]\n"
"s_waitcnt lgkmcnt(2)\n"
"ds_read_b128 a[164:167], v15 offset:8192\n"
"v_cmp_eq_u32 vcc, 8, v93\n"
"v_cndmask_b32 v53, 0, v94, s[78:79]\n"
"v_cndmask_b32 v61, 0, v90, s[78:79]\n"
"v_cndmask_b32 v69, 0, v91, s[78:79]\n"
"v_mfma_f32_32x32x16_bf16 v[144:159], a[40:43], a[180:183], v[144:159]\n"
"ds_read_b128 a[168:171], v15 offset:12288\n"
"buffer_load_dwordx4 v43, s[28:31], s66 offen lds\n"
"s_mul_i32 s66, s54, 6\n"
"s_add_u32 s66, s66, 5\n"
"s_mul_i32 s66, s66, 81920\n"
"s_add_u32 m0, s59, 0\n"
"v_mfma_f32_32x32x16_bf16 v[160:175], a[40:43], a[184:187], v[160:175]\n"
"s_waitcnt lgkmcnt(2)\n"
"ds_read_b128 a[172:175], v15 offset:16384\n"
"s_waitcnt vmcnt(7)\n"
"v_cmp_ne_u32 s[0:1], s56, v3\n"
"s_nop 0\n"
"s_and_b32 s0, s0, s7\n"
"s_cbranch_scc1 Lh2A_slow%=\n"
"Lh2A_ok%=:\n"
"s_mul_i32 s2, s54, 6\n"
"s_add_u32 s2, s2, 4\n"
"s_lshl_b32 s2, s2, 12\n"
"v_mfma_f32_32x32x16_bf16 v[176:191], a[40:43], a[188:191], v[176:191]\n"
"ds_read_b128 a[176:179], v16 offset:4096\n"
"s_add_u32 s2, s2, 81920\n"
"s_add_u32 s2, s2, s65\n"
"s_add_u32 s3, s2, 4096\n"
"buffer_load_dwordx4 a[88:91], v44, s[40:43], s2 offen offset:0 sc1\n"
"v_mfma_f32_32x32x16_bf16 v[128:143], a[44:47], a[160:163], v[128:143]\n"
"s_waitcnt lgkmcnt(2)\n"
"ds_read_b128 a[180:183], v16 offset:8192\n"
"buffer_load_dwordx4 a[92:95], v44, s[40:43], s2 offen offset:1024 sc1\n"
"v_cmp_eq_u32 s[78:79], 9, v93\n"
"v_cndmask_b32 v54, 0, v94, vcc\n"
"v_cndmask_b32 v62, 0, v90, vcc\n"
"v_mfma_f32_32x32x16_bf16 v[144:159], a[44:47], a[164:167], v[144:159]\n"
"ds_read_b128 a[184:187], v16 offset:12288\n"
"v_cndmask_b32 v70, 0, v91, vcc\n"
"v_cmp_eq_u32 vcc, 10, v93\n"
"v_cndmask_b32 v55, 0, v94, s[78:79]\n"
"v_cndmask_b32 v63, 0, v90, s[78:79]\n"
"v_cndmask_b32 v71, 0, v91, s[78:79]\n"
"v_mfma_f32_32x32x16_bf16 v[160:175], a[44:47], a[168:171], v[160:175]\n"
"s_waitcnt lgkmcnt(2)\n"
"ds_read_b128 a[188:191], v16 offset:16384\n"
"buffer_load_dwordx4 a[96:99], v44, s[40:43], s2 offen offset:2048 sc1\n"
"v_cmp_eq_u32 s[78:79], 11, v93\n"
"v_cndmask_b32 v56, 0, v94, vcc\n"
"v_cndmask_b32 v64, 0, v90, vcc\n"
"v_mfma_f32_32x32x16_bf16 v[176:191], a[44:47], a[172:175], v[176:191]\n"
"ds_read_b128 a[160:163], v17 offset:4096\n"
"v_cndmask_b32 v72, 0, v91, vcc\n"
"v_cndmask_b32 v57, 0, v94, s[78:79]\n"
"v_cndmask_b32 v65, 0, v90, s[78:79]\n"
"v_cndmask_b32 v73, 0, v91, s[78:79]\n"
"v_mfma_f32_32x32x16_bf16 v[128:143], a[48:51], a[176:179], v[128:143]\n"
"s_waitcnt lgkmcnt(2)\n"
"ds_read_b128 a[164:167], v17 offset:8192\n"
"buffer_load_dwordx4 v39, s[28:31], s66 offen offset:0 lds\n"
"v_mfma_f32_32x32x16_bf16 v[144:159], a[48:51], a[180:183], v[144:159]\n"
"ds_read_b128 a[168:171], v17 offset:12288\n"
"v_mfma_f32_32x32x16_bf16 v[160:175], a[48:51], a[184:187], v[160:175]\n"
"s_barrier\n"
"s_waitcnt lgkmcnt(2)\n"
"ds_read_b128 a[172:175], v17 offset:16384\n"
"buffer_load_dwordx4 v40, s[28:31], s66 offen offset:1024 lds\n"
"v_mfma_f32_32x32x16_bf16 v[176:191], a[48:51], a[188:191], v[176:191]\n"
"ds_read_b128 a[176:179], v14 offset:45056\n"
"v_mfma_f32_32x32x16_bf16 v[128:143], a[52:55], a[160:163], v[128:143]\n"
"s_waitcnt lgkmcnt(2)\n"
"ds_read_b128 a[180:183], v14 offset:49152\n"
"buffer_load_dwordx4 v41, s[28:31], s66 offen offset:2048 lds\n"
"v_mfma_f32_32x32x16_bf16 v[144:159], a[52:55], a[164:167], v[144:159]\n"
"ds_read_b128 a[184:187], v14 offset:53248\n"
"v_mfma_f32_32x32x16_bf16 v[160:175], a[52:55], a[168:171], v[160:175]\n"
"s_waitcnt lgkmcnt(2)\n"
"ds_read_b128 a[188:191], v14 offset:57344\n"
"buffer_load_dwordx4 v42, s[28:31], s66 offen offset:3072 lds\n"
"s_add_u32 m0, s59, 4096\n"
"v_mfma_f32_32x32x16_bf16 v[176:191], a[52:55], a[172:175], v[176:191]\n"
"ds_read_b128 a[160:163], v15 offset:45056\n"
"v_mfma_f32_32x32x16_bf16 v[128:143], a[56:59], a[176:179], v[128:143]\n"
"s_waitcnt lgkmcnt(2)\n"
"ds_read_b128 a[164:167], v15 offset:49152\n"
"buffer_load_dwordx4 v43, s[28:31], s66 offen lds\n"
"s_and_b32 s66, s55, 1\n"
"s_mul_i32 s66, s66, -4\n"
"s_add_u32 s66, s66, 3\n"
"s_add_u32 s66, s66, s64\n"
"v_mfma_f32_32x32x16_bf16 v[144:159], a[56:59], a[180:183], v[144:159]\n"
"ds_read_b128 a[168:171], v15 offset:53248\n"
"s_mul_i32 s66, s66, 81920\n"
"s_add_u32 m0, s59, 20480\n"
"v_mfma_f32_32x32x16_bf16 v[160:175], a[56:59], a[184:187], v[160:175]\n"
"s_waitcnt lgkmcnt(2)\n"
"ds_read_b128 a[172:175], v15 offset:57344\n"
"buffer_load_dwordx4 v39, s[28:31], s66 offen offset:0 lds\n"
"v_mfma_f32_32x32x16_bf16 v[176:191], a[56:59], a[188:191], v[176:191]\n"
"ds_read_b128 a[176:179], v16 offset:45056\n"
"v_mfma_f32_32x32x16_bf16 v[128:143], a[60:63], a[160:163], v[128:143]\n"
"s_waitcnt lgkmcnt(2)\n"
"ds_read_b128 a[180:183], v16 offset:49152\n"
"buffer_load_dwordx4 v40, s[28:31], s66 offen offset:1024 lds\n"
"v_mfma_f32_32x32x16_bf16 v[144:159], a[60:63], a[164:167], v[144:159]\n"
"ds_read_b128 a[184:187], v16 offset:53248\n"
"v_mfma_f32_32x32x16_bf16 v[160:175], a[60:63], a[168:171], v[160:175]\n"
"s_waitcnt lgkmcnt(2)\n"
"ds_read_b128 a[188:191], v16 offset:57344\n"
"buffer_load_dwordx4 v41, s[28:31], s66 offen offset:2048 lds\n"
"v_mfma_f32_32x32x16_bf16 v[176:191], a[60:63], a[172:175], v[176:191]\n"
"ds_read_b128 a[160:163], v17 offset:45056\n"
"v_mfma_f32_32x32x16_bf16 v[128:143], a[64:67], a[176:179], v[128:143]\n"
"s_waitcnt lgkmcnt(2)\n"
"ds_read_b128 a[164:167], v17 offset:49152\n"
"buffer_load_dwordx4 v42, s[28:31], s66 offen offset:3072 lds\n"
"s_add_u32 m0, s59, 24576\n"
"v_mfma_f32_32x32x16_bf16 v[144:159], a[64:67], a[180:183], v[144:159]\n"
"ds_read_b128 a[168:171], v17 offset:53248\n"
"v_mfma_f32_32x32x16_bf16 v[160:175], a[64:67], a[184:187], v[160:175]\n"
"s_waitcnt vmcnt(17)\n"
"s_barrier\n"
"s_waitcnt lgkmcnt(2)\n"
"ds_read_b128 a[172:175], v17 offset:57344\n"
"buffer_load_dwordx4 v43, s[28:31], s66 offen lds\n"
"v_mfma_f32_32x32x16_bf16 v[176:191], a[64:67], a[188:191], v[176:191]\n"
"ds_read_b64_tr_b16 a[176:177], v27 offset:20544\n"
"ds_read_b64_tr_b16 a[178:179], v27 offset:21824\n"
"v_mfma_f32_32x32x16_bf16 v[128:143], a[68:71], a[160:163], v[128:143]\n"
"s_waitcnt lgkmcnt(3)\n"
"ds_read_b64_tr_b16 a[180:181], v27 offset:20608\n"
"ds_read_b64_tr_b16 a[182:183], v27 offset:21888\n"
"v_mfma_f32_32x32x16_bf16 v[144:159], a[68:71], a[164:167], v[144:159]\n"
"ds_read_b64_tr_b16 a[184:185], v27 offset:20672\n"
"ds_read_b64_tr_b16 a[186:187], v27 offset:21952\n"
"v_mfma_f32_32x32x16_bf16 v[160:175], a[68:71], a[168:171], v[160:175]\n"
"s_waitcnt lgkmcnt(4)\n"
"ds_read_b64_tr_b16 a[188:189], v27 offset:20736\n"
"ds_read_b64_tr_b16 a[190:191], v27 offset:22016\n"
"buffer_load_dwordx4 a[100:103], v44, s[40:43], s2 offen offset:3072 sc1\n"
"s_mul_i32 s2, s54, 6\n"
"s_add_u32 s2, s2, 5\n"
"s_lshl_b32 s2, s2, 12\n"
"v_mfma_f32_32x32x16_bf16 v[176:191], a[68:71], a[172:175], v[176:191]\n"
"ds_read_b64_tr_b16 a[160:161], v27 offset:25664\n"
"ds_read_b64_tr_b16 a[162:163], v27 offset:26944\n"
"s_add_u32 s2, s2, 81920\n"
"s_add_u32 s2, s2, s65\n"
"s_add_u32 s3, s2, 4096\n"
"v_mfma_f32_32x32x16_bf16 v[128:143], a[72:75], a[176:179], v[128:143]\n"
"s_waitcnt lgkmcnt(4)\n"
"ds_read_b64_tr_b16 a[164:165], v27 offset:25728\n"
"ds_read_b64_tr_b16 a[166:167], v27 offset:27008\n"
"buffer_load_dwordx4 a[104:107], v44, s[40:43], s2 offen offset:0 sc1\n"
"v_mfma_f32_32x32x16_bf16 v[144:159], a[72:75], a[180:183], v[144:159]\n"
"ds_read_b64_tr_b16 a[168:169], v27 offset:25792\n"
"ds_read_b64_tr_b16 a[170:171], v27 offset:27072\n"
"v_mfma_f32_32x32x16_bf16 v[160:175], a[72:75], a[184:187], v[160:175]\n"
"s_waitcnt lgkmcnt(4)\n"
"ds_read_b64_tr_b16 a[172:173], v27 offset:25856\n"
"ds_read_b64_tr_b16 a[174:175], v27 offset:27136\n"
"buffer_load_dwordx4 a[108:111], v44, s[40:43], s2 offen offset:1024 sc1\n"
"v_mfma_f32_32x32x16_bf16 v[176:191], a[72:75], a[188:191], v[176:191]\n"
"ds_read_b64_tr_b16 a[176:177], v27 offset:30784\n"
"ds_read_b64_tr_b16 a[178:179], v27 offset:32064\n"
"buffer_load_dwordx4 a[112:115], v44, s[40:43], s2 offen offset:2048 sc1\n"
"v_mfma_f32_32x32x16_bf16 v[128:143], a[76:79], a[160:163], v[128:143]\n"
"s_waitcnt lgkmcnt(4)\n"
"ds_read_b64_tr_b16 a[180:181], v27 offset:30848\n"
"ds_read_b64_tr_b16 a[182:183], v27 offset:32128\n"
"v_mfma_f32_32x32x16_bf16 v[144:159], a[76:79], a[164:167], v[144:159]\n"
"ds_read_b64_tr_b16 a[184:185], v27 offset:30912\n"
"ds_read_b64_tr_b16 a[186:187], v27 offset:32192\n"
"buffer_load_dwordx4 a[116:119], v44, s[40:43], s2 offen offset:3072 sc1\n"
"s_and_b32 s2, s55, 1\n"
"s_mul_i32 s2, s2, -4\n"
"s_add_u32 s2, s2, 3\n"
"v_mfma_f32_32x32x16_bf16 v[160:175], a[76:79], a[168:171], v[160:175]\n"
"s_waitcnt lgkmcnt(4)\n"
"ds_read_b64_tr_b16 a[188:189], v27 offset:30976\n"
"ds_read_b64_tr_b16 a[190:191], v27 offset:32256\n"
"s_add_u32 s2, s2, s64\n"
"s_lshl_b32 s2, s2, 12\n"
"s_add_u32 s2, s2, 81920\n"
"s_add_u32 s2, s2, s65\n"
"s_add_u32 s3, s2, 4096\n"
"v_mfma_f32_32x32x16_bf16 v[176:191], a[76:79], a[172:175], v[176:191]\n"
"ds_read_b64_tr_b16 a[160:161], v27 offset:35904\n"
"ds_read_b64_tr_b16 a[162:163], v27 offset:37184\n"
"buffer_load_dwordx4 a[120:123], v44, s[40:43], s2 offen offset:0 sc1\n"
"v_mfma_f32_32x32x16_bf16 v[128:143], a[80:83], a[176:179], v[128:143]\n"
"s_waitcnt lgkmcnt(4)\n"
"ds_read_b64_tr_b16 a[164:165], v27 offset:35968\n"
"ds_read_b64_tr_b16 a[166:167], v27 offset:37248\n"
"v_mfma_f32_32x32x16_bf16 v[144:159], a[80:83], a[180:183], v[144:159]\n"
"ds_read_b64_tr_b16 a[168:169], v27 offset:36032\n"
"ds_read_b64_tr_b16 a[170:171], v27 offset:37312\n"
"buffer_load_dwordx4 a[124:127], v44, s[40:43], s2 offen offset:1024 sc1\n"
"v_mfma_f32_32x32x16_bf16 v[160:175], a[80:83], a[184:187], v[160:175]\n"
"s_waitcnt vmcnt(20)\n"
"s_barrier\n"
"s_waitcnt lgkmcnt(4)\n"
"ds_read_b64_tr_b16 a[172:173], v27 offset:36096\n"
"ds_read_b64_tr_b16 a[174:175], v27 offset:37376\n"
"v_mfma_f32_32x32x16_bf16 v[176:191], a[80:83], a[188:191], v[176:191]\n"
"ds_read_b64_tr_b16 a[176:177], v80 offset:64\n"
"ds_read_b64_tr_b16 a[178:179], v80 offset:1344\n"
"buffer_load_dwordx4 a[128:131], v44, s[40:43], s2 offen offset:2048 sc1\n"
"v_mfma_f32_32x32x16_bf16 v[128:143], a[84:87], a[160:163], v[128:143]\n"
"s_waitcnt lgkmcnt(4)\n"
"ds_read_b64_tr_b16 a[180:181], v80 offset:128\n"
"ds_read_b64_tr_b16 a[182:183], v80 offset:1408\n"
"v_mfma_f32_32x32x16_bf16 v[144:159], a[84:87], a[164:167], v[144:159]\n"
"ds_read_b64_tr_b16 a[184:185], v80 offset:192\n"
"ds_read_b64_tr_b16 a[186:187], v80 offset:1472\n"
"buffer_load_dwordx4 a[132:135], v44, s[40:43], s2 offen offset:3072 sc1\n"
"v_cmp_eq_u32 s[0:1], s54, v2\n"
"v_mov_b32 v4, 0x70000000\n"
"v_mfma_f32_32x32x16_bf16 v[160:175], a[84:87], a[168:171], v[160:175]\n"
"s_waitcnt vmcnt(21) lgkmcnt(4)\n"
"ds_read_b64_tr_b16 a[188:189], v80 offset:256\n"
"ds_read_b64_tr_b16 a[190:191], v80 offset:1536\n"
"s_lshl_b32 s7, 1, s54\n"
"s_andn2_b32 s7, 15, s7\n"
"v_cndmask_b32 v4, v4, v1, s[0:1]\n"
"v_mfma_f32_32x32x16_bf16 v[176:191], a[84:87], a[172:175], v[176:191]\n"
"ds_read_b64_tr_b16 a[160:161], v80 offset:5184\n"
"ds_read_b64_tr_b16 a[162:163], v80 offset:6464\n"
"v_mfma_f32_32x32x16_bf16 v[128:143], a[88:91], a[176:179], v[128:143]\n"
"s_waitcnt lgkmcnt(4)\n"
"ds_read_b64_tr_b16 a[164:165], v80 offset:5248\n"
"ds_read_b64_tr_b16 a[166:167], v80 offset:6528\n"
"v_mfma_f32_32x32x16_bf16 v[144:159], a[88:91], a[180:183], v[144:159]\n"
"ds_read_b64_tr_b16 a[168:169], v80 offset:5312\n"
"ds_read_b64_tr_b16 a[170:171], v80 offset:6592\n"
"v_mfma_f32_32x32x16_bf16 v[160:175], a[88:91], a[184:187], v[160:175]\n"
"s_waitcnt vmcnt(20) lgkmcnt(4)\n"
"ds_read_b64_tr_b16 a[172:173], v80 offset:5376\n"
"ds_read_b64_tr_b16 a[174:175], v80 offset:6656\n"
"v_mfma_f32_32x32x16_bf16 v[176:191], a[88:91], a[188:191], v[176:191]\n"
"ds_read_b64_tr_b16 a[176:177], v80 offset:10304\n"
"ds_read_b64_tr_b16 a[178:179], v80 offset:11584\n"
"v_mfma_f32_32x32x16_bf16 v[128:143], a[92:95], a[160:163], v[128:143]\n"
"s_waitcnt lgkmcnt(4)\n"
"ds_read_b64_tr_b16 a[180:181], v80 offset:10368\n"
"ds_read_b64_tr_b16 a[182:183], v80 offset:11648\n"
"v_mfma_f32_32x32x16_bf16 v[144:159], a[92:95], a[164:167], v[144:159]\n"
"ds_read_b64_tr_b16 a[184:185], v80 offset:10432\n"
"ds_read_b64_tr_b16 a[186:187], v80 offset:11712\n"
"v_mfma_f32_32x32x16_bf16 v[160:175], a[92:95], a[168:171], v[160:175]\n"
"s_waitcnt vmcnt(19) lgkmcnt(4)\n"
"ds_read_b64_tr_b16 a[188:189], v80 offset:10496\n"
"ds_read_b64_tr_b16 a[190:191], v80 offset:11776\n"
"v_mfma_f32_32x32x16_bf16 v[176:191], a[92:95], a[172:175], v[176:191]\n"
"ds_read_b64_tr_b16 a[160:161], v80 offset:15424\n"
"ds_read_b64_tr_b16 a[162:163], v80 offset:16704\n"
"v_mfma_f32_32x32x16_bf16 v[128:143], a[96:99], a[176:179], v[128:143]\n"
"s_waitcnt lgkmcnt(4)\n"
"ds_read_b64_tr_b16 a[164:165], v80 offset:15488\n"
"ds_read_b64_tr_b16 a[166:167], v80 offset:16768\n"
"v_mfma_f32_32x32x16_bf16 v[144:159], a[96:99], a[180:183], v[144:159]\n"
"ds_read_b64_tr_b16 a[168:169], v80 offset:15552\n"
"ds_read_b64_tr_b16 a[170:171], v80 offset:16832\n"
"v_mfma_f32_32x32x16_bf16 v[160:175], a[96:99], a[184:187], v[160:175]\n"
"s_waitcnt vmcnt(8) lgkmcnt(4)\n"
"ds_read_b64_tr_b16 a[172:173], v80 offset:15616\n"
"ds_read_b64_tr_b16 a[174:175], v80 offset:16896\n"
"v_mfma_f32_32x32x16_bf16 v[176:191], a[96:99], a[188:191], v[176:191]\n"
"v_mfma_f32_32x32x16_bf16 v[128:143], a[100:103], a[160:163], v[128:143]\n"
"s_waitcnt lgkmcnt(2)\n"
"v_mfma_f32_32x32x16_bf16 v[144:159], a[100:103], a[164:167], v[144:159]\n"
"v_mfma_f32_32x32x16_bf16 v[160:175], a[100:103], a[168:171], v[160:175]\n"
"s_barrier\n"
"s_waitcnt lgkmcnt(0)\n"
"v_mfma_f32_32x32x16_bf16 v[176:191], a[100:103], a[172:175], v[176:191]\n"
"ds_read_b64_tr_b16 a[176:177], v26 offset:256\n"
"ds_read_b64_tr_b16 a[178:179], v26 offset:1536\n"
"v_mfma_f32_32x32x16_bf16 v[176:191], a[248:251], v[50:53], v[176:191]\n"
"ds_read_b64_tr_b16 a[180:181], v26 offset:5376\n"
"ds_read_b64_tr_b16 a[182:183], v26 offset:6656\n"
"v_mfma_f32_32x32x16_bf16 v[176:191], a[252:255], v[54:57], v[176:191]\n"
"ds_read_b64_tr_b16 a[184:185], v26 offset:10496\n"
"ds_read_b64_tr_b16 a[186:187], v26 offset:11776\n"
"v_mfma_f32_32x32x16_bf16 v[176:191], v[232:235], v[66:69], v[176:191]\n"
"s_waitcnt vmcnt(7) lgkmcnt(4)\n"
"ds_read_b64_tr_b16 a[188:189], v26 offset:15616\n"
"ds_read_b64_tr_b16 a[190:191], v26 offset:16896\n"
"v_mfma_f32_32x32x16_bf16 v[176:191], v[236:239], v[70:73], v[176:191]\n"
"ds_read_b64_tr_b16 a[160:161], v26 offset:20736\n"
"ds_read_b64_tr_b16 a[162:163], v26 offset:22016\n"
"v_mfma_f32_32x32x16_bf16 v[176:191], a[104:107], a[176:179], v[176:191]\n"
"s_waitcnt vmcnt(5) lgkmcnt(4)\n"
"ds_read_b64_tr_b16 a[164:165], v26 offset:25856\n"
"ds_read_b64_tr_b16 a[166:167], v26 offset:27136\n"
"v_mfma_f32_32x32x16_bf16 v[176:191], a[108:111], a[180:183], v[176:191]\n"
"ds_read_b64_tr_b16 a[168:169], v26 offset:30976\n"
"ds_read_b64_tr_b16 a[170:171], v26 offset:32256\n"
"v_mfma_f32_32x32x16_bf16 v[176:191], a[112:115], a[184:187], v[176:191]\n"
"s_waitcnt vmcnt(3) lgkmcnt(4)\n"
"ds_read_b64_tr_b16 a[172:173], v26 offset:36096\n"
"ds_read_b64_tr_b16 a[174:175], v26 offset:37376\n"
"v_mfma_f32_32x32x16_bf16 v[176:191], a[116:119], a[188:191], v[176:191]\n"
"ds_read_b64_tr_b16 a[176:177], v26 offset:64\n"
"ds_read_b64_tr_b16 a[178:179], v26 offset:1344\n"
"v_mfma_f32_32x32x16_bf16 v[176:191], a[120:123], a[160:163], v[176:191]\n"
"s_waitcnt vmcnt(1) lgkmcnt(4)\n"
"ds_read_b64_tr_b16 a[180:181], v26 offset:5184\n"
"ds_read_b64_tr_b16 a[182:183], v26 offset:6464\n"
"v_mfma_f32_32x32x16_bf16 v[176:191], a[124:127], a[164:167], v[176:191]\n"
"ds_read_b64_tr_b16 a[184:185], v26 offset:10304\n"
"ds_read_b64_tr_b16 a[186:187], v26 offset:11584\n"
"v_mfma_f32_32x32x16_bf16 v[176:191], a[128:131], a[168:171], v[176:191]\n"
"s_waitcnt vmcnt(0) lgkmcnt(4)\n"
"ds_read_b64_tr_b16 a[188:189], v26 offset:15424\n"
"ds_read_b64_tr_b16 a[190:191], v26 offset:16704\n"
"v_mfma_f32_32x32x16_bf16 v[176:191], a[132:135], a[172:175], v[176:191]\n"
"ds_read_b64_tr_b16 a[160:161], v26 offset:20544\n"
"ds_read_b64_tr_b16 a[162:163], v26 offset:21824\n"
"v_mfma_f32_32x32x16_bf16 v[128:143], a[104:107], a[176:179], v[128:143]\n"
"s_waitcnt lgkmcnt(4)\n"
"ds_read_b64_tr_b16 a[164:165], v26 offset:25664\n"
"ds_read_b64_tr_b16 a[166:167], v26 offset:26944\n"
"v_mfma_f32_32x32x16_bf16 v[128:143], a[108:111], a[180:183], v[128:143]\n"
"ds_read_b64_tr_b16 a[168:169], v26 offset:30784\n"
"ds_read_b64_tr_b16 a[170:171], v26 offset:32064\n"
"s_nop 2\n"
"v_cvt_pk_bf16_f32 v240, v176, v177\n"
"v_cvt_pk_bf16_f32 v241, v178, v179\n"
"v_cvt_pk_bf16_f32 v242, v180, v181\n"
"v_cvt_pk_bf16_f32 v243, v182, v183\n"
"v_mfma_f32_32x32x16_bf16 v[128:143], a[112:115], a[184:187], v[128:143]\n"
"s_waitcnt lgkmcnt(4)\n"
"ds_read_b64_tr_b16 a[172:173], v26 offset:35904\n"
"ds_read_b64_tr_b16 a[174:175], v26 offset:37184\n"
"buffer_store_dwordx4 v[240:243], v44, s[40:43], s15 offen offset:0 sc1\n"
"v_cvt_pk_bf16_f32 v244, v184, v185\n"
"v_cvt_pk_bf16_f32 v245, v186, v187\n"
"v_mfma_f32_32x32x16_bf16 v[128:143], a[116:119], a[188:191], v[128:143]\n"
"ds_read_b64_tr_b16 a[176:177], v26 offset:128\n"
"ds_read_b64_tr_b16 a[178:179], v26 offset:1408\n"
"v_cvt_pk_bf16_f32 v246, v188, v189\n"
"v_cvt_pk_bf16_f32 v247, v190, v191\n"
"v_mfma_f32_32x32x16_bf16 v[128:143], a[120:123], a[160:163], v[128:143]\n"
"s_waitcnt lgkmcnt(4)\n"
"ds_read_b64_tr_b16 a[180:181], v26 offset:5248\n"
"ds_read_b64_tr_b16 a[182:183], v26 offset:6528\n"
"v_mfma_f32_32x32x16_bf16 v[128:143], a[124:127], a[164:167], v[128:143]\n"
"ds_read_b64_tr_b16 a[184:185], v26 offset:10368\n"
"ds_read_b64_tr_b16 a[186:187], v26 offset:11648\n"
"buffer_store_dwordx4 v[244:247], v44, s[40:43], s15 offen offset:1024 sc1\n"
"v_mfma_f32_32x32x16_bf16 v[128:143], a[128:131], a[168:171], v[128:143]\n"
"s_waitcnt lgkmcnt(4)\n"
"ds_read_b64_tr_b16 a[188:189], v26 offset:15488\n"
"ds_read_b64_tr_b16 a[190:191], v26 offset:16768\n"
"v_mfma_f32_32x32x16_bf16 v[128:143], a[132:135], a[172:175], v[128:143]\n"
"ds_read_b64_tr_b16 a[160:161], v26 offset:20608\n"
"ds_read_b64_tr_b16 a[162:163], v26 offset:21888\n"
"v_mfma_f32_32x32x16_bf16 v[144:159], a[104:107], a[176:179], v[144:159]\n"
"s_waitcnt lgkmcnt(4)\n"
"ds_read_b64_tr_b16 a[164:165], v26 offset:25728\n"
"ds_read_b64_tr_b16 a[166:167], v26 offset:27008\n"
"v_mfma_f32_32x32x16_bf16 v[144:159], a[108:111], a[180:183], v[144:159]\n"
"ds_read_b64_tr_b16 a[168:169], v26 offset:30848\n"
"ds_read_b64_tr_b16 a[170:171], v26 offset:32128\n"
"s_nop 2\n"
"v_cvt_pk_bf16_f32 v128, v128, v129\n"
"v_cvt_pk_bf16_f32 v129, v130, v131\n"
"v_cvt_pk_bf16_f32 v130, v132, v133\n"
"v_cvt_pk_bf16_f32 v131, v134, v135\n"
"v_mfma_f32_32x32x16_bf16 v[144:159], a[112:115], a[184:187], v[144:159]\n"
"s_waitcnt lgkmcnt(4)\n"
"ds_read_b64_tr_b16 a[172:173], v26 offset:35968\n"
"ds_read_b64_tr_b16 a[174:175], v26 offset:37248\n"
"buffer_store_dwordx4 v[128:131], v44, s[40:43], s12 offen offset:0 sc1\n"
"v_cvt_pk_bf16_f32 v132, v136, v137\n"
"v_cvt_pk_bf16_f32 v133, v138, v139\n"
"v_mfma_f32_32x32x16_bf16 v[144:159], a[116:119], a[188:191], v[144:159]\n"
"ds_read_b64_tr_b16 a[176:177], v26 offset:192\n"
"ds_read_b64_tr_b16 a[178:179], v26 offset:1472\n"
"v_cvt_pk_bf16_f32 v134, v140, v141\n"
"v_cvt_pk_bf16_f32 v135, v142, v143\n"
"v_mfma_f32_32x32x16_bf16 v[144:159], a[120:123], a[160:163], v[144:159]\n"
"s_waitcnt lgkmcnt(4)\n"
"ds_read_b64_tr_b16 a[180:181], v26 offset:5312\n"
"ds_read_b64_tr_b16 a[182:183], v26 offset:6592\n"
"v_mfma_f32_32x32x16_bf16 v[144:159], a[124:127], a[164:167], v[144:159]\n"
"ds_read_b64_tr_b16 a[184:185], v26 offset:10432\n"
"ds_read_b64_tr_b16 a[186:187], v26 offset:11712\n"
"buffer_store_dwordx4 v[132:135], v44, s[40:43], s12 offen offset:1024 sc1\n"
"v_mfma_f32_32x32x16_bf16 v[144:159], a[128:131], a[168:171], v[144:159]\n"
"s_waitcnt lgkmcnt(4)\n"
"ds_read_b64_tr_b16 a[188:189], v26 offset:15552\n"
"ds_read_b64_tr_b16 a[190:191], v26 offset:16832\n"
"v_mfma_f32_32x32x16_bf16 v[144:159], a[132:135], a[172:175], v[144:159]\n"
"ds_read_b64_tr_b16 a[160:161], v26 offset:20672\n"
"ds_read_b64_tr_b16 a[162:163], v26 offset:21952\n"
"v_mfma_f32_32x32x16_bf16 v[160:175], a[104:107], a[176:179], v[160:175]\n"
"s_waitcnt lgkmcnt(4)\n"
"ds_read_b64_tr_b16 a[164:165], v26 offset:25792\n"
"ds_read_b64_tr_b16 a[166:167], v26 offset:27072\n"
"v_mfma_f32_32x32x16_bf16 v[160:175], a[108:111], a[180:183], v[160:175]\n"
"ds_read_b64_tr_b16 a[168:169], v26 offset:30912\n"
"ds_read_b64_tr_b16 a[170:171], v26 offset:32192\n"
"s_nop 2\n"
"v_cvt_pk_bf16_f32 v144, v144, v145\n"
"v_cvt_pk_bf16_f32 v145, v146, v147\n"
"v_cvt_pk_bf16_f32 v146, v148, v149\n"
"v_cvt_pk_bf16_f32 v147, v150, v151\n"
"v_mfma_f32_32x32x16_bf16 v[160:175], a[112:115], a[184:187], v[160:175]\n"
"s_waitcnt lgkmcnt(4)\n"
"ds_read_b64_tr_b16 a[172:173], v26 offset:36032\n"
"ds_read_b64_tr_b16 a[174:175], v26 offset:37312\n"
"buffer_store_dwordx4 v[144:147], v44, s[40:43], s13 offen offset:0 sc1\n"
"v_cvt_pk_bf16_f32 v148, v152, v153\n"
"v_cvt_pk_bf16_f32 v149, v154, v155\n"
"v_mfma_f32_32x32x16_bf16 v[160:175], a[116:119], a[188:191], v[160:175]\n"
"ds_read_b128 a[176:179], v14 offset:0\n"
"v_cvt_pk_bf16_f32 v150, v156, v157\n"
"v_cvt_pk_bf16_f32 v151, v158, v159\n"
"v_mfma_f32_32x32x16_bf16 v[160:175], a[120:123], a[160:163], v[160:175]\n"
"s_waitcnt lgkmcnt(3)\n"
"ds_read_b128 a[180:183], v15 offset:0\n"
"v_mfma_f32_32x32x16_bf16 v[160:175], a[124:127], a[164:167], v[160:175]\n"
"ds_read_b128 a[184:187], v16 offset:0\n"
"buffer_store_dwordx4 v[148:151], v44, s[40:43], s13 offen offset:1024 sc1\n"
"v_mfma_f32_32x32x16_bf16 v[160:175], a[128:131], a[168:171], v[160:175]\n"
"s_waitcnt lgkmcnt(2)\n"
"ds_read_b128 a[188:191], v17 offset:0\n"
"v_mfma_f32_32x32x16_bf16 v[160:175], a[132:135], a[172:175], v[160:175]\n"
"ds_read_b128 a[160:163], v14 offset:40960\n"
"v_mfma_f32_32x32x16_bf16 v[112:127], a[40:43], a[176:179], v[112:127]\n"
"s_waitcnt lgkmcnt(2)\n"
"ds_read_b128 a[164:167], v15 offset:40960\n"
"v_mfma_f32_32x32x16_bf16 v[112:127], a[44:47], a[180:183], v[112:127]\n"
"ds_read_b128 a[168:171], v16 offset:40960\n"
"s_nop 5\n"
"v_cvt_pk_bf16_f32 v160, v160, v161\n"
"v_cvt_pk_bf16_f32 v161, v162, v163\n"
"v_cvt_pk_bf16_f32 v162, v164, v165\n"
"v_cvt_pk_bf16_f32 v163, v166, v167\n"
"v_mfma_f32_32x32x16_bf16 v[112:127], a[48:51], a[184:187], v[112:127]\n"
"s_waitcnt lgkmcnt(2)\n"
"ds_read_b128 a[172:175], v17 offset:40960\n"
"buffer_store_dwordx4 v[160:163], v44, s[40:43], s14 offen offset:0 sc1\n"
"v_cvt_pk_bf16_f32 v164, v168, v169\n"
"v_cvt_pk_bf16_f32 v165, v170, v171\n"
"v_cvt_pk_bf16_f32 v166, v172, v173\n"
"v_mfma_f32_32x32x16_bf16 v[112:127], a[52:55], a[188:191], v[112:127]\n"
"ds_read_b64_tr_b16 a[176:177], v27 offset:20480\n"
"ds_read_b64_tr_b16 a[178:179], v27 offset:21760\n"
"v_cvt_pk_bf16_f32 v167, v174, v175\n"
"v_mfma_f32_32x32x16_bf16 v[112:127], a[56:59], a[160:163], v[112:127]\n"
"s_waitcnt lgkmcnt(3)\n"
"ds_read_b64_tr_b16 a[180:181], v27 offset:25600\n"
"ds_read_b64_tr_b16 a[182:183], v27 offset:26880\n"
"v_mfma_f32_32x32x16_bf16 v[112:127], a[60:63], a[164:167], v[112:127]\n"
"ds_read_b64_tr_b16 a[184:185], v27 offset:30720\n"
"ds_read_b64_tr_b16 a[186:187], v27 offset:32000\n"
"buffer_store_dwordx4 v[164:167], v44, s[40:43], s14 offen offset:1024 sc1\n"
"v_mfma_f32_32x32x16_bf16 v[112:127], a[64:67], a[168:171], v[112:127]\n"
"s_waitcnt lgkmcnt(4)\n"
"ds_read_b64_tr_b16 a[188:189], v27 offset:35840\n"
"ds_read_b64_tr_b16 a[190:191], v27 offset:37120\n"
"v_mfma_f32_32x32x16_bf16 v[112:127], a[68:71], a[172:175], v[112:127]\n"
"ds_read_b64_tr_b16 a[160:161], v80 offset:0\n"
"ds_read_b64_tr_b16 a[162:163], v80 offset:1280\n"
"v_mfma_f32_32x32x16_bf16 v[112:127], a[72:75], a[176:179], v[112:127]\n"
"s_waitcnt lgkmcnt(4)\n"
"ds_read_b64_tr_b16 a[164:165], v80 offset:5120\n"
"ds_read_b64_tr_b16 a[166:167], v80 offset:6400\n"
"v_mfma_f32_32x32x16_bf16 v[112:127], a[76:79], a[180:183], v[112:127]\n"
"ds_read_b64_tr_b16 a[168:169], v80 offset:10240\n"
"ds_read_b64_tr_b16 a[170:171], v80 offset:11520\n"
"v_mfma_f32_32x32x16_bf16 v[112:127], a[80:83], a[184:187], v[112:127]\n"
"s_waitcnt lgkmcnt(4)\n"
"ds_read_b64_tr_b16 a[172:173], v80 offset:15360\n"
"ds_read_b64_tr_b16 a[174:175], v80 offset:16640\n"
"v_mfma_f32_32x32x16_bf16 v[112:127], a[84:87], a[188:191], v[112:127]\n"
"ds_read_b64_tr_b16 a[176:177], v26 offset:0\n"
"ds_read_b64_tr_b16 a[178:179], v26 offset:1280\n"
"v_mfma_f32_32x32x16_bf16 v[112:127], a[88:91], a[160:163], v[112:127]\n"
"s_waitcnt lgkmcnt(4)\n"
"ds_read_b64_tr_b16 a[180:181], v26 offset:5120\n"
"ds_read_b64_tr_b16 a[182:183], v26 offset:6400\n"
"v_mfma_f32_32x32x16_bf16 v[112:127], a[92:95], a[164:167], v[112:127]\n"
"ds_read_b64_tr_b16 a[184:185], v26 offset:10240\n"
"ds_read_b64_tr_b16 a[186:187], v26 offset:11520\n"
"v_mfma_f32_32x32x16_bf16 v[112:127], a[96:99], a[168:171], v[112:127]\n"
"s_waitcnt lgkmcnt(4)\n"
"ds_read_b64_tr_b16 a[188:189], v26 offset:15360\n"
"ds_read_b64_tr_b16 a[190:191], v26 offset:16640\n"
"v_mfma_f32_32x32x16_bf16 v[112:127], a[100:103], a[172:175], v[112:127]\n"
"ds_read_b64_tr_b16 a[160:161], v26 offset:20480\n"
"ds_read_b64_tr_b16 a[162:163], v26 offset:21760\n"
"v_mfma_f32_32x32x16_bf16 v[112:127], a[104:107], a[176:179], v[112:127]\n"
"s_waitcnt lgkmcnt(4)\n"
"ds_read_b64_tr_b16 a[164:165], v26 offset:25600\n"
"ds_read_b64_tr_b16 a[166:167], v26 offset:26880\n"
"v_mfma_f32_32x32x16_bf16 v[112:127], a[108:111], a[180:183], v[112:127]\n"
"ds_read_b64_tr_b16 a[168:169], v26 offset:30720\n"
"ds_read_b64_tr_b16 a[170:171], v26 offset:32000\n"
"v_mfma_f32_32x32x16_bf16 v[112:127], a[112:115], a[184:187], v[112:127]\n"
"s_waitcnt lgkmcnt(4)\n"
"ds_read_b64_tr_b16 a[172:173], v26 offset:35840\n"
"ds_read_b64_tr_b16 a[174:175], v26 offset:37120\n"
"s_waitcnt vmcnt(0)\n"
"buffer_store_dword v7, v4, s[40:43], s24 offen sc1\n"
"v_mfma_f32_32x32x16_bf16 v[112:127], a[116:119], a[188:191], v[112:127]\n"
"v_mfma_f32_32x32x16_bf16 v[112:127], a[120:123], a[160:163], v[112:127]\n"
"s_waitcnt lgkmcnt(2)\n"
"v_mfma_f32_32x32x16_bf16 v[112:127], a[124:127], a[164:167], v[112:127]\n"
"v_mfma_f32_32x32x16_bf16 v[112:127], a[128:131], a[168:171], v[112:127]\n"
"s_waitcnt lgkmcnt(0)\n"
"v_mfma_f32_32x32x16_bf16 v[112:127], a[132:135], a[172:175], v[112:127]\n"
"v_mfma_f32_32x32x16_bf16 v[112:127], a[240:243], v[50:53], v[112:127]\n"
"v_mfma_f32_32x32x16_bf16 v[112:127], a[244:247], v[54:57], v[112:127]\n"
"v_mfma_f32_32x32x16_bf16 v[112:127], v[224:227], v[58:61], v[112:127]\n"
"v_mfma_f32_32x32x16_bf16 v[112:127], v[228:231], v[62:65], v[112:127]\n"
"s_waitcnt vmcnt(0)\n"
"v_mov_b32 v3, 0\n"
"buffer_atomic_add v3, v1, s[40:43], s24 offen offset:0 sc0 sc1\n"
"s_waitcnt vmcnt(0)\n"
"v_cmp_ne_u32 s[0:1], s56, v3\n"
"s_nop 0\n"
"s_and_b32 s0, s0, s7\n"
"s_cbranch_scc1 LppA_slow%=\n"
"LppA_ok%=:\n"
"s_cmp_lg_u32 s52, s54\n"
"s_cbranch_scc1 LrcvA%=\n"
"buffer_load_dwordx4 v[152:155], v44, s[40:43], s19 offen offset:0 sc1\n"
"buffer_load_dwordx4 v[156:159], v44, s[40:43], s19 offen offset:1024 sc1\n"
"buffer_load_dwordx4 v[160:163], v44, s[40:43], s20 offen offset:0 sc1\n"
"buffer_load_dwordx4 v[164:167], v44, s[40:43], s20 offen offset:1024 sc1\n"
"buffer_load_dwordx4 v[168:171], v44, s[40:43], s21 offen offset:0 sc1\n"
"buffer_load_dwordx4 v[172:175], v44, s[40:43], s21 offen offset:1024 sc1\n"
"LrcvA%=:\n"
"buffer_load_dwordx4 v[128:131], v44, s[40:43], s16 offen offset:0 sc1\n"
"buffer_load_dwordx4 v[132:135], v44, s[40:43], s16 offen offset:1024 sc1\n"
"buffer_load_dwordx4 v[136:139], v44, s[40:43], s17 offen offset:0 sc1\n"
"buffer_load_dwordx4 v[140:143], v44, s[40:43], s17 offen offset:1024 sc1\n"
"buffer_load_dwordx4 v[144:147], v44, s[40:43], s18 offen offset:0 sc1\n"
"buffer_load_dwordx4 v[148:151], v44, s[40:43], s18 offen offset:1024 sc1\n"
"s_lshl_b32 s60, s98, 2\n"
"s_add_u32 s60, s60, s52\n"
"s_mov_b32 s61, 0x1708000\n"
"s_cmp_eq_u32 s60, 240\n"
"s_cselect_b32 s60, s61, 0x70000000\n"
"buffer_store_dword v7, v4, s[40:43], s60 offen sc1\n"
"s_waitcnt vmcnt(6)\n"
"v_dot2c_f32_bf16 v112, s26, v128\n"
"v_dot2c_f32_bf16 v113, s27, v128\n"
"v_dot2c_f32_bf16 v114, s26, v129\n"
"v_dot2c_f32_bf16 v115, s27, v129\n"
"v_dot2c_f32_bf16 v116, s26, v130\n"
"v_dot2c_f32_bf16 v117, s27, v130\n"
"v_dot2c_f32_bf16 v118, s26, v131\n"
"v_dot2c_f32_bf16 v119, s27, v131\n"
"s_waitcnt vmcnt(5)\n"
"v_dot2c_f32_bf16 v120, s26, v132\n"
"v_dot2c_f32_bf16 v121, s27, v132\n"
"v_dot2c_f32_bf16 v122, s26, v133\n"
"v_dot2c_f32_bf16 v123, s27, v133\n"
"v_dot2c_f32_bf16 v124, s26, v134\n"
"v_dot2c_f32_bf16 v125, s27, v134\n"
"v_dot2c_f32_bf16 v126, s26, v135\n"
"v_dot2c_f32_bf16 v127, s27, v135\n"
"s_waitcnt vmcnt(4)\n"
"v_dot2c_f32_bf16 v112, s26, v136\n"
"v_dot2c_f32_bf16 v113, s27, v136\n"
"v_dot2c_f32_bf16 v114, s26, v137\n"
"v_dot2c_f32_bf16 v115, s27, v137\n"
"v_dot2c_f32_bf16 v116, s26, v138\n"
"v_dot2c_f32_bf16 v117, s27, v138\n"
"v_dot2c_f32_bf16 v118, s26, v139\n"
"v_dot2c_f32_bf16 v119, s27, v139\n"
"s_waitcnt vmcnt(3)\n"
"v_dot2c_f32_bf16 v120, s26, v140\n"
"v_dot2c_f32_bf16 v121, s27, v140\n"
"v_dot2c_f32_bf16 v122, s26, v141\n"
"v_dot2c_f32_bf16 v123, s27, v141\n"
"v_dot2c_f32_bf16 v124, s26, v142\n"
"v_dot2c_f32_bf16 v125, s27, v142\n"
"v_dot2c_f32_bf16 v126, s26, v143\n"
"v_dot2c_f32_bf16 v127, s27, v143\n"
"s_waitcnt vmcnt(2)\n"
"v_dot2c_f32_bf16 v112, s26, v144\n"
"v_dot2c_f32_bf16 v113, s27, v144\n"
"v_dot2c_f32_bf16 v114, s26, v145\n"
"v_dot2c_f32_bf16 v115, s27, v145\n"
"v_dot2c_f32_bf16 v116, s26, v146\n"
"v_dot2c_f32_bf16 v117, s27, v146\n"
"v_dot2c_f32_bf16 v118, s26, v147\n"
"v_dot2c_f32_bf16 v119, s27, v147\n"
"s_waitcnt vmcnt(1)\n"
"v_dot2c_f32_bf16 v120, s26, v148\n"
"v_dot2c_f32_bf16 v121, s27, v148\n"
"v_dot2c_f32_bf16 v122, s26, v149\n"
"v_dot2c_f32_bf16 v123, s27, v149\n"
"v_dot2c_f32_bf16 v124, s26, v150\n"
"v_dot2c_f32_bf16 v125, s27, v150\n"
"v_dot2c_f32_bf16 v126, s26, v151\n"
"v_dot2c_f32_bf16 v127, s27, v151\n"
"s_add_u32 s62, s22, 0\n"
"buffer_store_dword v112, v95, s[44:47], s62 offen offset:0 sc1\n"
"buffer_store_dword v113, v95, s[44:47], s62 offen offset:2560 sc1\n"
"s_add_u32 s62, s22, 5120\n"
"buffer_store_dword v114, v95, s[44:47], s62 offen offset:0 sc1\n"
"buffer_store_dword v115, v95, s[44:47], s62 offen offset:2560 sc1\n"
"s_add_u32 s62, s22, 10240\n"
"buffer_store_dword v116, v95, s[44:47], s62 offen offset:0 sc1\n"
"buffer_store_dword v117, v95, s[44:47], s62 offen offset:2560 sc1\n"
"s_add_u32 s62, s22, 15360\n"
"buffer_store_dword v118, v95, s[44:47], s62 offen offset:0 sc1\n"
"buffer_store_dword v119, v95, s[44:47], s62 offen offset:2560 sc1\n"
"s_add_u32 s62, s22, 40960\n"
"buffer_store_dword v120, v95, s[44:47], s62 offen offset:0 sc1\n"
"buffer_store_dword v121, v95, s[44:47], s62 offen offset:2560 sc1\n"
"s_add_u32 s62, s22, 46080\n"
"buffer_store_dword v122, v95, s[44:47], s62 offen offset:0 sc1\n"
"buffer_store_dword v123, v95, s[44:47], s62 offen offset:2560 sc1\n"
"s_add_u32 s62, s22, 51200\n"
"buffer_store_dword v124, v95, s[44:47], s62 offen offset:0 sc1\n"
"buffer_store_dword v125, v95, s[44:47], s62 offen offset:2560 sc1\n"
"s_add_u32 s62, s22, 56320\n"
"buffer_store_dword v126, v95, s[44:47], s62 offen offset:0 sc1\n"
"buffer_store_dword v127, v95, s[44:47], s62 offen offset:2560 sc1\n"
"s_cmp_lg_u32 s52, s54\n"
"s_cbranch_scc1 LfinA%=\n"
"v_dot2c_f32_bf16 v176, s26, v152\n"
"v_dot2c_f32_bf16 v177, s27, v152\n"
"v_dot2c_f32_bf16 v178, s26, v153\n"
"v_dot2c_f32_bf16 v179, s27, v153\n"
"v_dot2c_f32_bf16 v180, s26, v154\n"
"v_dot2c_f32_bf16 v181, s27, v154\n"
"v_dot2c_f32_bf16 v182, s26, v155\n"
"v_dot2c_f32_bf16 v183, s27, v155\n"
"v_dot2c_f32_bf16 v184, s26, v156\n"
"v_dot2c_f32_bf16 v185, s27, v156\n"
"v_dot2c_f32_bf16 v186, s26, v157\n"
"v_dot2c_f32_bf16 v187, s27, v157\n"
"v_dot2c_f32_bf16 v188, s26, v158\n"
"v_dot2c_f32_bf16 v189, s27, v158\n"
"v_dot2c_f32_bf16 v190, s26, v159\n"
"v_dot2c_f32_bf16 v191, s27, v159\n"
"v_dot2c_f32_bf16 v176, s26, v160\n"
"v_dot2c_f32_bf16 v177, s27, v160\n"
"v_dot2c_f32_bf16 v178, s26, v161\n"
"v_dot2c_f32_bf16 v179, s27, v161\n"
"v_dot2c_f32_bf16 v180, s26, v162\n"
"v_dot2c_f32_bf16 v181, s27, v162\n"
"v_dot2c_f32_bf16 v182, s26, v163\n"
"v_dot2c_f32_bf16 v183, s27, v163\n"
"v_dot2c_f32_bf16 v184, s26, v164\n"
"v_dot2c_f32_bf16 v185, s27, v164\n"
"v_dot2c_f32_bf16 v186, s26, v165\n"
"v_dot2c_f32_bf16 v187, s27, v165\n"
"v_dot2c_f32_bf16 v188, s26, v166\n"
"v_dot2c_f32_bf16 v189, s27, v166\n"
"v_dot2c_f32_bf16 v190, s26, v167\n"
"v_dot2c_f32_bf16 v191, s27, v167\n"
"v_dot2c_f32_bf16 v176, s26, v168\n"
"v_dot2c_f32_bf16 v177, s27, v168\n"
"v_dot2c_f32_bf16 v178, s26, v169\n"
"v_dot2c_f32_bf16 v179, s27, v169\n"
"v_dot2c_f32_bf16 v180, s26, v170\n"
"v_dot2c_f32_bf16 v181, s27, v170\n"
"v_dot2c_f32_bf16 v182, s26, v171\n"
"v_dot2c_f32_bf16 v183, s27, v171\n"
"v_dot2c_f32_bf16 v184, s26, v172\n"
"v_dot2c_f32_bf16 v185, s27, v172\n"
"v_dot2c_f32_bf16 v186, s26, v173\n"
"v_dot2c_f32_bf16 v187, s27, v173\n"
"v_dot2c_f32_bf16 v188, s26, v174\n"
"v_dot2c_f32_bf16 v189, s27, v174\n"
"v_dot2c_f32_bf16 v190, s26, v175\n"
"v_dot2c_f32_bf16 v191, s27, v175\n"
"s_add_u32 s62, s23, 0\n"
"buffer_store_dword v176, v95, s[44:47], s62 offen offset:0 sc1\n"
"buffer_store_dword v177, v95, s[44:47], s62 offen offset:2560 sc1\n"
"s_add_u32 s62, s23, 5120\n"
"buffer_store_dword v178, v95, s[44:47], s62 offen offset:0 sc1\n"
"buffer_store_dword v179, v95, s[44:47], s62 offen offset:2560 sc1\n"
"s_add_u32 s62, s23, 10240\n"
"buffer_store_dword v180, v95, s[44:47], s62 offen offset:0 sc1\n"
"buffer_store_dword v181, v95, s[44:47], s62 offen offset:2560 sc1\n"
"s_add_u32 s62, s23, 15360\n"
"buffer_store_dword v182, v95, s[44:47], s62 offen offset:0 sc1\n"
"buffer_store_dword v183, v95, s[44:47], s62 offen offset:2560 sc1\n"
"s_add_u32 s62, s23, 40960\n"
"buffer_store_dword v184, v95, s[44:47], s62 offen offset:0 sc1\n"
"buffer_store_dword v185, v95, s[44:47], s62 offen offset:2560 sc1\n"
"s_add_u32 s62, s23, 46080\n"
"buffer_store_dword v186, v95, s[44:47], s62 offen offset:0 sc1\n"
"buffer_store_dword v187, v95, s[44:47], s62 offen offset:2560 sc1\n"
"s_add_u32 s62, s23, 51200\n"
"buffer_store_dword v188, v95, s[44:47], s62 offen offset:0 sc1\n"
"buffer_store_dword v189, v95, s[44:47], s62 offen offset:2560 sc1\n"
"s_add_u32 s62, s23, 56320\n"
"buffer_store_dword v190, v95, s[44:47], s62 offen offset:0 sc1\n"
"buffer_store_dword v191, v95, s[44:47], s62 offen offset:2560 sc1\n"
"LfinA%=:\n"
"s_branch Lend%=\n"
"LtypeB%=:\n"
"s_and_b32 s60, s54, 1\n"
"s_lshl_b32 s60, s60, 1\n"
"s_add_u32 s63, s63, s60\n"
"s_lshl_b32 s61, s55, 2\n"
"s_add_u32 s63, s63, s61\n"
"s_add_u32 s64, s64, s55\n"
"s_add_u32 s64, s64, 4\n"
"s_add_u32 s61, s63, s52\n"
"s_mul_i32 s61, s61, 20480\n"
"v_mul_u32_u24 v86, 1280, v61\n"
"v_add3_u32 v38, v86, v64, s61\n"
"s_add_u32 s61, s61, 9216\n"
"v_add3_u32 v39, v86, v65, s61\n"
"s_sub_u32 s16, s16, 2048\n"
"s_subb_u32 s17, s17, 0\n"
"s_add_u32 m0, s57, 0\n"
"s_nop 0\n"
"buffer_load_dwordx4 v38, s[12:15], s68 offen offset:0 lds\n"
"buffer_load_dwordx4 v39, s[12:15], s68 offen offset:1024 lds\n"
"buffer_load_dwordx4 v38, s[16:19], s68 offen offset:2048 lds\n"
"buffer_load_dwordx4 v39, s[16:19], s68 offen offset:3072 lds\n"
"v_mov_b32 v4, 0\n"
"s_mov_b32 s81, 0x1708000\n"
"buffer_load_dword v7, v4, s[40:43], s81 offen sc1\n"
"s_lshl_b32 s4, s53, 2\n"
"s_add_u32 s4, s4, s54\n"
"s_lshl_b32 s4, s4, 2\n"
"s_add_u32 s4, s4, s55\n"
"s_lshl_b32 s4, s4, 2\n"
"s_add_u32 s4, s4, s52\n"
"s_mul_i32 s67, s4, 2560\n"
"v_lshrrev_b32 v214, 5, v2\n"
"v_and_b32 v215, 31, v2\n"
"v_mul_u32_u24 v212, 1280, v214\n"
"v_lshl_add_u32 v212, v215, 3, v212\n"
"buffer_load_dwordx2 v[192:193], v212, s[8:11], s67 offen offset:0\n"
"buffer_load_dwordx2 v[194:195], v212, s[8:11], s67 offen offset:256\n"
"buffer_load_dwordx2 v[196:197], v212, s[8:11], s67 offen offset:512\n"
"buffer_load_dwordx2 v[198:199], v212, s[8:11], s67 offen offset:768\n"
"buffer_load_dwordx2 v[200:201], v212, s[8:11], s67 offen offset:1024\n"
"s_add_u32 s69, s68, 128\n"
"s_cmp_eq_u32 s69, 1280\n"
"s_cselect_b32 s69, 0, s69\n"
"s_add_u32 s70, s69, 128\n"
"s_cmp_eq_u32 s70, 1280\n"
"s_cselect_b32 s70, 0, s70\n"
"s_add_u32 m0, s57, 118784\n"
"s_nop 0\n"
"buffer_load_dwordx4 v30, s[8:11], s69 offen offset:0 lds\n"
"s_add_u32 s71, s70, 128\n"
"s_cmp_eq_u32 s71, 1280\n"
"s_cselect_b32 s71, 0, s71\n"
"s_mul_i32 s60, s52, 49152\n"
"s_lshl_b32 s61, s64, 7\n"
"s_add_u32 s60, s60, s61\n"
"v_mul_u32_u24 v86, 3072, v61\n"
"buffer_load_dwordx4 v31, s[8:11], s69 offen offset:1024 lds\n"
"v_and_b32 v87, 1, v63\n"
"v_lshlrev_b32 v87, 1, v87\n"
"v_xor_b32 v87, v87, v62\n"
"v_lshlrev_b32 v87, 4, v87\n"
"v_add3_u32 v34, v86, v87, s60\n"
"v_add_u32 v35, 23552, v34\n"
"v_xor_b32 v36, 64, v34\n"
"buffer_load_dwordx4 v32, s[8:11], s69 offen offset:2048 lds\n"
"v_xor_b32 v37, 64, v35\n"
"s_sub_u32 s24, s24, 2048\n"
"s_subb_u32 s25, s25, 0\n"
"s_mov_b32 s100, 0xffff0000\n"
"v_and_b32 v61, 31, v2\n"
"v_lshrrev_b32 v62, 5, v2\n"
"v_bfe_u32 v64, v2, 1, 3\n"
"buffer_load_dwordx4 v33, s[8:11], s69 offen offset:3072 lds\n"
"v_xor_b32 v64, v64, v62\n"
"v_lshlrev_b32 v64, 4, v64\n"
"v_lshl_add_u32 v10, v61, 7, v64\n"
"v_xor_b32 v11, 32, v10\n"
"v_xor_b32 v12, 64, v10\n"
"v_xor_b32 v13, 96, v10\n"
"s_add_u32 s60, s57, 102400\n"
"s_add_u32 m0, s57, 20480\n"
"s_nop 0\n"
"buffer_load_dwordx4 v38, s[12:15], s69 offen offset:0 lds\n"
"v_add_u32 v26, s60, v10\n"
"v_add_u32 v27, s60, v11\n"
"v_add_u32 v28, s60, v12\n"
"v_add_u32 v29, s60, v13\n"
"v_add_u32 v224, s57, v10\n"
"v_add_u32 v225, s57, v11\n"
"v_add_u32 v226, s57, v12\n"
"buffer_load_dwordx4 v39, s[12:15], s69 offen offset:1024 lds\n"
"v_add_u32 v227, s57, v13\n"
"v_bfe_u32 v64, v2, 2, 2\n"
"v_and_b32 v65, 1, v2\n"
"v_lshlrev_b32 v65, 4, v65\n"
"v_and_b32 v86, 2, v2\n"
"v_lshl_or_b32 v65, v86, 2, v65\n"
"v_and_b32 v86, 1, v63\n"
"buffer_load_dwordx4 v38, s[16:19], s69 offen offset:2048 lds\n"
"v_lshl_add_u32 v87, v62, 3, v64\n"
"v_lshrrev_b32 v61, 2, v2\n"
"v_xor_b32 v61, v61, v63\n"
"v_and_b32 v61, 1, v61\n"
"v_and_or_b32 v61, v2, 3, v61\n"
"v_cmp_eq_u32 vcc, 0, v61\n"
"s_mov_b32 s62, 0x3f803f80\n"
"buffer_load_dwordx4 v39, s[16:19], s69 offen offset:3072 lds\n"
"v_mov_b32 v61, s62\n"
"v_cndmask_b32 v61, 0, v61, vcc\n"
"v_accvgpr_write_b32 a244, v61\n"
"v_accvgpr_write_b32 a245, v61\n"
"v_accvgpr_write_b32 a246, v61\n"
"v_accvgpr_write_b32 a247, v61\n"
"s_waitcnt vmcnt(18)\n"
"ds_read_b128 a[0:3], v26 offset:0\n"
"ds_read_b128 a[4:7], v27 offset:0\n"
"ds_read_b128 a[8:11], v28 offset:0\n"
"ds_read_b128 a[12:15], v29 offset:0\n"
"s_waitcnt vmcnt(14)\n"
"s_barrier\n"
"ds_read_b128 a[160:163], v10 offset:0\n"
"ds_read_b128 a[164:167], v10 offset:4096\n"
"ds_read_b128 a[168:171], v224 offset:0\n"
"ds_read_b128 a[172:175], v10 offset:8192\n"
"ds_read_b128 a[176:179], v10 offset:12288\n"
"s_waitcnt lgkmcnt(4)\n"
"v_mfma_f32_32x32x16_bf16 v[112:127], a[0:3], a[160:163], 0\n"
"s_waitcnt lgkmcnt(2)\n"
"ds_read_b128 a[180:183], v11 offset:0\n"
"s_add_u32 m0, s57, 135168\n"
"s_nop 0\n"
"buffer_load_dwordx4 v30, s[8:11], s70 offen offset:0 lds\n"
"v_and_b32 v106, 63, v2\n"
"v_lshlrev_b32 v106, 1, v106\n"
"v_mfma_f32_32x32x16_bf16 v[128:143], a[0:3], a[164:167], 0\n"
"ds_read_b128 a[184:187], v11 offset:4096\n"
"s_lshl_b32 s60, s63, 5\n"
"buffer_load_ushort v107, v106, s[48:51], s60 offen\n"
"s_mov_b64 s[48:49], s[72:73]\n"
"v_mfma_f32_16x16x32_bf16 a[248:251], a[244:247], a[168:171], 0\n"
"s_waitcnt lgkmcnt(2)\n"
"ds_read_b128 a[188:191], v225 offset:0\n"
"buffer_load_dwordx4 v31, s[8:11], s70 offen offset:1024 lds\n"
"v_mfma_f32_32x32x16_bf16 v[144:159], a[0:3], a[172:175], 0\n"
"ds_read_b128 a[160:163], v11 offset:8192\n"
"v_mfma_f32_32x32x16_bf16 v[160:175], a[0:3], a[176:179], 0\n"
"s_waitcnt lgkmcnt(2)\n"
"ds_read_b128 a[164:167], v11 offset:12288\n"
"buffer_load_dwordx4 v32, s[8:11], s70 offen offset:2048 lds\n"
"v_mfma_f32_32x32x16_bf16 v[112:127], a[4:7], a[180:183], v[112:127]\n"
"ds_read_b128 a[168:171], v12 offset:0\n"
"buffer_load_ushort v108, v106, s[48:51], s60 offen\n"
"s_mov_b64 s[48:49], s[74:75]\n"
"v_mfma_f32_32x32x16_bf16 v[128:143], a[4:7], a[184:187], v[128:143]\n"
"s_waitcnt lgkmcnt(2)\n"
"ds_read_b128 a[172:175], v12 offset:4096\n"
"buffer_load_dwordx4 v33, s[8:11], s70 offen offset:3072 lds\n"
"s_add_u32 m0, s57, 40960\n"
"v_mfma_f32_16x16x32_bf16 a[248:251], a[244:247], a[188:191], a[248:251]\n"
"ds_read_b128 a[176:179], v226 offset:0\n"
"buffer_load_ushort v109, v106, s[48:51], s60 offen\n"
"v_mfma_f32_32x32x16_bf16 v[144:159], a[4:7], a[160:163], v[144:159]\n"
"s_waitcnt lgkmcnt(2)\n"
"ds_read_b128 a[180:183], v12 offset:8192\n"
"buffer_load_dwordx4 v38, s[12:15], s70 offen offset:0 lds\n"
"s_mov_b64 s[48:49], s[76:77]\n"
"s_add_u32 s72, s71, 128\n"
"s_cmp_eq_u32 s72, 1280\n"
"s_cselect_b32 s72, 0, s72\n"
"v_mfma_f32_32x32x16_bf16 v[160:175], a[4:7], a[164:167], v[160:175]\n"
"ds_read_b128 a[184:187], v12 offset:12288\n"
"s_add_u32 s73, s72, 128\n"
"s_cmp_eq_u32 s73, 1280\n"
"s_cselect_b32 s73, 0, s73\n"
"s_add_u32 s74, s73, 128\n"
"s_cmp_eq_u32 s74, 1280\n"
"s_cselect_b32 s74, 0, s74\n"
"v_mfma_f32_32x32x16_bf16 v[112:127], a[8:11], a[168:171], v[112:127]\n"
"s_waitcnt lgkmcnt(2)\n"
"ds_read_b128 a[188:191], v13 offset:0\n"
"buffer_load_dwordx4 v39, s[12:15], s70 offen offset:1024 lds\n"
"s_add_u32 s75, s74, 128\n"
"s_cmp_eq_u32 s75, 1280\n"
"s_cselect_b32 s75, 0, s75\n"
"v_mfma_f32_32x32x16_bf16 v[128:143], a[8:11], a[172:175], v[128:143]\n"
"s_waitcnt vmcnt(13)\n"
"ds_read_b128 a[16:19], v26 offset:16384\n"
"ds_read_b128 a[20:23], v27 offset:16384\n"
"ds_read_b128 a[160:163], v13 offset:4096\n"
"s_add_u32 s76, s75, 128\n"
"s_cmp_eq_u32 s76, 1280\n"
"s_cselect_b32 s76, 0, s76\n"
"v_mfma_f32_16x16x32_bf16 a[248:251], a[244:247], a[176:179], a[248:251]\n"
"s_waitcnt lgkmcnt(4)\n"
"ds_read_b128 a[164:167], v227 offset:0\n"
"buffer_load_dwordx4 v38, s[16:19], s70 offen offset:2048 lds\n"
"v_mfma_f32_32x32x16_bf16 v[144:159], a[8:11], a[180:183], v[144:159]\n"
"ds_read_b128 a[24:27], v28 offset:16384\n"
"ds_read_b128 a[28:31], v29 offset:16384\n"
"ds_read_b128 a[168:171], v13 offset:8192\n"
"s_add_u32 s77, s76, 128\n"
"s_cmp_eq_u32 s77, 1280\n"
"s_cselect_b32 s77, 0, s77\n"
"s_mov_b32 s80, 0x3f803f80\n"
"v_readfirstlane_b32 s56, v7\n"
"v_mov_b32 v216, 0\n"
"v_mov_b32 v217, 0\n"
"s_add_u32 s56, s56, 1\n"
"v_mov_b32 v7, s56\n"
"v_dot2c_f32_bf16 v216, s80, v192\n"
"v_dot2c_f32_bf16 v217, v192, v192\n"
"v_dot2c_f32_bf16 v216, s80, v193\n"
"v_dot2c_f32_bf16 v217, v193, v193\n"
"v_dot2c_f32_bf16 v216, s80, v194\n"
"v_dot2c_f32_bf16 v217, v194, v194\n"
"v_dot2c_f32_bf16 v216, s80, v195\n"
"v_dot2c_f32_bf16 v217, v195, v195\n"
"v_dot2c_f32_bf16 v216, s80, v196\n"
"v_dot2c_f32_bf16 v217, v196, v196\n"
"v_dot2c_f32_bf16 v216, s80, v197\n"
"v_dot2c_f32_bf16 v217, v197, v197\n"
"v_dot2c_f32_bf16 v216, s80, v198\n"
"v_dot2c_f32_bf16 v217, v198, v198\n"
"v_dot2c_f32_bf16 v216, s80, v199\n"
"v_dot2c_f32_bf16 v217, v199, v199\n"
"v_dot2c_f32_bf16 v216, s80, v200\n"
"v_dot2c_f32_bf16 v217, v200, v200\n"
"v_dot2c_f32_bf16 v216, s80, v201\n"
"v_dot2c_f32_bf16 v217, v201, v201\n"
"v_mfma_f32_32x32x16_bf16 v[160:175], a[8:11], a[184:187], v[160:175]\n"
"s_waitcnt vmcnt(10)\n"
"s_barrier\n"
"s_waitcnt lgkmcnt(4)\n"
"ds_read_b128 a[172:175], v13 offset:12288\n"
"buffer_load_dwordx4 v39, s[16:19], s70 offen offset:3072 lds\n"
"s_add_u32 m0, s57, 102400\n"
"v_mfma_f32_32x32x16_bf16 v[112:127], a[12:15], a[188:191], v[112:127]\n"
"ds_read_b128 a[176:179], v10 offset:20480\n"
"v_mov_b32 v193, s56\n"
"v_mov_b32 v194, 0x70000000\n"
"v_cmp_eq_u32 vcc, 0, v215\n"
"v_add_f32_dpp v216, v216, v216 quad_perm:[1,0,3,2] row_mask:0xf bank_mask:0xf\n"
"v_add_f32_dpp v217, v217, v217 quad_perm:[1,0,3,2] row_mask:0xf bank_mask:0xf\n"
"v_mfma_f32_32x32x16_bf16 v[128:143], a[12:15], a[160:163], v[128:143]\n"
"s_waitcnt lgkmcnt(2)\n"
"ds_read_b128 a[180:183], v10 offset:24576\n"
"buffer_load_dwordx4 v30, s[8:11], s71 offen offset:0 lds\n"
"v_add_f32_dpp v216, v216, v216 quad_perm:[2,3,0,1] row_mask:0xf bank_mask:0xf\n"
"v_add_f32_dpp v217, v217, v217 quad_perm:[2,3,0,1] row_mask:0xf bank_mask:0xf\n"
"s_nop 0\n"
"v_add_f32_dpp v216, v216, v216 row_half_mirror row_mask:0xf bank_mask:0xf\n"
"v_mfma_f32_16x16x32_bf16 a[248:251], a[244:247], a[164:167], a[248:251]\n"
"ds_read_b128 a[184:187], v224 offset:20480\n"
"v_add_f32_dpp v217, v217, v217 row_half_mirror row_mask:0xf bank_mask:0xf\n"
"v_mfma_f32_32x32x16_bf16 v[144:159], a[12:15], a[168:171], v[144:159]\n"
"s_waitcnt lgkmcnt(2)\n"
"ds_read_b128 a[188:191], v10 offset:28672\n"
"buffer_load_dwordx4 v31, s[8:11], s71 offen offset:1024 lds\n"
"v_add_f32_dpp v216, v216, v216 row_mirror row_mask:0xf bank_mask:0xf\n"
"v_add_f32_dpp v217, v217, v217 row_mirror row_mask:0xf bank_mask:0xf\n"
"v_mov_b32 v196, v216\n"
"v_mfma_f32_32x32x16_bf16 v[160:175], a[12:15], a[172:175], v[160:175]\n"
"ds_read_b128 a[160:163], v10 offset:32768\n"
"v_mov_b32 v197, v217\n"
"v_lshlrev_b32 v214, 3, v214\n"
"v_cndmask_b32 v214, v194, v214, vcc\n"
"v_permlane16_swap_b32 v216, v196\n"
"v_permlane16_swap_b32 v217, v197\n"
"v_mfma_f32_32x32x16_bf16 v[112:127], a[16:19], a[176:179], v[112:127]\n"
"s_waitcnt lgkmcnt(2)\n"
"ds_read_b128 a[164:167], v11 offset:20480\n"
"buffer_load_dwordx4 v32, s[8:11], s71 offen offset:2048 lds\n"
"v_add_f32 v216, v216, v196\n"
"v_add_f32 v217, v217, v197\n"
"v_mul_f32 v216, 0x3acccccd, v216\n"
"v_mfma_f32_32x32x16_bf16 v[128:143], a[16:19], a[180:183], v[128:143]\n"
"ds_read_b128 a[168:171], v11 offset:24576\n"
"v_mul_f32 v217, 0x3acccccd, v217\n"
"v_fma_f32 v217, -v216, v216, v217\n"
"v_max_f32 v217, 0, v217\n"
"v_cvt_pk_bf16_f32 v217, v217, v217\n"
"v_and_b32 v217, s100, v217\n"
"v_mfma_f32_16x16x32_bf16 a[248:251], a[244:247], a[184:187], a[248:251]\n"
"s_waitcnt lgkmcnt(2)\n"
"ds_read_b128 a[172:175], v225 offset:20480\n"
"buffer_load_dwordx4 v33, s[8:11], s71 offen offset:3072 lds\n"
"v_mfma_f32_32x32x16_bf16 v[144:159], a[16:19], a[188:191], v[144:159]\n"
"ds_read_b128 a[176:179], v11 offset:28672\n"
"s_add_u32 m0, s57, 61440\n"
"v_add_f32 v217, 0x3727c5ac, v217\n"
"v_cvt_pk_bf16_f32 v217, v217, v217\n"
"v_and_b32 v217, s100, v217\n"
"v_mfma_f32_32x32x16_bf16 v[160:175], a[16:19], a[160:163], v[160:175]\n"
"s_waitcnt lgkmcnt(2)\n"
"ds_read_b128 a[180:183], v11 offset:32768\n"
"buffer_load_dwordx4 v38, s[12:15], s71 offen offset:0 lds\n"
"v_sqrt_f32 v217, v217\n"
"s_lshl_b32 s60, s4, 4\n"
"v_mfma_f32_32x32x16_bf16 v[112:127], a[20:23], a[164:167], v[112:127]\n"
"ds_read_b128 a[184:187], v12 offset:20480\n"
"s_add_u32 s60, s60, 0x1700000\n"
"v_cvt_pk_bf16_f32 v192, v216, v217\n"
"v_mfma_f32_32x32x16_bf16 v[128:143], a[20:23], a[168:171], v[128:143]\n"
"s_waitcnt lgkmcnt(2)\n"
"ds_read_b128 a[188:191], v12 offset:24576\n"
"buffer_store_dwordx2 v[192:193], v214, s[40:43], s60 offen sc1\n"
"v_add_u32 v14, 61440, v10\n"
"v_add_u32 v15, 61440, v11\n"
"v_mfma_f32_16x16x32_bf16 a[248:251], a[244:247], a[172:175], a[248:251]\n"
"ds_read_b128 a[160:163], v226 offset:20480\n"
"buffer_load_dwordx4 v39, s[12:15], s71 offen offset:1024 lds\n"
"v_mfma_f32_32x32x16_bf16 v[144:159], a[20:23], a[176:179], v[144:159]\n"
"s_waitcnt lgkmcnt(2)\n"
"ds_read_b128 a[164:167], v12 offset:28672\n"
"v_add_u32 v16, 61440, v12\n"
"v_add_u32 v17, 61440, v13\n"
"s_bitcmp1_b32 s101, 0\n"
"s_cselect_b64 vcc, 0, -1\n"
"v_cndmask_b32 v61, 0, v61, vcc\n"
"v_mfma_f32_32x32x16_bf16 v[160:175], a[20:23], a[180:183], v[160:175]\n"
"ds_read_b128 a[168:171], v12 offset:32768\n"
"buffer_load_dwordx4 v38, s[16:19], s71 offen offset:2048 lds\n"
"v_accvgpr_write_b32 a252, v61\n"
"v_accvgpr_write_b32 a253, v61\n"
"v_accvgpr_write_b32 a254, v61\n"
"v_mfma_f32_32x32x16_bf16 v[112:127], a[24:27], a[184:187], v[112:127]\n"
"s_waitcnt lgkmcnt(2)\n"
"ds_read_b128 a[172:175], v13 offset:20480\n"
"v_accvgpr_write_b32 a255, v61\n"
"v_lshlrev_b32 v18, 7, v87\n"
"v_lshl_add_u32 v18, v86, 11, v18\n"
"v_add_u32 v18, v18, v65\n"
"v_lshrrev_b32 v61, 1, v64\n"
"v_mfma_f32_32x32x16_bf16 v[128:143], a[24:27], a[188:191], v[128:143]\n"
"s_waitcnt vmcnt(13)\n"
"ds_read_b128 a[32:35], v26 offset:32768\n"
"ds_read_b128 a[36:39], v27 offset:32768\n"
"ds_read_b128 a[176:179], v13 offset:24576\n"
"buffer_load_dwordx4 v39, s[16:19], s71 offen offset:3072 lds\n"
"s_add_u32 m0, s57, 118784\n"
"v_mfma_f32_16x16x32_bf16 a[248:251], a[244:247], a[160:163], a[248:251]\n"
"s_waitcnt lgkmcnt(4)\n"
"ds_read_b128 a[180:183], v227 offset:20480\n"
"v_mfma_f32_32x32x16_bf16 v[144:159], a[24:27], a[164:167], v[144:159]\n"
"ds_read_b128 a[40:43], v28 offset:32768\n"
"ds_read_b128 a[44:47], v29 offset:32768\n"
"ds_read_b128 a[184:187], v13 offset:28672\n"
"buffer_load_dwordx4 v30, s[8:11], s72 offen offset:0 lds\n"
"v_lshl_or_b32 v61, v86, 1, v61\n"
"v_mfma_f32_32x32x16_bf16 v[160:175], a[24:27], a[168:171], v[160:175]\n"
"s_waitcnt vmcnt(10)\n"
"s_barrier\n"
"s_waitcnt lgkmcnt(4)\n"
"ds_read_b128 a[188:191], v13 offset:32768\n"
"v_lshl_add_u32 v18, v61, 5, v18\n"
"v_xor_b32 v19, 32, v18\n"
"v_xor_b32 v20, 64, v18\n"
"v_mfma_f32_32x32x16_bf16 v[112:127], a[28:31], a[172:175], v[112:127]\n"
"ds_read_b128 a[160:163], v10 offset:40960\n"
"buffer_load_dwordx4 v31, s[8:11], s72 offen offset:1024 lds\n"
"v_xor_b32 v21, 96, v18\n"
"s_lshl_b32 s61, s52, 5\n"
"v_xor_b32 v8, s61, v18\n"
"v_mfma_f32_32x32x16_bf16 v[128:143], a[28:31], a[176:179], v[128:143]\n"
"s_waitcnt lgkmcnt(2)\n"
"ds_read_b128 a[164:167], v10 offset:45056\n"
"v_add_u32 v22, 61440, v18\n"
"v_add_u32 v23, 61440, v19\n"
"v_add_u32 v24, 61440, v20\n"
"v_add_u32 v25, 61440, v21\n"
"v_mfma_f32_16x16x32_bf16 a[248:251], a[244:247], a[180:183], a[248:251]\n"
"ds_read_b128 a[168:171], v224 offset:40960\n"
"buffer_load_dwordx4 v32, s[8:11], s72 offen offset:2048 lds\n"
"v_mfma_f32_32x32x16_bf16 v[144:159], a[28:31], a[184:187], v[144:159]\n"
"s_waitcnt lgkmcnt(2)\n"
"ds_read_b128 a[172:175], v10 offset:49152\n"
"v_add_u32 v9, 61440, v8\n"
"v_lshlrev_b32 v44, 4, v2\n"
"v_and_b32 v61, 31, v2\n"
"v_and_b32 v88, 0xfffffff3, v61\n"
"v_and_b32 v60, 4, v61\n"
"v_mfma_f32_32x32x16_bf16 v[160:175], a[28:31], a[188:191], v[160:175]\n"
"ds_read_b128 a[176:179], v10 offset:53248\n"
"buffer_load_dwordx4 v33, s[8:11], s72 offen offset:3072 lds\n"
"s_add_u32 m0, s57, 81920\n"
"v_and_b32 v89, 8, v61\n"
"v_lshl_or_b32 v88, v60, 1, v88\n"
"v_lshrrev_b32 v89, 1, v89\n"
"v_or_b32 v88, v88, v89\n"
"v_lshlrev_b32 v60, 3, v88\n"
"v_mfma_f32_32x32x16_bf16 v[112:127], a[32:35], a[160:163], v[112:127]\n"
"s_waitcnt lgkmcnt(2)\n"
"ds_read_b128 a[180:183], v11 offset:40960\n"
"buffer_load_dwordx4 v38, s[12:15], s72 offen offset:0 lds\n"
"s_sub_u32 s32, s32, 3072\n"
"s_subb_u32 s33, s33, 0\n"
"s_sub_u32 s36, s36, 3072\n"
"s_subb_u32 s37, s37, 0\n"
"v_mfma_f32_32x32x16_bf16 v[128:143], a[32:35], a[164:167], v[128:143]\n"
"ds_read_b128 a[184:187], v11 offset:45056\n"
"s_mul_i32 s59, s52, 5120\n"
"s_mul_i32 s60, s52, 1280\n"
"s_add_u32 s60, s60, 152576\n"
"v_mul_u32_u24 v81, 80, v87\n"
"v_mfma_f32_16x16x32_bf16 a[248:251], a[252:255], a[168:171], a[248:251]\n"
"s_waitcnt lgkmcnt(2)\n"
"ds_read_b128 a[188:191], v225 offset:40960\n"
"buffer_load_dwordx4 v39, s[12:15], s72 offen offset:1024 lds\n"
"v_mfma_f32_32x32x16_bf16 v[144:159], a[32:35], a[172:175], v[144:159]\n"
"ds_read_b128 a[160:163], v11 offset:49152\n"
"v_lshl_add_u32 v81, v86, 5, v81\n"
"v_add3_u32 v81, v81, v65, s60\n"
"v_mul_u32_u24 v87, 320, v87\n"
"v_lshl_add_u32 v87, v86, 5, v87\n"
"v_and_b32 v65, 3, v2\n"
"v_mfma_f32_32x32x16_bf16 v[160:175], a[32:35], a[176:179], v[160:175]\n"
"s_waitcnt lgkmcnt(2)\n"
"ds_read_b128 a[164:167], v11 offset:53248\n"
"buffer_load_dwordx4 v38, s[16:19], s72 offen offset:2048 lds\n"
"v_lshl_add_u32 v87, v65, 3, v87\n"
"v_accvgpr_write_b32 a198, v87\n"
"v_lshl_add_u32 v61, v86, 1, v62\n"
"v_mfma_f32_32x32x16_bf16 v[112:127], a[36:39], a[180:183], v[112:127]\n"
"ds_read_b128 a[168:171], v12 offset:40960\n"
"v_and_b32 v64, 15, v2\n"
"v_mul_u32_u24 v45, 80, v64\n"
"v_lshl_add_u32 v45, v61, 4, v45\n"
"v_add_u32 v45, s60, v45\n"
"v_mul_u32_u24 v65, 144, v64\n"
"v_mfma_f32_32x32x16_bf16 v[128:143], a[36:39], a[184:187], v[128:143]\n"
"s_waitcnt lgkmcnt(2)\n"
"ds_read_b128 a[172:175], v12 offset:45056\n"
"buffer_load_dwordx4 v39, s[16:19], s72 offen offset:3072 lds\n"
"s_add_u32 m0, s57, 135168\n"
"s_lshl_b32 s60, s52, 5\n"
"v_mfma_f32_16x16x32_bf16 a[248:251], a[252:255], a[188:191], a[248:251]\n"
"ds_read_b128 a[176:179], v226 offset:40960\n"
"s_add_u32 s60, s60, 143360\n"
"v_mfma_f32_32x32x16_bf16 v[144:159], a[36:39], a[160:163], v[144:159]\n"
"s_waitcnt lgkmcnt(2)\n"
"ds_read_b128 a[180:183], v12 offset:49152\n"
"buffer_load_dwordx4 v30, s[8:11], s73 offen offset:0 lds\n"
"v_lshl_add_u32 v74, v61, 3, v65\n"
"v_add_u32 v74, s60, v74\n"
"s_mul_i32 s60, s52, 2304\n"
"v_mfma_f32_32x32x16_bf16 v[160:175], a[36:39], a[164:167], v[160:175]\n"
"ds_read_b128 a[184:187], v12 offset:53248\n"
"s_add_u32 s60, s60, 143360\n"
"v_lshl_add_u32 v75, v61, 5, v65\n"
"v_add_u32 v75, s60, v75\n"
"v_cvt_f32_u32 v53, v62\n"
"s_mul_i32 s5, s54, 10\n"
"v_mfma_f32_32x32x16_bf16 v[112:127], a[40:43], a[168:171], v[112:127]\n"
"s_waitcnt lgkmcnt(2)\n"
"ds_read_b128 a[188:191], v13 offset:40960\n"
"buffer_load_dwordx4 v31, s[8:11], s73 offen offset:1024 lds\n"
"s_and_b32 s78, s54, 1\n"
"s_xor_b32 s79, s78, 1\n"
"s_lshl_b32 s79, s79, 3\n"
"v_mfma_f32_32x32x16_bf16 v[128:143], a[40:43], a[172:175], v[128:143]\n"
"s_waitcnt vmcnt(15)\n"
"ds_read_b128 a[48:51], v26 offset:0\n"
"ds_read_b128 a[52:55], v27 offset:0\n"
"ds_read_b128 a[160:163], v13 offset:45056\n"
"s_add_u32 s6, s5, s79\n"
"s_lshl_b32 s79, s78, 1\n"
"s_add_u32 s5, s5, s79\n"
"v_mfma_f32_16x16x32_bf16 a[248:251], a[252:255], a[176:179], a[248:251]\n"
"s_waitcnt lgkmcnt(4)\n"
"ds_read_b128 a[164:167], v227 offset:40960\n"
"buffer_load_dwordx4 v32, s[8:11], s73 offen offset:2048 lds\n"
"v_mfma_f32_32x32x16_bf16 v[144:159], a[40:43], a[180:183], v[144:159]\n"
"ds_read_b128 a[56:59], v28 offset:0\n"
"ds_read_b128 a[60:63], v29 offset:0\n"
"ds_read_b128 a[168:171], v13 offset:49152\n"
"v_and_b32 v62, 7, v2\n"
"v_xor_b32 v61, v63, v62\n"
"v_lshlrev_b32 v61, 4, v61\n"
"v_mfma_f32_32x32x16_bf16 v[160:175], a[40:43], a[184:187], v[160:175]\n"
"s_waitcnt vmcnt(11)\n"
"s_barrier\n"
"s_waitcnt lgkmcnt(4)\n"
"ds_read_b128 a[172:175], v13 offset:53248\n"
"buffer_load_dwordx4 v33, s[8:11], s73 offen offset:3072 lds\n"
"s_add_u32 m0, s57, 0\n"
"v_mfma_f32_32x32x16_bf16 v[112:127], a[44:47], a[188:191], v[112:127]\n"
"ds_read_b128 a[176:179], v14 offset:0\n"
"v_lshrrev_b32 v62, 3, v2\n"
"v_mul_u32_u24 v62, 1280, v62\n"
"s_mul_i32 s60, s52, 5\n"
"s_mul_i32 s62, s55, 20\n"
"s_add_u32 s78, s60, 0\n"
"v_mfma_f32_32x32x16_bf16 v[128:143], a[44:47], a[160:163], v[128:143]\n"
"s_waitcnt lgkmcnt(2)\n"
"ds_read_b128 a[180:183], v14 offset:4096\n"
"buffer_load_dwordx4 v38, s[12:15], s73 offen offset:0 lds\n"
"s_lshl_b32 s79, s54, 2\n"
"s_add_u32 s79, s79, s78\n"
"s_and_b32 s79, s79, 15\n"
"v_mfma_f32_16x16x32_bf16 a[248:251], a[252:255], a[164:167], a[248:251]\n"
"ds_read_b128 a[184:187], v14 offset:8192\n"
"s_cmp_ge_u32 s78, 16\n"
"s_cselect_b32 s79, s78, s79\n"
"v_mfma_f32_32x32x16_bf16 v[144:159], a[44:47], a[168:171], v[144:159]\n"
"s_waitcnt lgkmcnt(2)\n"
"ds_read_b128 a[188:191], v14 offset:12288\n"
"buffer_load_dwordx4 v39, s[12:15], s73 offen offset:1024 lds\n"
"s_add_u32 s79, s79, s62\n"
"s_mul_i32 s79, s79, 10240\n"
"s_add_u32 s79, s79, 3072\n"
"v_mfma_f32_32x32x16_bf16 v[160:175], a[44:47], a[172:175], v[160:175]\n"
"ds_read_b128 a[160:163], v15 offset:0\n"
"s_and_b32 s78, s78, 1\n"
"s_lshl_b32 s78, s78, 6\n"
"v_accvgpr_read_b32 v82, a248\n"
"v_cmp_gt_u32 s[0:1], 32, v2\n"
"v_and_b32 v84, 31, v2\n"
"v_mfma_f32_32x32x16_bf16 v[112:127], a[48:51], a[176:179], v[112:127]\n"
"s_waitcnt lgkmcnt(2)\n"
"ds_read_b128 a[164:167], v15 offset:4096\n"
"buffer_load_dwordx4 v38, s[16:19], s73 offen offset:2048 lds\n"
"v_lshlrev_b32 v84, 4, v84\n"
"s_lshl_b32 s2, s52, 10\n"
"v_add_u32 v85, s2, v84\n"
"v_mfma_f32_32x32x16_bf16 v[128:143], a[48:51], a[180:183], v[128:143]\n"
"ds_read_b128 a[168:171], v15 offset:8192\n"
"v_mov_b32 v84, 0x70000000\n"
"v_cvt_pk_bf16_f32 v83, v82, v82\n"
"v_cndmask_b32 v84, v84, v85, s[0:1]\n"
"v_and_b32 v83, s100, v83\n"
"s_lshl_b32 s3, s54, 2\n"
"v_mfma_f32_32x32x16_bf16 v[144:159], a[48:51], a[184:187], v[144:159]\n"
"s_waitcnt lgkmcnt(2)\n"
"ds_read_b128 a[172:175], v15 offset:12288\n"
"s_add_u32 s3, s3, s55\n"
"s_mul_i32 s3, s3, 10240\n"
"s_add_u32 s3, s3, 0x1680000\n"
"buffer_load_dwordx4 v39, s[16:19], s73 offen offset:3072 lds\n"
"v_mfma_f32_32x32x16_bf16 v[160:175], a[48:51], a[188:191], v[160:175]\n"
"ds_read_b128 a[176:179], v16 offset:0\n"
"s_add_u32 m0, s57, 102400\n"
"v_sub_f32 v82, v82, v83\n"
"v_cvt_pk_bf16_f32 v82, v83, v82\n"
"v_mov_b32 v83, s56\n"
"s_lshr_b32 s2, s101, 1\n"
"v_mfma_f32_32x32x16_bf16 v[112:127], a[52:55], a[160:163], v[112:127]\n"
"s_waitcnt lgkmcnt(2)\n"
"ds_read_b128 a[180:183], v16 offset:4096\n"
"s_lshl_b32 s2, s2, 9\n"
"s_and_b32 s7, s101, 1\n"
"s_lshl_b32 s7, s7, 3\n"
"s_add_u32 s2, s2, s7\n"
"buffer_load_dwordx4 v30, s[8:11], s74 offen offset:0 lds\n"
"v_mfma_f32_32x32x16_bf16 v[128:143], a[52:55], a[164:167], v[128:143]\n"
"ds_read_b128 a[184:187], v16 offset:8192\n"
"s_add_u32 s2, s2, s3\n"
"s_cmp_lt_u32 s101, 4\n"
"s_cselect_b32 s2, s2, 0x60000000\n"
"buffer_store_dwordx2 v[82:83], v84, s[40:43], s2 offen sc1\n"
"v_mfma_f32_32x32x16_bf16 v[144:159], a[52:55], a[168:171], v[144:159]\n"
"s_waitcnt lgkmcnt(2)\n"
"ds_read_b128 a[188:191], v16 offset:12288\n"
"buffer_load_dwordx4 v31, s[8:11], s74 offen offset:1024 lds\n"
"v_xor_b32 v64, s78, v61\n"
"v_add3_u32 v64, v64, v62, s79\n"
"v_accvgpr_write_b32 a192, v64\n"
"v_mfma_f32_32x32x16_bf16 v[160:175], a[52:55], a[172:175], v[160:175]\n"
"ds_read_b128 a[160:163], v17 offset:0\n"
"s_add_u32 s78, s60, 1\n"
"s_lshl_b32 s79, s54, 2\n"
"s_add_u32 s79, s79, s78\n"
"s_and_b32 s79, s79, 15\n"
"s_cmp_ge_u32 s78, 16\n"
"s_cselect_b32 s79, s78, s79\n"
"v_mfma_f32_32x32x16_bf16 v[112:127], a[56:59], a[176:179], v[112:127]\n"
"s_waitcnt lgkmcnt(2)\n"
"s_waitcnt vmcnt(15)\n"
"ds_read_b128 a[64:67], v26 offset:16384\n"
"ds_read_b128 a[68:71], v27 offset:16384\n"
"ds_read_b128 a[164:167], v17 offset:4096\n"
"buffer_load_dwordx4 v32, s[8:11], s74 offen offset:2048 lds\n"
"v_mfma_f32_32x32x16_bf16 v[128:143], a[56:59], a[180:183], v[128:143]\n"
"ds_read_b128 a[72:75], v28 offset:16384\n"
"ds_read_b128 a[76:79], v29 offset:16384\n"
"ds_read_b128 a[168:171], v17 offset:8192\n"
"s_add_u32 s79, s79, s62\n"
"s_mul_i32 s79, s79, 10240\n"
"s_add_u32 s79, s79, 2048\n"
"v_mfma_f32_32x32x16_bf16 v[144:159], a[56:59], a[184:187], v[144:159]\n"
"s_waitcnt vmcnt(12)\n"
"s_barrier\n"
"s_waitcnt lgkmcnt(6)\n"
"ds_read_b128 a[172:175], v17 offset:12288\n"
"buffer_load_dwordx4 v33, s[8:11], s74 offen offset:3072 lds\n"
"s_add_u32 m0, s57, 20480\n"
"v_mfma_f32_32x32x16_bf16 v[160:175], a[56:59], a[188:191], v[160:175]\n"
"ds_read_b128 a[176:179], v14 offset:20480\n"
"s_and_b32 s78, s78, 1\n"
"s_lshl_b32 s78, s78, 6\n"
"v_xor_b32 v64, s78, v61\n"
"v_add3_u32 v64, v64, v62, s79\n"
"v_accvgpr_write_b32 a193, v64\n"
"v_mfma_f32_32x32x16_bf16 v[112:127], a[60:63], a[160:163], v[112:127]\n"
"s_waitcnt lgkmcnt(2)\n"
"ds_read_b128 a[180:183], v14 offset:24576\n"
"buffer_load_dwordx4 v38, s[12:15], s74 offen offset:0 lds\n"
"s_add_u32 s78, s60, 2\n"
"s_lshl_b32 s79, s54, 2\n"
"s_add_u32 s79, s79, s78\n"
"v_mfma_f32_32x32x16_bf16 v[128:143], a[60:63], a[164:167], v[128:143]\n"
"ds_read_b128 a[184:187], v14 offset:28672\n"
"s_and_b32 s79, s79, 15\n"
"s_cmp_ge_u32 s78, 16\n"
"s_cselect_b32 s79, s78, s79\n"
"s_add_u32 s79, s79, s62\n"
"s_mul_i32 s79, s79, 10240\n"
"v_mfma_f32_32x32x16_bf16 v[144:159], a[60:63], a[168:171], v[144:159]\n"
"s_waitcnt lgkmcnt(2)\n"
"ds_read_b128 a[188:191], v14 offset:32768\n"
"buffer_load_dwordx4 v39, s[12:15], s74 offen offset:1024 lds\n"
"s_add_u32 s79, s79, 1024\n"
"s_and_b32 s78, s78, 1\n"
"s_lshl_b32 s78, s78, 6\n"
"v_mfma_f32_32x32x16_bf16 v[160:175], a[60:63], a[172:175], v[160:175]\n"
"ds_read_b128 a[160:163], v15 offset:20480\n"
"v_xor_b32 v64, s78, v61\n"
"v_add3_u32 v64, v64, v62, s79\n"
"v_accvgpr_write_b32 a194, v64\n"
"s_add_u32 s78, s60, 3\n"
"s_lshl_b32 s79, s54, 2\n"
"v_mfma_f32_32x32x16_bf16 v[112:127], a[64:67], a[176:179], v[112:127]\n"
"s_waitcnt lgkmcnt(2)\n"
"ds_read_b128 a[164:167], v15 offset:24576\n"
"buffer_load_dwordx4 v38, s[16:19], s74 offen offset:2048 lds\n"
"s_add_u32 s79, s79, s78\n"
"s_and_b32 s79, s79, 15\n"
"s_cmp_ge_u32 s78, 16\n"
"s_cselect_b32 s79, s78, s79\n"
"v_mfma_f32_32x32x16_bf16 v[128:143], a[64:67], a[180:183], v[128:143]\n"
"ds_read_b128 a[168:171], v15 offset:28672\n"
"s_add_u32 s79, s79, s62\n"
"s_mul_i32 s79, s79, 10240\n"
"s_add_u32 s79, s79, 0\n"
"s_and_b32 s78, s78, 1\n"
"s_lshl_b32 s78, s78, 6\n"
"v_mfma_f32_32x32x16_bf16 v[144:159], a[64:67], a[184:187], v[144:159]\n"
"s_waitcnt lgkmcnt(2)\n"
"ds_read_b128 a[172:175], v15 offset:32768\n"
"buffer_load_dwordx4 v39, s[16:19], s74 offen offset:3072 lds\n"
"s_add_u32 m0, s57, 118784\n"
"v_xor_b32 v64, s78, v61\n"
"v_mfma_f32_32x32x16_bf16 v[160:175], a[64:67], a[188:191], v[160:175]\n"
"ds_read_b128 a[176:179], v16 offset:20480\n"
"v_add3_u32 v64, v64, v62, s79\n"
"v_accvgpr_write_b32 a195, v64\n"
"s_add_u32 s78, s60, 4\n"
"s_lshl_b32 s79, s54, 2\n"
"s_add_u32 s79, s79, s78\n"
"v_mfma_f32_32x32x16_bf16 v[112:127], a[68:71], a[160:163], v[112:127]\n"
"s_waitcnt lgkmcnt(2)\n"
"ds_read_b128 a[180:183], v16 offset:24576\n"
"buffer_load_dwordx4 v30, s[8:11], s75 offen offset:0 lds\n"
"s_and_b32 s79, s79, 15\n"
"s_cmp_ge_u32 s78, 16\n"
"s_cselect_b32 s79, s78, s79\n"
"v_mfma_f32_32x32x16_bf16 v[128:143], a[68:71], a[164:167], v[128:143]\n"
"ds_read_b128 a[184:187], v16 offset:28672\n"
"s_add_u32 s79, s79, s62\n"
"s_mul_i32 s79, s79, 10240\n"
"s_add_u32 s79, s79, 3072\n"
"s_and_b32 s78, s78, 1\n"
"s_lshl_b32 s78, s78, 6\n"
"v_mfma_f32_32x32x16_bf16 v[144:159], a[68:71], a[168:171], v[144:159]\n"
"s_waitcnt lgkmcnt(2)\n"
"ds_read_b128 a[188:191], v16 offset:32768\n"
"buffer_load_dwordx4 v31, s[8:11], s75 offen offset:1024 lds\n"
"v_xor_b32 v64, s78, v61\n"
"v_add3_u32 v64, v64, v62, s79\n"
"v_accvgpr_write_b32 a196, v64\n"
"v_mfma_f32_32x32x16_bf16 v[160:175], a[68:71], a[172:175], v[160:175]\n"
"ds_read_b128 a[160:163], v17 offset:20480\n"
"s_mul_i32 s78, s52, 20480\n"
"s_mul_i32 s79, s55, 320\n"
"s_add_u32 s78, s78, s79\n"
"s_lshl_b32 s79, s54, 2\n"
"v_add_u32 v61, 0, v2\n"
"v_mfma_f32_32x32x16_bf16 v[112:127], a[72:75], a[176:179], v[112:127]\n"
"s_waitcnt lgkmcnt(2)\n"
"s_waitcnt vmcnt(15)\n"
"ds_read_b128 a[80:83], v26 offset:32768\n"
"ds_read_b128 a[84:87], v27 offset:32768\n"
"ds_read_b128 a[164:167], v17 offset:24576\n"
"buffer_load_dwordx4 v32, s[8:11], s75 offen offset:2048 lds\n"
"v_mfma_f32_32x32x16_bf16 v[128:143], a[72:75], a[180:183], v[128:143]\n"
"ds_read_b128 a[88:91], v28 offset:32768\n"
"ds_read_b128 a[92:95], v29 offset:32768\n"
"ds_read_b128 a[168:171], v17 offset:28672\n"
"v_mul_u32_u24 v64, 3277, v61\n"
"v_lshrrev_b32 v64, 16, v64\n"
"v_mul_u32_u24 v62, 20, v64\n"
"v_mfma_f32_32x32x16_bf16 v[144:159], a[72:75], a[184:187], v[144:159]\n"
"s_waitcnt vmcnt(12)\n"
"s_barrier\n"
"s_waitcnt lgkmcnt(6)\n"
"ds_read_b128 a[172:175], v17 offset:32768\n"
"buffer_load_dwordx4 v33, s[8:11], s75 offen offset:3072 lds\n"
"s_add_u32 m0, s57, 40960\n"
"v_mfma_f32_32x32x16_bf16 v[160:175], a[72:75], a[188:191], v[160:175]\n"
"ds_read_b128 a[176:179], v10 offset:0\n"
"v_sub_u32 v61, v61, v62\n"
"v_add_u32 v62, s79, v61\n"
"v_cmp_gt_u32 vcc, 16, v61\n"
"v_and_b32 v62, 15, v62\n"
"v_mul_u32_u24 v64, 1280, v64\n"
"v_mfma_f32_32x32x16_bf16 v[112:127], a[76:79], a[160:163], v[112:127]\n"
"s_waitcnt lgkmcnt(2)\n"
"ds_read_b128 a[180:183], v10 offset:4096\n"
"buffer_load_dwordx4 v38, s[12:15], s75 offen offset:0 lds\n"
"v_cndmask_b32 v61, v61, v62, vcc\n"
"s_sub_u32 s60, s78, 0\n"
"v_lshlrev_b32 v61, 4, v61\n"
"v_mfma_f32_32x32x16_bf16 v[128:143], a[76:79], a[164:167], v[128:143]\n"
"ds_read_b128 a[184:187], v10 offset:8192\n"
"v_add3_u32 v64, v64, v61, s60\n"
"v_accvgpr_write_b32 a197, v64\n"
"v_add_u32 v61, 64, v2\n"
"v_mul_u32_u24 v40, 3277, v61\n"
"v_lshrrev_b32 v40, 16, v40\n"
"v_mfma_f32_32x32x16_bf16 v[144:159], a[76:79], a[168:171], v[144:159]\n"
"s_waitcnt lgkmcnt(2)\n"
"ds_read_b128 a[188:191], v10 offset:12288\n"
"buffer_load_dwordx4 v39, s[12:15], s75 offen offset:1024 lds\n"
"v_mul_u32_u24 v62, 20, v40\n"
"v_sub_u32 v61, v61, v62\n"
"v_add_u32 v62, s79, v61\n"
"v_mfma_f32_32x32x16_bf16 v[160:175], a[76:79], a[172:175], v[160:175]\n"
"ds_read_b128 a[160:163], v11 offset:0\n"
"v_cmp_gt_u32 vcc, 16, v61\n"
"v_and_b32 v62, 15, v62\n"
"v_mul_u32_u24 v40, 1280, v40\n"
"v_cndmask_b32 v61, v61, v62, vcc\n"
"s_sub_u32 s60, s78, 1024\n"
"v_mfma_f32_32x32x16_bf16 v[112:127], a[80:83], a[176:179], v[112:127]\n"
"s_waitcnt lgkmcnt(2)\n"
"ds_read_b128 a[164:167], v11 offset:4096\n"
"s_lshl_b32 s7, s53, 2\n"
"s_add_u32 s7, s7, s52\n"
"s_lshl_b32 s7, s7, 8\n"
"s_add_u32 s7, s7, 0x1700000\n"
"buffer_load_dwordx4 v38, s[16:19], s75 offen offset:2048 lds\n"
"v_mfma_f32_32x32x16_bf16 v[128:143], a[80:83], a[180:183], v[128:143]\n"
"ds_read_b128 a[168:171], v11 offset:8192\n"
"buffer_load_dwordx2 v[58:59], v60, s[40:43], s7 offen sc1\n"
"v_lshlrev_b32 v61, 4, v61\n"
"v_add3_u32 v40, v40, v61, s60\n"
"v_add_u32 v61, 128, v2\n"
"v_mfma_f32_32x32x16_bf16 v[144:159], a[80:83], a[184:187], v[144:159]\n"
"s_waitcnt lgkmcnt(2)\n"
"ds_read_b128 a[172:175], v11 offset:12288\n"
"buffer_load_dwordx4 v39, s[16:19], s75 offen offset:3072 lds\n"
"s_add_u32 m0, s57, 135168\n"
"v_mul_u32_u24 v41, 3277, v61\n"
"v_mfma_f32_32x32x16_bf16 v[160:175], a[80:83], a[188:191], v[160:175]\n"
"ds_read_b128 a[176:179], v12 offset:0\n"
"v_lshrrev_b32 v41, 16, v41\n"
"v_mul_u32_u24 v62, 20, v41\n"
"v_sub_u32 v61, v61, v62\n"
"v_add_u32 v62, s79, v61\n"
"v_cmp_gt_u32 vcc, 16, v61\n"
"v_mfma_f32_32x32x16_bf16 v[112:127], a[84:87], a[160:163], v[112:127]\n"
"s_waitcnt lgkmcnt(2)\n"
"ds_read_b128 a[180:183], v12 offset:4096\n"
"buffer_load_dwordx4 v30, s[8:11], s76 offen offset:0 lds\n"
"v_and_b32 v62, 15, v62\n"
"v_mul_u32_u24 v41, 1280, v41\n"
"v_cndmask_b32 v61, v61, v62, vcc\n"
"v_mfma_f32_32x32x16_bf16 v[128:143], a[84:87], a[164:167], v[128:143]\n"
"ds_read_b128 a[184:187], v12 offset:8192\n"
"s_sub_u32 s60, s78, 2048\n"
"v_lshlrev_b32 v61, 4, v61\n"
"v_add3_u32 v41, v41, v61, s60\n"
"v_add_u32 v61, 192, v2\n"
"v_mul_u32_u24 v42, 3277, v61\n"
"v_mfma_f32_32x32x16_bf16 v[144:159], a[84:87], a[168:171], v[144:159]\n"
"s_waitcnt lgkmcnt(2)\n"
"ds_read_b128 a[188:191], v12 offset:12288\n"
"buffer_load_dwordx4 v31, s[8:11], s76 offen offset:1024 lds\n"
"v_lshrrev_b32 v42, 16, v42\n"
"v_mul_u32_u24 v62, 20, v42\n"
"v_sub_u32 v61, v61, v62\n"
"v_mfma_f32_32x32x16_bf16 v[160:175], a[84:87], a[172:175], v[160:175]\n"
"ds_read_b128 a[160:163], v13 offset:0\n"
"v_add_u32 v62, s79, v61\n"
"v_cmp_gt_u32 vcc, 16, v61\n"
"v_and_b32 v62, 15, v62\n"
"v_mul_u32_u24 v42, 1280, v42\n"
"v_cndmask_b32 v61, v61, v62, vcc\n"
"v_mfma_f32_32x32x16_bf16 v[112:127], a[88:91], a[176:179], v[112:127]\n"
"s_waitcnt lgkmcnt(2)\n"
"s_waitcnt vmcnt(15)\n"
"ds_read_b128 a[96:99], v26 offset:0\n"
"ds_read_b128 a[100:103], v27 offset:0\n"
"ds_read_b128 a[164:167], v13 offset:4096\n"
"buffer_load_dwordx4 v32, s[8:11], s76 offen offset:2048 lds\n"
"v_mfma_f32_32x32x16_bf16 v[128:143], a[88:91], a[180:183], v[128:143]\n"
"ds_read_b128 a[104:107], v28 offset:0\n"
"ds_read_b128 a[108:111], v29 offset:0\n"
"ds_read_b128 a[168:171], v13 offset:8192\n"
"s_sub_u32 s60, s78, 3072\n"
"v_lshlrev_b32 v61, 4, v61\n"
"v_add3_u32 v42, v42, v61, s60\n"
"v_mfma_f32_32x32x16_bf16 v[144:159], a[88:91], a[184:187], v[144:159]\n"
"s_waitcnt vmcnt(12)\n"
"s_barrier\n"
"s_waitcnt lgkmcnt(6)\n"
"ds_read_b128 a[172:175], v13 offset:12288\n"
"buffer_load_dwordx4 v33, s[8:11], s76 offen offset:3072 lds\n"
"s_add_u32 m0, s57, 61440\n"
"v_mfma_f32_32x32x16_bf16 v[160:175], a[88:91], a[188:191], v[160:175]\n"
"ds_read_b128 a[176:179], v10 offset:20480\n"
"v_add_u32 v61, 256, v2\n"
"v_mul_u32_u24 v43, 3277, v61\n"
"v_lshrrev_b32 v43, 16, v43\n"
"v_mul_u32_u24 v62, 20, v43\n"
"v_sub_u32 v61, v61, v62\n"
"v_mfma_f32_32x32x16_bf16 v[112:127], a[92:95], a[160:163], v[112:127]\n"
"s_waitcnt lgkmcnt(2)\n"
"ds_read_b128 a[180:183], v10 offset:24576\n"
"buffer_load_dwordx4 v38, s[12:15], s76 offen offset:0 lds\n"
"v_add_u32 v62, s79, v61\n"
"v_cmp_gt_u32 vcc, 16, v61\n"
"v_and_b32 v62, 15, v62\n"
"v_mfma_f32_32x32x16_bf16 v[128:143], a[92:95], a[164:167], v[128:143]\n"
"ds_read_b128 a[184:187], v10 offset:28672\n"
"v_mul_u32_u24 v43, 1280, v43\n"
"v_cndmask_b32 v61, v61, v62, vcc\n"
"s_sub_u32 s60, s78, 0\n"
"v_lshlrev_b32 v61, 4, v61\n"
"v_add3_u32 v43, v43, v61, s60\n"
"v_mfma_f32_32x32x16_bf16 v[144:159], a[92:95], a[168:171], v[144:159]\n"
"s_waitcnt lgkmcnt(2)\n"
"ds_read_b128 a[188:191], v10 offset:32768\n"
"buffer_load_dwordx4 v39, s[12:15], s76 offen offset:1024 lds\n"
"s_lshl_b32 s4, s53, 2\n"
"s_add_u32 s4, s4, s54\n"
"s_lshl_b32 s4, s4, 2\n"
"v_mfma_f32_32x32x16_bf16 v[160:175], a[92:95], a[172:175], v[160:175]\n"
"ds_read_b128 a[160:163], v11 offset:20480\n"
"s_add_u32 s4, s4, s52\n"
"s_mul_i32 s4, s4, 384\n"
"s_add_u32 s4, s4, 0x1780000\n"
"v_cmp_gt_u32 vcc, 4, v2\n"
"v_mov_b32 v61, 0x70000000\n"
"v_mfma_f32_32x32x16_bf16 v[112:127], a[96:99], a[176:179], v[112:127]\n"
"s_waitcnt lgkmcnt(2)\n"
"ds_read_b128 a[164:167], v11 offset:24576\n"
"buffer_load_dwordx4 v38, s[16:19], s76 offen offset:2048 lds\n"
"v_lshlrev_b32 v1, 2, v2\n"
"v_cndmask_b32 v1, v61, v1, vcc\n"
"v_cmp_eq_u32 vcc, s55, v2\n"
"v_mfma_f32_32x32x16_bf16 v[128:143], a[96:99], a[180:183], v[128:143]\n"
"ds_read_b128 a[168:171], v11 offset:28672\n"
"v_nop\n"
"v_nop\n"
"v_cndmask_b32 v4, v61, v1, vcc\n"
"v_lshrrev_b32 v62, 5, v2\n"
"v_bfe_u32 v64, v2, 4, 1\n"
"v_mfma_f32_32x32x16_bf16 v[144:159], a[96:99], a[184:187], v[144:159]\n"
"s_waitcnt lgkmcnt(2)\n"
"ds_read_b128 a[172:175], v11 offset:32768\n"
"buffer_load_dwordx4 v39, s[16:19], s76 offen offset:3072 lds\n"
"s_add_u32 m0, s57, 102400\n"
"v_lshl_add_u32 v62, v64, 1, v62\n"
"v_mfma_f32_32x32x16_bf16 v[160:175], a[96:99], a[188:191], v[160:175]\n"
"ds_read_b128 a[176:179], v12 offset:20480\n"
"v_lshlrev_b32 v62, 2, v62\n"
"v_sub_u32 v62, s53, v62\n"
"v_and_b32 v64, 15, v2\n"
"v_lshlrev_b32 v64, 4, v64\n"
"s_movk_i32 s62, 10240\n"
"v_mfma_f32_32x32x16_bf16 v[112:127], a[100:103], a[160:163], v[112:127]\n"
"s_waitcnt lgkmcnt(2)\n"
"ds_read_b128 a[180:183], v12 offset:24576\n"
"buffer_load_dwordx4 v30, s[8:11], s77 offen offset:0 lds\n"
"v_add_u32 v62, -1, v62\n"
"v_cmp_le_i32 vcc, 0, v62\n"
"v_mad_u32_u24 v65, v62, s62, v64\n"
"v_mfma_f32_32x32x16_bf16 v[128:143], a[100:103], a[164:167], v[128:143]\n"
"ds_read_b128 a[184:187], v12 offset:28672\n"
"v_nop\n"
"v_cndmask_b32 v65, v61, v65, vcc\n"
"v_cndmask_b32 v86, 0, v7, vcc\n"
"v_accvgpr_write_b32 a208, v65\n"
"v_accvgpr_write_b32 a212, v86\n"
"v_mfma_f32_32x32x16_bf16 v[144:159], a[100:103], a[168:171], v[144:159]\n"
"s_waitcnt lgkmcnt(2)\n"
"ds_read_b128 a[188:191], v12 offset:32768\n"
"buffer_load_dwordx4 v31, s[8:11], s77 offen offset:1024 lds\n"
"v_add_u32 v62, -1, v62\n"
"v_cmp_le_i32 vcc, 0, v62\n"
"v_mad_u32_u24 v65, v62, s62, v64\n"
"v_mfma_f32_32x32x16_bf16 v[160:175], a[100:103], a[172:175], v[160:175]\n"
"ds_read_b128 a[160:163], v13 offset:20480\n"
"v_nop\n"
"v_cndmask_b32 v65, v61, v65, vcc\n"
"v_cndmask_b32 v86, 0, v7, vcc\n"
"v_accvgpr_write_b32 a209, v65\n"
"v_accvgpr_write_b32 a213, v86\n"
"v_mfma_f32_32x32x16_bf16 v[112:127], a[104:107], a[176:179], v[112:127]\n"
"s_waitcnt lgkmcnt(2)\n"
"s_waitcnt vmcnt(15)\n"
"ds_read_b128 a[112:115], v26 offset:16384\n"
"ds_read_b128 a[116:119], v27 offset:16384\n"
"ds_read_b128 a[164:167], v13 offset:24576\n"
"buffer_load_dwordx4 v32, s[8:11], s77 offen offset:2048 lds\n"
"v_mfma_f32_32x32x16_bf16 v[128:143], a[104:107], a[180:183], v[128:143]\n"
"ds_read_b128 a[120:123], v28 offset:16384\n"
"ds_read_b128 a[124:127], v29 offset:16384\n"
"ds_read_b128 a[168:171], v13 offset:28672\n"
"v_add_u32 v62, -1, v62\n"
"v_cmp_le_i32 vcc, 0, v62\n"
"v_mad_u32_u24 v65, v62, s62, v64\n"
"v_mfma_f32_32x32x16_bf16 v[144:159], a[104:107], a[184:187], v[144:159]\n"
"s_waitcnt vmcnt(11)\n"
"s_barrier\n"
"s_waitcnt lgkmcnt(6)\n"
"ds_read_b128 a[172:175], v13 offset:32768\n"
"buffer_load_dwordx4 v33, s[8:11], s77 offen offset:3072 lds\n"
"s_add_u32 m0, s57, 81920\n"
"v_mfma_f32_32x32x16_bf16 v[160:175], a[104:107], a[188:191], v[160:175]\n"
"ds_read_b128 a[176:179], v10 offset:40960\n"
"v_nop\n"
"v_cndmask_b32 v65, v61, v65, vcc\n"
"v_cndmask_b32 v86, 0, v7, vcc\n"
"v_accvgpr_write_b32 a210, v65\n"
"v_accvgpr_write_b32 a214, v86\n"
"v_mfma_f32_32x32x16_bf16 v[112:127], a[108:111], a[160:163], v[112:127]\n"
"s_waitcnt lgkmcnt(2)\n"
"ds_read_b128 a[180:183], v10 offset:45056\n"
"buffer_load_dwordx4 v38, s[12:15], s77 offen offset:0 lds\n"
"v_add_u32 v62, -1, v62\n"
"v_cmp_le_i32 vcc, 0, v62\n"
"v_mad_u32_u24 v65, v62, s62, v64\n"
"v_mfma_f32_32x32x16_bf16 v[128:143], a[108:111], a[164:167], v[128:143]\n"
"ds_read_b128 a[184:187], v10 offset:49152\n"
"v_cmp_ne_u32 s[0:1], s56, v59\n"
"s_nop 0\n"
"s_cmp_lg_u64 s[0:1], 0\n"
"s_cbranch_scc1 LstB_slow%=\n"
"LstB_ok%=:\n"
"v_and_b32 v59, s100, v58\n"
"v_lshlrev_b32 v58, 16, v58\n"
"v_mfma_f32_32x32x16_bf16 v[144:159], a[108:111], a[168:171], v[144:159]\n"
"s_waitcnt lgkmcnt(2)\n"
"ds_read_b128 a[188:191], v10 offset:53248\n"
"buffer_load_dwordx4 v39, s[12:15], s77 offen offset:1024 lds\n"
"v_mul_f32 v52, v59, v59\n"
"v_rcp_f32 v5, v59\n"
"v_mfma_f32_32x32x16_bf16 v[160:175], a[108:111], a[172:175], v[160:175]\n"
"ds_read_b128 a[160:163], v11 offset:40960\n"
"v_cvt_pk_bf16_f32 v54, -v58, -v58\n"
"v_mov_b32 v55, 0\n"
"v_mov_b32 v56, v54\n"
"v_mov_b32 v57, 0\n"
"v_mov_b32 v59, 0.5\n"
"v_mfma_f32_32x32x16_bf16 v[112:127], a[112:115], a[176:179], v[112:127]\n"
"s_waitcnt lgkmcnt(2)\n"
"ds_read_b128 a[164:167], v11 offset:45056\n"
"buffer_load_dwordx4 v38, s[16:19], s77 offen offset:2048 lds\n"
"v_mul_f32 v6, v58, v5\n"
"v_mul_f32 v50, 0xc0135761, v5\n"
"v_mul_f32 v51, v5, v5\n"
"v_mfma_f32_32x32x16_bf16 v[128:143], a[112:115], a[180:183], v[128:143]\n"
"ds_read_b128 a[168:171], v11 offset:49152\n"
"v_mul_f32 v51, v51, v50\n"
"v_mul_f32 v51, 0x3d372713, v51\n"
"s_lshl_b32 s3, s54, 2\n"
"s_add_u32 s3, s3, s55\n"
"s_mul_i32 s3, s3, 10240\n"
"v_mfma_f32_32x32x16_bf16 v[144:159], a[112:115], a[184:187], v[144:159]\n"
"s_waitcnt lgkmcnt(2)\n"
"ds_read_b128 a[172:175], v11 offset:53248\n"
"s_add_u32 s3, s3, 0x1680000\n"
"s_add_u32 s2, s3, 4096\n"
"buffer_load_dwordx4 a[224:227], v44, s[40:43], s3 offen offset:0 sc1\n"
"v_nop\n"
"v_mfma_f32_32x32x16_bf16 v[160:175], a[112:115], a[188:191], v[160:175]\n"
"ds_read_b128 a[176:179], v12 offset:40960\n"
"buffer_load_dwordx4 a[228:231], v44, s[40:43], s3 offen offset:1024 sc1\n"
"v_cndmask_b32 v65, v61, v65, vcc\n"
"v_cndmask_b32 v86, 0, v7, vcc\n"
"v_accvgpr_write_b32 a211, v65\n"
"v_mfma_f32_32x32x16_bf16 v[112:127], a[116:119], a[160:163], v[112:127]\n"
"s_waitcnt lgkmcnt(2)\n"
"ds_read_b128 a[180:183], v12 offset:45056\n"
"v_accvgpr_write_b32 a215, v86\n"
"v_lshlrev_b32 v109, 16, v109\n"
"v_lshlrev_b32 v107, 16, v107\n"
"v_lshlrev_b32 v108, 16, v108\n"
"v_mul_f32 v109, 0x3fb8aa3b, v109\n"
"v_mfma_f32_32x32x16_bf16 v[128:143], a[116:119], a[164:167], v[128:143]\n"
"ds_read_b128 a[184:187], v12 offset:49152\n"
"buffer_load_dwordx4 a[232:235], v44, s[40:43], s3 offen offset:2048 sc1\n"
"v_exp_f32 v109, v109\n"
"v_mul_f32 v110, v107, v107\n"
"v_mfma_f32_32x32x16_bf16 v[144:159], a[116:119], a[168:171], v[144:159]\n"
"s_waitcnt lgkmcnt(2)\n"
"ds_read_b128 a[188:191], v12 offset:53248\n"
"buffer_load_dwordx4 v39, s[16:19], s77 offen offset:3072 lds\n"
"s_mul_i32 s66, s68, 1536\n"
"s_add_u32 m0, s57, 0\n"
"v_fmac_f32 v110, v108, v108\n"
"v_mfma_f32_32x32x16_bf16 v[160:175], a[116:119], a[172:175], v[160:175]\n"
"ds_read_b128 a[160:163], v13 offset:40960\n"
"v_cvt_pk_bf16_f32 v109, v109, v109\n"
"v_rcp_f32 v110, v110\n"
"v_lshlrev_b32 v109, 16, v109\n"
"v_mul_f32 v90, v109, v107\n"
"v_mfma_f32_32x32x16_bf16 v[112:127], a[120:123], a[176:179], v[112:127]\n"
"s_waitcnt lgkmcnt(2)\n"
"s_waitcnt vmcnt(15)\n"
"ds_read_b128 a[128:131], v26 offset:32768\n"
"ds_read_b128 a[132:135], v27 offset:32768\n"
"ds_read_b128 a[164:167], v13 offset:45056\n"
"v_mul_f32 v91, v109, v108\n"
"v_mul_f32 v90, 0x3fb8aa3b, v90\n"
"v_mfma_f32_32x32x16_bf16 v[128:143], a[120:123], a[180:183], v[128:143]\n"
"ds_read_b128 a[136:139], v28 offset:32768\n"
"ds_read_b128 a[140:143], v29 offset:32768\n"
"ds_read_b128 a[168:171], v13 offset:49152\n"
"buffer_load_dwordx4 a[236:239], v44, s[40:43], s3 offen offset:3072 sc1\n"
"v_mul_f32 v91, 0.15915494, v91\n"
"v_mfma_f32_32x32x16_bf16 v[144:159], a[120:123], a[184:187], v[144:159]\n"
"s_waitcnt vmcnt(12)\n"
"s_barrier\n"
"s_waitcnt lgkmcnt(6)\n"
"ds_read_b128 a[172:175], v13 offset:53248\n"
"v_exp_f32 v92, v90\n"
"v_fract_f32 v91, v91\n"
"v_mfma_f32_32x32x16_bf16 v[160:175], a[120:123], a[188:191], v[160:175]\n"
"ds_read_b128 a[176:179], v14 offset:0\n"
"buffer_load_dwordx4 v34, s[20:23], s66 offen offset:0 lds\n"
"v_cos_f32 v90, v91\n"
"v_mfma_f32_32x32x16_bf16 v[112:127], a[124:127], a[160:163], v[112:127]\n"
"s_waitcnt lgkmcnt(2)\n"
"ds_read_b128 a[180:183], v14 offset:4096\n"
"v_sin_f32 v105, v91\n"
"v_add_f32 v110, v110, v110\n"
"v_mul_f32 v90, v90, v92\n"
"v_mul_f32 v91, v105, v92\n"
"v_mfma_f32_32x32x16_bf16 v[128:143], a[124:127], a[164:167], v[128:143]\n"
"ds_read_b128 a[184:187], v14 offset:8192\n"
"buffer_load_dwordx4 v35, s[20:23], s66 offen offset:1024 lds\n"
"v_add_f32 v111, -1.0, v90\n"
"v_mul_f32 v94, v111, v107\n"
"v_fmac_f32 v94, v91, v108\n"
"v_mfma_f32_32x32x16_bf16 v[144:159], a[124:127], a[168:171], v[144:159]\n"
"s_waitcnt lgkmcnt(2)\n"
"ds_read_b128 a[188:191], v14 offset:12288\n"
"v_mul_f32 v94, v94, v110\n"
"v_mul_f32 v95, v111, v108\n"
"v_fma_f32 v95, -v91, v107, v95\n"
"v_mul_f32 v95, v95, v110\n"
"v_mov_b32 v176, v90\n"
"v_mfma_f32_32x32x16_bf16 v[160:175], a[124:127], a[172:175], v[160:175]\n"
"ds_read_b128 a[160:163], v15 offset:0\n"
"buffer_load_dwordx4 v36, s[24:27], s66 offen offset:2048 lds\n"
"v_mov_b32 v184, v90\n"
"v_mov_b32 v177, v91\n"
"v_mov_b32 v185, v91\n"
"v_mfma_f32_32x32x16_bf16 v[112:127], a[128:131], a[176:179], v[112:127]\n"
"s_waitcnt lgkmcnt(2)\n"
"ds_read_b128 a[164:167], v15 offset:4096\n"
"v_mov_b32 v178, v94\n"
"v_mov_b32 v186, v94\n"
"v_mov_b32 v179, v95\n"
"v_mov_b32 v187, v95\n"
"v_permlane32_swap_b32 v176, v184\n"
"v_mfma_f32_32x32x16_bf16 v[128:143], a[128:131], a[180:183], v[128:143]\n"
"ds_read_b128 a[168:171], v15 offset:8192\n"
"buffer_load_dwordx4 v37, s[24:27], s66 offen offset:3072 lds\n"
"s_mul_i32 s66, s69, 1536\n"
"s_add_u32 m0, s57, 20480\n"
"v_permlane32_swap_b32 v177, v185\n"
"v_mfma_f32_32x32x16_bf16 v[144:159], a[128:131], a[184:187], v[144:159]\n"
"s_waitcnt lgkmcnt(2)\n"
"ds_read_b128 a[172:175], v15 offset:12288\n"
"v_permlane32_swap_b32 v178, v186\n"
"v_permlane32_swap_b32 v179, v187\n"
"v_mov_b32 v180, v176\n"
"v_mov_b32 v188, v184\n"
"v_mov_b32 v181, v177\n"
"v_mfma_f32_32x32x16_bf16 v[160:175], a[128:131], a[188:191], v[160:175]\n"
"ds_read_b128 a[176:179], v16 offset:0\n"
"buffer_load_dwordx4 v34, s[20:23], s66 offen offset:0 lds\n"
"v_mov_b32 v189, v185\n"
"v_mov_b32 v182, v178\n"
"v_mov_b32 v190, v186\n"
"v_mfma_f32_32x32x16_bf16 v[112:127], a[132:135], a[160:163], v[112:127]\n"
"s_waitcnt lgkmcnt(2)\n"
"ds_read_b128 a[180:183], v16 offset:4096\n"
"v_mov_b32 v183, v179\n"
"v_mov_b32 v191, v187\n"
"v_permlane16_swap_b32 v176, v180\n"
"v_permlane16_swap_b32 v184, v188\n"
"v_permlane16_swap_b32 v177, v181\n"
"v_mfma_f32_32x32x16_bf16 v[128:143], a[132:135], a[164:167], v[128:143]\n"
"ds_read_b128 a[184:187], v16 offset:8192\n"
"buffer_load_dwordx4 v35, s[20:23], s66 offen offset:1024 lds\n"
"v_permlane16_swap_b32 v185, v189\n"
"v_permlane16_swap_b32 v178, v182\n"
"v_permlane16_swap_b32 v186, v190\n"
"v_mfma_f32_32x32x16_bf16 v[144:159], a[132:135], a[168:171], v[144:159]\n"
"s_waitcnt lgkmcnt(2)\n"
"ds_read_b128 a[188:191], v16 offset:12288\n"
"v_permlane16_swap_b32 v179, v183\n"
"v_permlane16_swap_b32 v187, v191\n"
"v_mov_b32 v90, v176\n"
"v_mov_b32 v91, v177\n"
"s_cmp_eq_u32 s52, 1\n"
"s_cselect_b64 vcc, -1, 0\n"
"v_mfma_f32_32x32x16_bf16 v[160:175], a[132:135], a[172:175], v[160:175]\n"
"ds_read_b128 a[160:163], v17 offset:0\n"
"buffer_load_dwordx4 v36, s[24:27], s66 offen offset:2048 lds\n"
"v_cndmask_b32 v90, v90, v180, vcc\n"
"v_cndmask_b32 v91, v91, v181, vcc\n"
"s_cmp_eq_u32 s52, 2\n"
"s_cselect_b64 vcc, -1, 0\n"
"v_mfma_f32_32x32x16_bf16 v[112:127], a[136:139], a[176:179], v[112:127]\n"
"s_waitcnt lgkmcnt(2)\n"
"s_waitcnt vmcnt(15)\n"
"ds_read_b128 a[144:147], v26 offset:0\n"
"ds_read_b128 a[148:151], v27 offset:0\n"
"ds_read_b128 a[164:167], v17 offset:4096\n"
"v_cndmask_b32 v90, v90, v184, vcc\n"
"v_cndmask_b32 v91, v91, v185, vcc\n"
"v_mfma_f32_32x32x16_bf16 v[128:143], a[136:139], a[180:183], v[128:143]\n"
"ds_read_b128 a[152:155], v28 offset:0\n"
"ds_read_b128 a[156:159], v29 offset:0\n"
"ds_read_b128 a[168:171], v17 offset:8192\n"
"buffer_load_dwordx4 v37, s[24:27], s66 offen offset:3072 lds\n"
"s_cmp_eq_u32 s52, 3\n"
"s_cselect_b64 vcc, -1, 0\n"
"v_mfma_f32_32x32x16_bf16 v[144:159], a[136:139], a[184:187], v[144:159]\n"
"s_waitcnt vmcnt(9)\n"
"s_barrier\n"
"s_waitcnt lgkmcnt(6)\n"
"ds_read_b128 a[172:175], v17 offset:12288\n"
"s_mul_i32 s66, s70, 1536\n"
"s_add_u32 m0, s57, 40960\n"
"v_cndmask_b32 v90, v90, v188, vcc\n"
"v_mfma_f32_32x32x16_bf16 v[160:175], a[136:139], a[188:191], v[160:175]\n"
"ds_read_b128 a[176:179], v14 offset:20480\n"
"buffer_load_dwordx4 v34, s[20:23], s66 offen offset:0 lds\n"
"v_cndmask_b32 v91, v91, v189, vcc\n"
"v_mul_f32 v92, v90, v90\n"
"v_add_f32 v93, v90, v90\n"
"v_mfma_f32_32x32x16_bf16 v[112:127], a[140:143], a[160:163], v[112:127]\n"
"s_waitcnt lgkmcnt(2)\n"
"ds_read_b128 a[180:183], v14 offset:24576\n"
"v_fma_f32 v92, -v91, v91, v92\n"
"v_mul_f32 v93, v93, v91\n"
"v_mul_f32 v98, v92, v92\n"
"v_add_f32 v99, v92, v92\n"
"v_fma_f32 v98, -v93, v93, v98\n"
"v_mfma_f32_32x32x16_bf16 v[128:143], a[140:143], a[164:167], v[128:143]\n"
"ds_read_b128 a[184:187], v14 offset:28672\n"
"buffer_load_dwordx4 v35, s[20:23], s66 offen offset:1024 lds\n"
"v_mul_f32 v99, v99, v93\n"
"v_mul_f32 v66, v98, v98\n"
"v_add_f32 v67, v98, v98\n"
"v_mfma_f32_32x32x16_bf16 v[144:159], a[140:143], a[168:171], v[144:159]\n"
"s_waitcnt lgkmcnt(2)\n"
"ds_read_b128 a[188:191], v14 offset:32768\n"
"v_fma_f32 v66, -v99, v99, v66\n"
"v_mul_f32 v67, v67, v99\n"
"v_mul_f32 v98, v66, v66\n"
"v_add_f32 v99, v66, v66\n"
"v_fma_f32 v98, -v67, v67, v98\n"
"v_mfma_f32_32x32x16_bf16 v[160:175], a[140:143], a[172:175], v[160:175]\n"
"ds_read_b128 a[160:163], v15 offset:20480\n"
"buffer_load_dwordx4 v36, s[24:27], s66 offen offset:2048 lds\n"
"v_mul_f32 v99, v99, v67\n"
"v_mul_f32 v68, v98, v98\n"
"v_add_f32 v69, v98, v98\n"
"v_mfma_f32_32x32x16_bf16 v[112:127], a[144:147], a[176:179], v[112:127]\n"
"s_waitcnt lgkmcnt(2)\n"
"ds_read_b128 a[164:167], v15 offset:24576\n"
"v_fma_f32 v68, -v99, v99, v68\n"
"v_mul_f32 v69, v69, v99\n"
"v_mul_f32 v70, v68, v68\n"
"v_add_f32 v71, v68, v68\n"
"v_fma_f32 v70, -v69, v69, v70\n"
"v_mfma_f32_32x32x16_bf16 v[128:143], a[144:147], a[180:183], v[128:143]\n"
"buffer_load_dwordx4 v37, s[24:27], s66 offen offset:3072 lds\n"
"v_mul_f32 v71, v71, v69\n"
"v_mul_f32 v98, v70, v70\n"
"v_add_f32 v99, v70, v70\n"
"v_fma_f32 v98, -v71, v71, v98\n"
"v_mfma_f32_32x32x16_bf16 v[144:159], a[144:147], a[184:187], v[144:159]\n"
"s_waitcnt lgkmcnt(1)\n"
"v_mul_f32 v99, v99, v71\n"
"v_mul_f32 v104, v98, v98\n"
"v_add_f32 v105, v98, v98\n"
"v_fma_f32 v104, -v99, v99, v104\n"
"v_mul_f32 v105, v105, v99\n"
"v_mul_f32 v100, v104, v104\n"
"v_mfma_f32_32x32x16_bf16 v[160:175], a[144:147], a[188:191], v[160:175]\n"
"v_add_f32 v101, v104, v104\n"
"v_fma_f32 v100, -v105, v105, v100\n"
"v_mul_f32 v101, v101, v105\n"
"v_mul_f32 v102, v100, v100\n"
"v_add_f32 v103, v100, v100\n"
"v_fma_f32 v102, -v101, v101, v102\n"
"v_mfma_f32_32x32x16_bf16 v[112:127], a[148:151], a[160:163], v[112:127]\n"
"s_waitcnt lgkmcnt(0)\n"
"v_accvgpr_read_b32 v83, a225\n"
"v_accvgpr_read_b32 v84, a227\n"
"v_xor_b32 v83, s56, v83\n"
"v_xor_b32 v84, s56, v84\n"
"v_or_b32 v82, v83, v84\n"
"v_accvgpr_read_b32 v83, a229\n"
"v_mfma_f32_32x32x16_bf16 v[128:143], a[148:151], a[164:167], v[128:143]\n"
"v_accvgpr_read_b32 v84, a231\n"
"v_xor_b32 v83, s56, v83\n"
"v_xor_b32 v84, s56, v84\n"
"v_or3_b32 v82, v82, v83, v84\n"
"v_accvgpr_read_b32 v83, a233\n"
"v_accvgpr_read_b32 v84, a235\n"
"v_mul_f32 v103, v103, v101\n"
"v_cmp_le_u32 vcc, 32, v2\n"
"v_mov_b32 v108, 1.0\n"
"v_and_b32 v107, 16, v2\n"
"v_cndmask_b32 v72, v108, v68, vcc\n"
"v_cndmask_b32 v73, 0, v69, vcc\n"
"v_cmp_ne_u32 vcc, 0, v107\n"
"v_nop\n"
"v_nop\n"
"v_cndmask_b32 v108, v108, v70, vcc\n"
"v_cndmask_b32 v109, 0, v71, vcc\n"
"v_mul_f32 v104, v72, v108\n"
"v_mul_f32 v105, v72, v109\n"
"v_fma_f32 v104, -v73, v109, v104\n"
"v_fmac_f32 v105, v73, v108\n"
"v_cndmask_b32 v72, 0, v72, vcc\n"
"v_cndmask_b32 v73, 0, v73, vcc\n"
"v_accvgpr_write_b32 a200, v98\n"
"v_accvgpr_write_b32 a201, v99\n"
"v_accvgpr_write_b32 a202, v100\n"
"v_accvgpr_write_b32 a203, v101\n"
"v_accvgpr_write_b32 a204, v102\n"
"v_accvgpr_write_b32 a205, v103\n"
"v_accvgpr_write_b32 a206, v104\n"
"v_accvgpr_write_b32 a207, v105\n"
"v_mul_f32 v46, v176, v176\n"
"v_add_f32 v47, v176, v176\n"
"v_fma_f32 v46, -v177, v177, v46\n"
"v_mul_f32 v47, v47, v177\n"
"v_mul_f32 v48, v180, v180\n"
"v_add_f32 v49, v180, v180\n"
"v_fma_f32 v48, -v181, v181, v48\n"
"v_mul_f32 v49, v49, v181\n"
"v_mul_f32 v76, v184, v184\n"
"v_add_f32 v77, v184, v184\n"
"v_fma_f32 v76, -v185, v185, v76\n"
"v_mul_f32 v77, v77, v185\n"
"v_mul_f32 v78, v188, v188\n"
"v_add_f32 v79, v188, v188\n"
"v_fma_f32 v78, -v189, v189, v78\n"
"v_mul_f32 v79, v79, v189\n"
"v_xor_b32 v83, s56, v83\n"
"v_xor_b32 v84, s56, v84\n"
"v_or3_b32 v82, v82, v83, v84\n"
"s_waitcnt vmcnt(12)\n"
"v_accvgpr_read_b32 v83, a237\n"
"v_accvgpr_read_b32 v84, a239\n"
"v_xor_b32 v83, s56, v83\n"
"v_xor_b32 v84, s56, v84\n"
"v_or3_b32 v82, v82, v83, v84\n"
"v_cmp_ne_u32 s[0:1], 0, v82\n"
"s_nop 0\n"
"s_cmp_lg_u64 s[0:1], 0\n"
"s_cbranch_scc1 Lcs0B_slow%=\n"
"Lcs0B_ok%=:\n"
"v_accvgpr_write_b32 a225, 0\n"
"v_accvgpr_write_b32 a227, 0\n"
"v_accvgpr_write_b32 a229, 0\n"
"v_accvgpr_write_b32 a231, 0\n"
"v_accvgpr_write_b32 a233, 0\n"
"v_accvgpr_write_b32 a235, 0\n"
"v_accvgpr_write_b32 a237, 0\n"
"v_accvgpr_write_b32 a239, 0\n"
"v_mfma_f32_32x32x16_bf16 v[112:127], v[54:57], a[224:227], v[112:127]\n"
"ds_read_b128 a[168:171], v15 offset:28672\n"
"s_lshr_b32 s80, s98, 3\n"
"s_cmp_eq_u32 s52, 3\n"
"s_cselect_b64 s[16:17], s[36:37], s[32:33]\n"
"s_mul_i32 s81, s54, 320\n"
"v_accvgpr_read_b32 v30, a192\n"
"v_mfma_f32_32x32x16_bf16 v[128:143], v[54:57], a[228:231], v[128:143]\n"
"ds_read_b128 a[172:175], v15 offset:32768\n"
"s_and_b32 s81, s81, 0xffffff80\n"
"s_mul_i32 s67, s80, 25600\n"
"s_add_u32 s81, s81, s67\n"
"s_add_u32 s81, s81, 3072\n"
"s_lshl_b32 s67, s54, 1\n"
"v_mfma_f32_32x32x16_bf16 v[144:159], v[54:57], a[232:235], v[144:159]\n"
"ds_read_b128 a[176:179], v16 offset:20480\n"
"s_add_u32 s67, s67, s52\n"
"s_lshl_b32 s67, s67, 5\n"
"s_add_u32 s67, s67, s80\n"
"s_mul_i32 s67, s67, 7680\n"
"v_accvgpr_read_b32 v31, a193\n"
"v_mfma_f32_32x32x16_bf16 v[160:175], v[54:57], a[236:239], v[160:175]\n"
"ds_read_b128 a[180:183], v16 offset:24576\n"
"s_cmp_lt_u32 s52, 2\n"
"s_cselect_b64 s[16:17], s[28:29], s[16:17]\n"
"s_cselect_b32 s18, s30, s34\n"
"s_cselect_b32 s81, s67, s81\n"
"s_cselect_b32 s67, 0, 896\n"
"v_mfma_f32_32x32x2_f32 v[96:111], v5, v59, 0\n"
"s_waitcnt lgkmcnt(2)\n"
"ds_read_b128 a[184:187], v16 offset:28672\n"
"v_accvgpr_read_b32 v32, a194\n"
"v_accvgpr_read_b32 v33, a195\n"
"v_accvgpr_read_b32 v38, a196\n"
"v_accvgpr_read_b32 v39, a197\n"
"v_mfma_f32_32x32x16_bf16 v[144:159], a[148:151], a[168:171], v[144:159]\n"
"ds_read_b128 a[188:191], v16 offset:32768\n"
"v_accvgpr_read_b32 v26, a198\n"
"v_add_u32 v27, 61440, v26\n"
"v_add_u32 v80, 61440, v27\n"
"s_and_b32 s78, s54, 1\n"
"s_lshl_b32 s78, s78, 6\n"
"v_mfma_f32_32x32x16_bf16 v[160:175], a[148:151], a[172:175], v[160:175]\n"
"s_waitcnt lgkmcnt(2)\n"
"ds_read_b128 a[160:163], v17 offset:20480\n"
"v_xor_b32 v28, s78, v10\n"
"v_xor_b32 v29, s78, v11\n"
"s_add_u32 s66, s64, 0\n"
"s_mul_i32 s66, s66, 81920\n"
"s_add_u32 m0, s59, 122880\n"
"v_mfma_f32_32x32x16_bf16 v[112:127], a[152:155], a[176:179], v[112:127]\n"
"ds_read_b128 a[164:167], v17 offset:24576\n"
"buffer_load_dwordx4 v39, s[28:31], s66 offen offset:0 lds\n"
"v_mul_u32_u24 v86, 21846, v2\n"
"v_lshrrev_b32 v86, 16, v86\n"
"v_lshlrev_b32 v87, 7, v2\n"
"v_mfma_f32_32x32x16_bf16 v[128:143], a[152:155], a[180:183], v[128:143]\n"
"s_waitcnt lgkmcnt(2)\n"
"ds_read_b128 a[168:171], v17 offset:28672\n"
"v_mad_u32_u24 v86, v86, s67, v87\n"
"s_lshr_b32 s67, s53, 3\n"
"v_xor_b32 v87, s67, v2\n"
"v_and_b32 v87, 1, v87\n"
"v_lshl_or_b32 v86, v87, 30, v86\n"
"v_mfma_f32_32x32x16_bf16 v[144:159], a[152:155], a[184:187], v[144:159]\n"
"s_waitcnt vmcnt(9)\n"
"s_barrier\n"
"ds_read_b128 a[172:175], v17 offset:32768\n"
"buffer_load_dwordx4 v40, s[28:31], s66 offen offset:1024 lds\n"
"v_mfma_f32_32x32x16_bf16 v[160:175], a[152:155], a[188:191], v[160:175]\n"
"s_waitcnt lgkmcnt(2)\n"
"ds_read_b64_tr_b16 a[176:177], v18 offset:0\n"
"ds_read_b64_tr_b16 a[178:179], v18 offset:512\n"
"v_mfma_f32_32x32x16_bf16 v[112:127], a[156:159], a[160:163], v[112:127]\n"
"ds_read_b64_tr_b16 a[180:181], v19 offset:0\n"
"ds_read_b64_tr_b16 a[182:183], v19 offset:512\n"
"buffer_load_dwordx4 v41, s[28:31], s66 offen offset:2048 lds\n"
"v_mfma_f32_32x32x16_bf16 v[128:143], a[156:159], a[164:167], v[128:143]\n"
"s_waitcnt lgkmcnt(4)\n"
"ds_read_b64_tr_b16 a[184:185], v8 offset:0\n"
"ds_read_b64_tr_b16 a[186:187], v8 offset:512\n"
"v_mfma_f32_32x32x16_bf16 v[144:159], a[156:159], a[168:171], v[144:159]\n"
"ds_read_b64_tr_b16 a[188:189], v20 offset:0\n"
"ds_read_b64_tr_b16 a[190:191], v20 offset:512\n"
"buffer_load_dwordx4 v42, s[28:31], s66 offen offset:3072 lds\n"
"s_add_u32 m0, s59, 126976\n"
"v_mfma_f32_32x32x16_bf16 v[160:175], a[156:159], a[172:175], v[160:175]\n"
"s_waitcnt lgkmcnt(4)\n"
"ds_read_b64_tr_b16 a[160:161], v21 offset:0\n"
"ds_read_b64_tr_b16 a[162:163], v21 offset:512\n"
"buffer_load_dword v3, v86, s[16:19], s81 offen\n"
"s_movk_i32 s67, 60\n"
"s_cmp_lt_u32 s52, 2\n"
"s_cselect_b32 s67, s67, 200\n"
"s_lshl_b32 s67, s67, 11\n"
"s_add_u32 s81, s81, s67\n"
"buffer_load_dword v3, v86, s[16:19], s81 offen\n"
"v_mfma_f32_32x32x16_bf16 v[192:207], a[176:179], a[0:3], 0\n"
"ds_read_b64_tr_b16 a[164:165], v18 offset:4096\n"
"ds_read_b64_tr_b16 a[166:167], v18 offset:4608\n"
"v_pk_mul_f32 v[112:113], v[112:113], v[96:97]\n"
"v_pk_mul_f32 v[114:115], v[114:115], v[98:99]\n"
"v_pk_mul_f32 v[116:117], v[116:117], v[100:101]\n"
"v_pk_mul_f32 v[118:119], v[118:119], v[102:103]\n"
"v_pk_mul_f32 v[120:121], v[120:121], v[104:105]\n"
"v_pk_mul_f32 v[122:123], v[122:123], v[106:107]\n"
"v_pk_mul_f32 v[124:125], v[124:125], v[108:109]\n"
"v_pk_mul_f32 v[126:127], v[126:127], v[110:111]\n"
"v_pk_mul_f32 v[128:129], v[128:129], v[96:97]\n"
"v_pk_mul_f32 v[130:131], v[130:131], v[98:99]\n"
"v_pk_mul_f32 v[132:133], v[132:133], v[100:101]\n"
"v_pk_mul_f32 v[134:135], v[134:135], v[102:103]\n"
"v_pk_mul_f32 v[136:137], v[136:137], v[104:105]\n"
"v_pk_mul_f32 v[138:139], v[138:139], v[106:107]\n"
"v_pk_mul_f32 v[140:141], v[140:141], v[108:109]\n"
"v_pk_mul_f32 v[142:143], v[142:143], v[110:111]\n"
"v_pk_mul_f32 v[144:145], v[144:145], v[96:97]\n"
"v_pk_mul_f32 v[146:147], v[146:147], v[98:99]\n"
"v_pk_mul_f32 v[148:149], v[148:149], v[100:101]\n"
"v_pk_mul_f32 v[150:151], v[150:151], v[102:103]\n"
"v_pk_mul_f32 v[152:153], v[152:153], v[104:105]\n"
"v_pk_mul_f32 v[154:155], v[154:155], v[106:107]\n"
"v_pk_mul_f32 v[156:157], v[156:157], v[108:109]\n"
"v_pk_mul_f32 v[158:159], v[158:159], v[110:111]\n"
"v_pk_mul_f32 v[160:161], v[160:161], v[96:97]\n"
"v_pk_mul_f32 v[162:163], v[162:163], v[98:99]\n"
"v_pk_mul_f32 v[164:165], v[164:165], v[100:101]\n"
"v_pk_mul_f32 v[166:167], v[166:167], v[102:103]\n"
"v_pk_mul_f32 v[168:169], v[168:169], v[104:105]\n"
"v_pk_mul_f32 v[170:171], v[170:171], v[106:107]\n"
"v_pk_mul_f32 v[172:173], v[172:173], v[108:109]\n"
"v_pk_mul_f32 v[174:175], v[174:175], v[110:111]\n"
"v_permlane16_swap_b32 v112, v120\n"
"v_permlane16_swap_b32 v113, v121\n"
"v_permlane16_swap_b32 v114, v122\n"
"v_permlane16_swap_b32 v115, v123\n"
"v_permlane16_swap_b32 v116, v124\n"
"v_permlane16_swap_b32 v117, v125\n"
"v_permlane16_swap_b32 v118, v126\n"
"v_permlane16_swap_b32 v119, v127\n"
"v_permlane16_swap_b32 v128, v136\n"
"v_permlane16_swap_b32 v129, v137\n"
"v_permlane16_swap_b32 v130, v138\n"
"v_permlane16_swap_b32 v131, v139\n"
"v_permlane16_swap_b32 v132, v140\n"
"v_permlane16_swap_b32 v133, v141\n"
"v_permlane16_swap_b32 v134, v142\n"
"v_permlane16_swap_b32 v135, v143\n"
"v_permlane16_swap_b32 v144, v152\n"
"v_permlane16_swap_b32 v145, v153\n"
"v_permlane16_swap_b32 v146, v154\n"
"v_permlane16_swap_b32 v147, v155\n"
"v_permlane16_swap_b32 v148, v156\n"
"v_permlane16_swap_b32 v149, v157\n"
"v_permlane16_swap_b32 v150, v158\n"
"v_permlane16_swap_b32 v151, v159\n"
"v_permlane16_swap_b32 v160, v168\n"
"v_permlane16_swap_b32 v161, v169\n"
"v_permlane16_swap_b32 v162, v170\n"
"v_permlane16_swap_b32 v163, v171\n"
"v_permlane16_swap_b32 v164, v172\n"
"v_permlane16_swap_b32 v165, v173\n"
"v_permlane16_swap_b32 v166, v174\n"
"v_permlane16_swap_b32 v167, v175\n"
"v_pk_fma_f32 v[106:107], v[46:47], v[112:113], v[114:115] op_sel:[0,0,0] op_sel_hi:[0,1,1]\n"
"v_pk_fma_f32 v[108:109], v[46:47], v[120:121], v[122:123] op_sel:[0,0,0] op_sel_hi:[0,1,1]\n"
"v_pk_fma_f32 v[106:107], v[46:47], v[120:121], v[106:107] op_sel:[1,0,0] op_sel_hi:[1,1,1] neg_lo:[1,0,0] neg_hi:[1,0,0]\n"
"v_pk_fma_f32 v[108:109], v[46:47], v[112:113], v[108:109] op_sel:[1,0,0] op_sel_hi:[1,1,1]\n"
"v_pk_fma_f32 v[102:103], v[46:47], v[106:107], v[116:117] op_sel:[0,0,0] op_sel_hi:[0,1,1]\n"
"v_pk_fma_f32 v[104:105], v[46:47], v[108:109], v[124:125] op_sel:[0,0,0] op_sel_hi:[0,1,1]\n"
"v_pk_fma_f32 v[102:103], v[46:47], v[108:109], v[102:103] op_sel:[1,0,0] op_sel_hi:[1,1,1] neg_lo:[1,0,0] neg_hi:[1,0,0]\n"
"v_pk_fma_f32 v[104:105], v[46:47], v[106:107], v[104:105] op_sel:[1,0,0] op_sel_hi:[1,1,1]\n"
"v_pk_fma_f32 v[106:107], v[46:47], v[102:103], v[118:119] op_sel:[0,0,0] op_sel_hi:[0,1,1]\n"
"v_pk_fma_f32 v[108:109], v[46:47], v[104:105], v[126:127] op_sel:[0,0,0] op_sel_hi:[0,1,1]\n"
"v_pk_fma_f32 v[106:107], v[46:47], v[104:105], v[106:107] op_sel:[1,0,0] op_sel_hi:[1,1,1] neg_lo:[1,0,0] neg_hi:[1,0,0]\n"
"v_pk_fma_f32 v[108:109], v[46:47], v[102:103], v[108:109] op_sel:[1,0,0] op_sel_hi:[1,1,1]\n"
"v_fma_f32 v110, v176, v106, v107\n"
"v_fma_f32 v111, v176, v108, v109\n"
"v_fma_f32 v110, -v177, v108, v110\n"
"v_fmac_f32 v111, v177, v106\n"
"ds_write_b64 v74, v[110:111] offset:0\n"
"v_pk_fma_f32 v[106:107], v[48:49], v[128:129], v[130:131] op_sel:[0,0,0] op_sel_hi:[0,1,1]\n"
"v_pk_fma_f32 v[108:109], v[48:49], v[136:137], v[138:139] op_sel:[0,0,0] op_sel_hi:[0,1,1]\n"
"v_pk_fma_f32 v[106:107], v[48:49], v[136:137], v[106:107] op_sel:[1,0,0] op_sel_hi:[1,1,1] neg_lo:[1,0,0] neg_hi:[1,0,0]\n"
"v_pk_fma_f32 v[108:109], v[48:49], v[128:129], v[108:109] op_sel:[1,0,0] op_sel_hi:[1,1,1]\n"
"v_pk_fma_f32 v[102:103], v[48:49], v[106:107], v[132:133] op_sel:[0,0,0] op_sel_hi:[0,1,1]\n"
"v_pk_fma_f32 v[104:105], v[48:49], v[108:109], v[140:141] op_sel:[0,0,0] op_sel_hi:[0,1,1]\n"
"v_pk_fma_f32 v[102:103], v[48:49], v[108:109], v[102:103] op_sel:[1,0,0] op_sel_hi:[1,1,1] neg_lo:[1,0,0] neg_hi:[1,0,0]\n"
"v_pk_fma_f32 v[104:105], v[48:49], v[106:107], v[104:105] op_sel:[1,0,0] op_sel_hi:[1,1,1]\n"
"v_pk_fma_f32 v[106:107], v[48:49], v[102:103], v[134:135] op_sel:[0,0,0] op_sel_hi:[0,1,1]\n"
"v_pk_fma_f32 v[108:109], v[48:49], v[104:105], v[142:143] op_sel:[0,0,0] op_sel_hi:[0,1,1]\n"
"v_pk_fma_f32 v[106:107], v[48:49], v[104:105], v[106:107] op_sel:[1,0,0] op_sel_hi:[1,1,1] neg_lo:[1,0,0] neg_hi:[1,0,0]\n"
"v_pk_fma_f32 v[108:109], v[48:49], v[102:103], v[108:109] op_sel:[1,0,0] op_sel_hi:[1,1,1]\n"
"v_fma_f32 v110, v180, v106, v107\n"
"v_fma_f32 v111, v180, v108, v109\n"
"v_fma_f32 v110, -v181, v108, v110\n"
"v_fmac_f32 v111, v181, v106\n"
"ds_write_b64 v74, v[110:111] offset:2304\n"
"v_pk_fma_f32 v[106:107], v[76:77], v[144:145], v[146:147] op_sel:[0,0,0] op_sel_hi:[0,1,1]\n"
"v_pk_fma_f32 v[108:109], v[76:77], v[152:153], v[154:155] op_sel:[0,0,0] op_sel_hi:[0,1,1]\n"
"v_pk_fma_f32 v[106:107], v[76:77], v[152:153], v[106:107] op_sel:[1,0,0] op_sel_hi:[1,1,1] neg_lo:[1,0,0] neg_hi:[1,0,0]\n"
"v_pk_fma_f32 v[108:109], v[76:77], v[144:145], v[108:109] op_sel:[1,0,0] op_sel_hi:[1,1,1]\n"
"v_pk_fma_f32 v[102:103], v[76:77], v[106:107], v[148:149] op_sel:[0,0,0] op_sel_hi:[0,1,1]\n"
"v_pk_fma_f32 v[104:105], v[76:77], v[108:109], v[156:157] op_sel:[0,0,0] op_sel_hi:[0,1,1]\n"
"v_pk_fma_f32 v[102:103], v[76:77], v[108:109], v[102:103] op_sel:[1,0,0] op_sel_hi:[1,1,1] neg_lo:[1,0,0] neg_hi:[1,0,0]\n"
"v_pk_fma_f32 v[104:105], v[76:77], v[106:107], v[104:105] op_sel:[1,0,0] op_sel_hi:[1,1,1]\n"
"v_pk_fma_f32 v[106:107], v[76:77], v[102:103], v[150:151] op_sel:[0,0,0] op_sel_hi:[0,1,1]\n"
"v_pk_fma_f32 v[108:109], v[76:77], v[104:105], v[158:159] op_sel:[0,0,0] op_sel_hi:[0,1,1]\n"
"v_pk_fma_f32 v[106:107], v[76:77], v[104:105], v[106:107] op_sel:[1,0,0] op_sel_hi:[1,1,1] neg_lo:[1,0,0] neg_hi:[1,0,0]\n"
"v_pk_fma_f32 v[108:109], v[76:77], v[102:103], v[108:109] op_sel:[1,0,0] op_sel_hi:[1,1,1]\n"
"v_fma_f32 v110, v184, v106, v107\n"
"v_fma_f32 v111, v184, v108, v109\n"
"v_fma_f32 v110, -v185, v108, v110\n"
"v_fmac_f32 v111, v185, v106\n"
"ds_write_b64 v74, v[110:111] offset:4608\n"
"v_pk_fma_f32 v[106:107], v[78:79], v[160:161], v[162:163] op_sel:[0,0,0] op_sel_hi:[0,1,1]\n"
"v_pk_fma_f32 v[108:109], v[78:79], v[168:169], v[170:171] op_sel:[0,0,0] op_sel_hi:[0,1,1]\n"
"v_pk_fma_f32 v[106:107], v[78:79], v[168:169], v[106:107] op_sel:[1,0,0] op_sel_hi:[1,1,1] neg_lo:[1,0,0] neg_hi:[1,0,0]\n"
"v_pk_fma_f32 v[108:109], v[78:79], v[160:161], v[108:109] op_sel:[1,0,0] op_sel_hi:[1,1,1]\n"
"v_pk_fma_f32 v[102:103], v[78:79], v[106:107], v[164:165] op_sel:[0,0,0] op_sel_hi:[0,1,1]\n"
"v_pk_fma_f32 v[104:105], v[78:79], v[108:109], v[172:173] op_sel:[0,0,0] op_sel_hi:[0,1,1]\n"
"v_pk_fma_f32 v[102:103], v[78:79], v[108:109], v[102:103] op_sel:[1,0,0] op_sel_hi:[1,1,1] neg_lo:[1,0,0] neg_hi:[1,0,0]\n"
"v_pk_fma_f32 v[104:105], v[78:79], v[106:107], v[104:105] op_sel:[1,0,0] op_sel_hi:[1,1,1]\n"
"v_pk_fma_f32 v[106:107], v[78:79], v[102:103], v[166:167] op_sel:[0,0,0] op_sel_hi:[0,1,1]\n"
"v_pk_fma_f32 v[108:109], v[78:79], v[104:105], v[174:175] op_sel:[0,0,0] op_sel_hi:[0,1,1]\n"
"v_pk_fma_f32 v[106:107], v[78:79], v[104:105], v[106:107] op_sel:[1,0,0] op_sel_hi:[1,1,1] neg_lo:[1,0,0] neg_hi:[1,0,0]\n"
"v_pk_fma_f32 v[108:109], v[78:79], v[102:103], v[108:109] op_sel:[1,0,0] op_sel_hi:[1,1,1]\n"
"v_fma_f32 v110, v188, v106, v107\n"
"v_fma_f32 v111, v188, v108, v109\n"
"v_fma_f32 v110, -v189, v108, v110\n"
"v_fmac_f32 v111, v189, v106\n"
"ds_write_b64 v74, v[110:111] offset:6912\n"
"s_mul_i32 s60, s53, 10240\n"
"s_lshl_b32 s61, s63, 8\n"
"s_add_u32 s61, s61, 0x1720000\n"
"s_lshl_b32 s62, s52, 8\n"
"s_add_u32 s61, s61, s62\n"
"s_add_u32 s60, s60, s61\n"
"s_waitcnt lgkmcnt(0)\n"
"s_barrier\n"
"ds_read_b128 v[90:93], v75\n"
"ds_read_b128 v[94:97], v75 offset:16\n"
"v_cmp_gt_u32 vcc, 16, v2\n"
"v_mov_b32 v106, 0x70000000\n"
"v_mov_b32 v99, s56\n"
"v_mov_b32 v101, s56\n"
"v_cndmask_b32 v106, v106, v44, vcc\n"
"s_waitcnt lgkmcnt(1)\n"
"v_fmac_f32 v92, v90, v66\n"
"v_fmac_f32 v93, v90, v67\n"
"v_fma_f32 v92, -v91, v67, v92\n"
"v_fmac_f32 v93, v91, v66\n"
"s_waitcnt lgkmcnt(0)\n"
"v_fmac_f32 v94, v92, v66\n"
"v_fmac_f32 v95, v92, v67\n"
"v_fma_f32 v94, -v93, v67, v94\n"
"v_fmac_f32 v95, v93, v66\n"
"v_fmac_f32 v96, v94, v66\n"
"v_fmac_f32 v97, v94, v67\n"
"v_fma_f32 v96, -v95, v67, v96\n"
"v_fmac_f32 v97, v95, v66\n"
"v_mov_b32 v90, v96\n"
"v_mov_b32 v91, v97\n"
"s_nop 0\n"
"v_permlane32_swap_b32 v96, v90\n"
"v_permlane32_swap_b32 v97, v91\n"
"v_fmac_f32 v90, v68, v96\n"
"v_fmac_f32 v91, v68, v97\n"
"v_fma_f32 v90, -v69, v97, v90\n"
"v_fmac_f32 v91, v69, v96\n"
"v_mov_b32 v92, v90\n"
"v_mov_b32 v93, v91\n"
"s_nop 0\n"
"v_permlane16_swap_b32 v90, v92\n"
"v_permlane16_swap_b32 v91, v93\n"
"v_fmac_f32 v92, v70, v90\n"
"v_fmac_f32 v93, v70, v91\n"
"v_fma_f32 v92, -v71, v91, v92\n"
"v_fmac_f32 v93, v71, v90\n"
"v_mov_b32 v98, v92\n"
"v_mov_b32 v100, v93\n"
"buffer_store_dwordx4 v[98:101], v106, s[40:43], s60 offen sc1\n"
"v_mul_f32 v86, v53, v96\n"
"v_mul_f32 v87, v53, v97\n"
"v_fmac_f32 v86, v72, v90\n"
"v_fmac_f32 v87, v72, v91\n"
"v_fma_f32 v86, -v73, v91, v86\n"
"v_fmac_f32 v87, v73, v90\n"
"v_mfma_f32_32x32x16_bf16 v[208:223], a[180:183], a[0:3], 0\n"
"ds_read_b64_tr_b16 a[168:169], v19 offset:4096\n"
"ds_read_b64_tr_b16 a[170:171], v19 offset:4608\n"
"s_add_u32 s67, s64, 0\n"
"s_lshl_b32 s67, s67, 12\n"
"s_add_u32 s67, s67, s65\n"
"s_add_u32 s67, s67, 81920\n"
"v_mfma_f32_16x16x32_bf16 a[248:251], a[244:247], a[184:187], 0\n"
"ds_read_b64_tr_b16 a[172:173], v8 offset:4096\n"
"ds_read_b64_tr_b16 a[174:175], v8 offset:4608\n"
"v_mfma_f32_32x32x16_bf16 v[224:239], a[188:191], a[0:3], 0\n"
"ds_read_b64_tr_b16 a[176:177], v20 offset:4096\n"
"ds_read_b64_tr_b16 a[178:179], v20 offset:4608\n"
"buffer_load_dwordx4 v43, s[28:31], s66 offen lds\n"
"s_mul_i32 s66, s71, 1536\n"
"s_add_u32 m0, s57, 61440\n"
"v_mfma_f32_32x32x16_bf16 v[240:255], a[160:163], a[0:3], 0\n"
"ds_read_b64_tr_b16 a[180:181], v21 offset:4096\n"
"ds_read_b64_tr_b16 a[182:183], v21 offset:4608\n"
"v_accvgpr_read_b32 v58, a208\n"
"v_accvgpr_read_b32 v59, a209\n"
"v_accvgpr_read_b32 v60, a210\n"
"v_mfma_f32_32x32x16_bf16 v[192:207], a[164:167], a[4:7], v[192:207]\n"
"s_waitcnt lgkmcnt(4)\n"
"ds_read_b64_tr_b16 a[184:185], v18 offset:8192\n"
"ds_read_b64_tr_b16 a[186:187], v18 offset:8704\n"
"v_accvgpr_read_b32 v61, a211\n"
"v_accvgpr_read_b32 v62, a212\n"
"v_accvgpr_read_b32 v63, a213\n"
"v_mfma_f32_32x32x16_bf16 v[208:223], a[168:171], a[4:7], v[208:223]\n"
"ds_read_b64_tr_b16 a[188:189], v19 offset:8192\n"
"ds_read_b64_tr_b16 a[190:191], v19 offset:8704\n"
"buffer_load_dwordx4 v34, s[20:23], s66 offen offset:0 lds\n"
"v_accvgpr_read_b32 v64, a214\n"
"v_accvgpr_read_b32 v65, a215\n"
"v_mfma_f32_16x16x32_bf16 a[248:251], a[244:247], a[172:175], a[248:251]\n"
"s_waitcnt lgkmcnt(4)\n"
"ds_read_b64_tr_b16 a[160:161], v8 offset:8192\n"
"ds_read_b64_tr_b16 a[162:163], v8 offset:8704\n"
"v_mfma_f32_32x32x16_bf16 v[224:239], a[176:179], a[4:7], v[224:239]\n"
"ds_read_b64_tr_b16 a[164:165], v20 offset:8192\n"
"ds_read_b64_tr_b16 a[166:167], v20 offset:8704\n"
"buffer_load_dwordx4 v35, s[20:23], s66 offen offset:1024 lds\n"
"v_accvgpr_read_b32 v68, a200\n"
"v_accvgpr_read_b32 v69, a201\n"
"v_mfma_f32_32x32x16_bf16 v[240:255], a[180:183], a[4:7], v[240:255]\n"
"s_waitcnt lgkmcnt(4)\n"
"ds_read_b64_tr_b16 a[168:169], v21 offset:8192\n"
"ds_read_b64_tr_b16 a[170:171], v21 offset:8704\n"
"v_accvgpr_read_b32 v70, a202\n"
"v_accvgpr_read_b32 v71, a203\n"
"v_accvgpr_read_b32 v72, a204\n"
"v_mfma_f32_32x32x16_bf16 v[192:207], a[184:187], a[8:11], v[192:207]\n"
"ds_read_b64_tr_b16 a[172:173], v18 offset:12288\n"
"ds_read_b64_tr_b16 a[174:175], v18 offset:12800\n"
"buffer_load_dwordx4 v36, s[24:27], s66 offen offset:2048 lds\n"
"v_accvgpr_read_b32 v73, a205\n"
"v_accvgpr_read_b32 v110, a206\n"
"v_mfma_f32_32x32x16_bf16 v[208:223], a[188:191], a[8:11], v[208:223]\n"
"s_waitcnt lgkmcnt(4)\n"
"ds_read_b64_tr_b16 a[176:177], v19 offset:12288\n"
"ds_read_b64_tr_b16 a[178:179], v19 offset:12800\n"
"v_accvgpr_read_b32 v111, a207\n"
"v_sub_f32 v106, 0, v178\n"
"v_mul_f32 v53, v178, v112\n"
"v_fmac_f32 v53, v179, v120\n"
"v_mfma_f32_16x16x32_bf16 a[248:251], a[244:247], a[160:163], a[248:251]\n"
"ds_read_b64_tr_b16 a[180:181], v8 offset:12288\n"
"ds_read_b64_tr_b16 a[182:183], v8 offset:12800\n"
"buffer_load_dwordx4 v37, s[24:27], s66 offen offset:3072 lds\n"
"v_mfma_f32_32x32x16_bf16 v[224:239], a[164:167], a[8:11], v[224:239]\n"
"s_waitcnt lgkmcnt(4)\n"
"ds_read_b64_tr_b16 a[184:185], v20 offset:12288\n"
"ds_read_b64_tr_b16 a[186:187], v20 offset:12800\n"
"s_mul_i32 s66, s72, 1536\n"
"s_add_u32 m0, s57, 102400\n"
"v_mul_f32 v120, v106, v120\n"
"v_fmac_f32 v120, v179, v112\n"
"v_mfma_f32_32x32x16_bf16 v[240:255], a[168:171], a[8:11], v[240:255]\n"
"s_waitcnt vmcnt(16)\n"
"s_barrier\n"
"ds_read_b64_tr_b16 a[188:189], v21 offset:12288\n"
"ds_read_b64_tr_b16 a[190:191], v21 offset:12800\n"
"buffer_load_dwordx4 v34, s[20:23], s66 offen offset:0 lds\n"
"v_mfma_f32_32x32x16_bf16 v[192:207], a[172:175], a[12:15], v[192:207]\n"
"s_waitcnt lgkmcnt(4)\n"
"ds_read_b64_tr_b16 a[160:161], v18 offset:20480\n"
"ds_read_b64_tr_b16 a[162:163], v18 offset:20992\n"
"v_mul_f32 v112, v178, v113\n"
"v_fmac_f32 v112, v179, v121\n"
"v_mul_f32 v121, v106, v121\n"
"v_fmac_f32 v121, v179, v113\n"
"v_mfma_f32_32x32x16_bf16 v[208:223], a[176:179], a[12:15], v[208:223]\n"
"ds_read_b64_tr_b16 a[164:165], v19 offset:20480\n"
"ds_read_b64_tr_b16 a[166:167], v19 offset:20992\n"
"buffer_load_dwordx4 v35, s[20:23], s66 offen offset:1024 lds\n"
"v_mul_f32 v113, v178, v114\n"
"v_fmac_f32 v113, v179, v122\n"
"v_mfma_f32_16x16x32_bf16 a[248:251], a[244:247], a[180:183], a[248:251]\n"
"s_waitcnt lgkmcnt(4)\n"
"ds_read_b64_tr_b16 a[168:169], v8 offset:20480\n"
"ds_read_b64_tr_b16 a[170:171], v8 offset:20992\n"
"v_mfma_f32_32x32x16_bf16 v[224:239], a[184:187], a[12:15], v[224:239]\n"
"ds_read_b64_tr_b16 a[172:173], v20 offset:20480\n"
"ds_read_b64_tr_b16 a[174:175], v20 offset:20992\n"
"buffer_load_dwordx4 v36, s[24:27], s66 offen offset:2048 lds\n"
"v_mul_f32 v122, v106, v122\n"
"v_fmac_f32 v122, v179, v114\n"
"v_mfma_f32_32x32x16_bf16 v[240:255], a[188:191], a[12:15], v[240:255]\n"
"s_waitcnt lgkmcnt(4)\n"
"ds_read_b64_tr_b16 a[176:177], v21 offset:20480\n"
"ds_read_b64_tr_b16 a[178:179], v21 offset:20992\n"
"v_mul_f32 v114, v178, v115\n"
"v_fmac_f32 v114, v179, v123\n"
"v_mul_f32 v123, v106, v123\n"
"v_fmac_f32 v123, v179, v115\n"
"v_mfma_f32_32x32x16_bf16 v[192:207], a[160:163], a[16:19], v[192:207]\n"
"ds_read_b64_tr_b16 a[180:181], v18 offset:24576\n"
"ds_read_b64_tr_b16 a[182:183], v18 offset:25088\n"
"buffer_load_dwordx4 v37, s[24:27], s66 offen offset:3072 lds\n"
"s_mul_i32 s66, s73, 1536\n"
"s_add_u32 m0, s57, 81920\n"
"v_mfma_f32_32x32x16_bf16 v[208:223], a[164:167], a[16:19], v[208:223]\n"
"s_waitcnt lgkmcnt(4)\n"
"ds_read_b64_tr_b16 a[184:185], v19 offset:24576\n"
"ds_read_b64_tr_b16 a[186:187], v19 offset:25088\n"
"v_mul_f32 v115, v178, v116\n"
"v_fmac_f32 v115, v179, v124\n"
"v_mul_f32 v124, v106, v124\n"
"v_fmac_f32 v124, v179, v116\n"
"v_mfma_f32_16x16x32_bf16 a[248:251], a[244:247], a[168:171], a[248:251]\n"
"ds_read_b64_tr_b16 a[188:189], v8 offset:24576\n"
"ds_read_b64_tr_b16 a[190:191], v8 offset:25088\n"
"buffer_load_dwordx4 v34, s[20:23], s66 offen offset:0 lds\n"
"v_mfma_f32_32x32x16_bf16 v[224:239], a[172:175], a[16:19], v[224:239]\n"
"s_waitcnt lgkmcnt(4)\n"
"ds_read_b64_tr_b16 a[160:161], v20 offset:24576\n"
"ds_read_b64_tr_b16 a[162:163], v20 offset:25088\n"
"v_mul_f32 v116, v178, v117\n"
"v_fmac_f32 v116, v179, v125\n"
"v_mul_f32 v125, v106, v125\n"
"v_fmac_f32 v125, v179, v117\n"
"v_mfma_f32_32x32x16_bf16 v[240:255], a[176:179], a[16:19], v[240:255]\n"
"ds_read_b64_tr_b16 a[164:165], v21 offset:24576\n"
"ds_read_b64_tr_b16 a[166:167], v21 offset:25088\n"
"buffer_load_dwordx4 v35, s[20:23], s66 offen offset:1024 lds\n"
"v_mul_f32 v117, v178, v118\n"
"v_fmac_f32 v117, v179, v126\n"
"v_mfma_f32_32x32x16_bf16 v[192:207], a[180:183], a[20:23], v[192:207]\n"
"s_waitcnt lgkmcnt(4)\n"
"ds_read_b64_tr_b16 a[168:169], v18 offset:28672\n"
"ds_read_b64_tr_b16 a[170:171], v18 offset:29184\n"
"v_mul_f32 v126, v106, v126\n"
"v_fmac_f32 v126, v179, v118\n"
"v_mul_f32 v118, v178, v119\n"
"v_fmac_f32 v118, v179, v127\n"
"v_mfma_f32_32x32x16_bf16 v[208:223], a[184:187], a[20:23], v[208:223]\n"
"ds_read_b64_tr_b16 a[172:173], v19 offset:28672\n"
"ds_read_b64_tr_b16 a[174:175], v19 offset:29184\n"
"buffer_load_dwordx4 v36, s[24:27], s66 offen offset:2048 lds\n"
"v_mul_f32 v127, v106, v127\n"
"v_fmac_f32 v127, v179, v119\n"
"v_mfma_f32_16x16x32_bf16 a[248:251], a[244:247], a[188:191], a[248:251]\n"
"s_waitcnt lgkmcnt(4)\n"
"ds_read_b64_tr_b16 a[176:177], v8 offset:28672\n"
"ds_read_b64_tr_b16 a[178:179], v8 offset:29184\n"
"v_mfma_f32_32x32x16_bf16 v[224:239], a[160:163], a[20:23], v[224:239]\n"
"ds_read_b64_tr_b16 a[180:181], v20 offset:28672\n"
"ds_read_b64_tr_b16 a[182:183], v20 offset:29184\n"
"buffer_load_dwordx4 v37, s[24:27], s66 offen offset:3072 lds\n"
"v_sub_f32 v106, 0, v182\n"
"v_mul_f32 v119, v182, v128\n"
"v_mfma_f32_32x32x16_bf16 v[240:255], a[164:167], a[20:23], v[240:255]\n"
"s_waitcnt lgkmcnt(4)\n"
"ds_read_b64_tr_b16 a[184:185], v21 offset:28672\n"
"ds_read_b64_tr_b16 a[186:187], v21 offset:29184\n"
"v_fmac_f32 v119, v183, v136\n"
"v_mul_f32 v136, v106, v136\n"
"v_fmac_f32 v136, v183, v128\n"
"v_mul_f32 v128, v182, v129\n"
"v_mfma_f32_32x32x16_bf16 v[192:207], a[168:171], a[24:27], v[192:207]\n"
"ds_read_b64_tr_b16 a[188:189], v18 offset:32768\n"
"ds_read_b64_tr_b16 a[190:191], v18 offset:33280\n"
"v_fmac_f32 v128, v183, v137\n"
"v_mul_f32 v137, v106, v137\n"
"v_fmac_f32 v137, v183, v129\n"
"v_mul_f32 v129, v182, v130\n"
"v_mfma_f32_32x32x16_bf16 v[208:223], a[172:175], a[24:27], v[208:223]\n"
"s_waitcnt lgkmcnt(4)\n"
"ds_read_b64_tr_b16 a[160:161], v19 offset:32768\n"
"ds_read_b64_tr_b16 a[162:163], v19 offset:33280\n"
"v_fmac_f32 v129, v183, v138\n"
"v_mul_f32 v138, v106, v138\n"
"v_fmac_f32 v138, v183, v130\n"
"v_mul_f32 v130, v182, v131\n"
"v_mfma_f32_16x16x32_bf16 a[248:251], a[244:247], a[176:179], a[248:251]\n"
"ds_read_b64_tr_b16 a[164:165], v8 offset:32768\n"
"ds_read_b64_tr_b16 a[166:167], v8 offset:33280\n"
"v_mfma_f32_32x32x16_bf16 v[224:239], a[180:183], a[24:27], v[224:239]\n"
"s_waitcnt lgkmcnt(4)\n"
"ds_read_b64_tr_b16 a[168:169], v20 offset:32768\n"
"ds_read_b64_tr_b16 a[170:171], v20 offset:33280\n"
"v_fmac_f32 v130, v183, v139\n"
"v_mul_f32 v139, v106, v139\n"
"v_fmac_f32 v139, v183, v131\n"
"v_mul_f32 v131, v182, v132\n"
"v_mfma_f32_32x32x16_bf16 v[240:255], a[184:187], a[24:27], v[240:255]\n"
"s_waitcnt vmcnt(20)\n"
"s_barrier\n"
"ds_read_b64_tr_b16 a[172:173], v21 offset:32768\n"
"ds_read_b64_tr_b16 a[174:175], v21 offset:33280\n"
"s_mul_i32 s66, s74, 1536\n"
"s_add_u32 m0, s57, 0\n"
"s_nop 0\n"
"buffer_load_dwordx4 v34, s[20:23], s66 offen offset:0 lds\n"
"v_mfma_f32_32x32x16_bf16 v[192:207], a[188:191], a[28:31], v[192:207]\n"
"s_waitcnt lgkmcnt(4)\n"
"ds_read_b64_tr_b16 a[176:177], v18 offset:40960\n"
"ds_read_b64_tr_b16 a[178:179], v18 offset:41472\n"
"v_fmac_f32 v131, v183, v140\n"
"v_mul_f32 v140, v106, v140\n"
"v_fmac_f32 v140, v183, v132\n"
"v_mul_f32 v132, v182, v133\n"
"v_mfma_f32_32x32x16_bf16 v[208:223], a[160:163], a[28:31], v[208:223]\n"
"ds_read_b64_tr_b16 a[180:181], v19 offset:40960\n"
"ds_read_b64_tr_b16 a[182:183], v19 offset:41472\n"
"buffer_load_dwordx4 v35, s[20:23], s66 offen offset:1024 lds\n"
"v_fmac_f32 v132, v183, v141\n"
"v_mul_f32 v141, v106, v141\n"
"v_mfma_f32_16x16x32_bf16 a[248:251], a[244:247], a[164:167], a[248:251]\n"
"s_waitcnt lgkmcnt(4)\n"
"ds_read_b64_tr_b16 a[184:185], v8 offset:40960\n"
"ds_read_b64_tr_b16 a[186:187], v8 offset:41472\n"
"v_mfma_f32_32x32x16_bf16 v[224:239], a[168:171], a[28:31], v[224:239]\n"
"ds_read_b64_tr_b16 a[188:189], v20 offset:40960\n"
"ds_read_b64_tr_b16 a[190:191], v20 offset:41472\n"
"buffer_load_dwordx4 v36, s[24:27], s66 offen offset:2048 lds\n"
"v_fmac_f32 v141, v183, v133\n"
"v_mul_f32 v133, v182, v134\n"
"v_mfma_f32_32x32x16_bf16 v[240:255], a[172:175], a[28:31], v[240:255]\n"
"s_waitcnt lgkmcnt(4)\n"
"ds_read_b64_tr_b16 a[160:161], v21 offset:40960\n"
"ds_read_b64_tr_b16 a[162:163], v21 offset:41472\n"
"v_fmac_f32 v133, v183, v142\n"
"v_mul_f32 v142, v106, v142\n"
"v_fmac_f32 v142, v183, v134\n"
"v_mul_f32 v134, v182, v135\n"
"v_mfma_f32_32x32x16_bf16 v[192:207], a[176:179], a[32:35], v[192:207]\n"
"ds_read_b64_tr_b16 a[164:165], v18 offset:45056\n"
"ds_read_b64_tr_b16 a[166:167], v18 offset:45568\n"
"buffer_load_dwordx4 v[90:93], v58, s[40:43], s61 offen sc1\n"
"v_fmac_f32 v134, v183, v143\n"
"v_mul_f32 v143, v106, v143\n"
"v_mfma_f32_32x32x16_bf16 v[208:223], a[180:183], a[32:35], v[208:223]\n"
"s_waitcnt lgkmcnt(4)\n"
"ds_read_b64_tr_b16 a[168:169], v19 offset:45056\n"
"ds_read_b64_tr_b16 a[170:171], v19 offset:45568\n"
"v_fmac_f32 v143, v183, v135\n"
"v_sub_f32 v106, 0, v186\n"
"v_mul_f32 v135, v186, v144\n"
"v_fmac_f32 v135, v187, v152\n"
"v_mfma_f32_16x16x32_bf16 a[248:251], a[252:255], a[184:187], a[248:251]\n"
"ds_read_b64_tr_b16 a[172:173], v8 offset:45056\n"
"ds_read_b64_tr_b16 a[174:175], v8 offset:45568\n"
"buffer_load_dwordx4 v[94:97], v59, s[40:43], s61 offen sc1\n"
"v_mfma_f32_32x32x16_bf16 v[224:239], a[188:191], a[32:35], v[224:239]\n"
"s_waitcnt lgkmcnt(4)\n"
"ds_read_b64_tr_b16 a[176:177], v20 offset:45056\n"
"ds_read_b64_tr_b16 a[178:179], v20 offset:45568\n"
"v_mul_f32 v152, v106, v152\n"
"v_fmac_f32 v152, v187, v144\n"
"v_mul_f32 v144, v186, v145\n"
"v_fmac_f32 v144, v187, v153\n"
"v_mfma_f32_32x32x16_bf16 v[240:255], a[160:163], a[32:35], v[240:255]\n"
"ds_read_b64_tr_b16 a[180:181], v21 offset:45056\n"
"ds_read_b64_tr_b16 a[182:183], v21 offset:45568\n"
"buffer_load_dwordx4 v[98:101], v60, s[40:43], s61 offen sc1\n"
"v_mul_f32 v153, v106, v153\n"
"v_fmac_f32 v153, v187, v145\n"
"v_mfma_f32_32x32x16_bf16 v[192:207], a[164:167], a[36:39], v[192:207]\n"
"s_waitcnt lgkmcnt(4)\n"
"ds_read_b64_tr_b16 a[184:185], v18 offset:49152\n"
"ds_read_b64_tr_b16 a[186:187], v18 offset:49664\n"
"buffer_load_dwordx4 v[102:105], v61, s[40:43], s61 offen sc1\n"
"v_mul_f32 v145, v186, v146\n"
"v_fmac_f32 v145, v187, v154\n"
"v_mfma_f32_32x32x16_bf16 v[208:223], a[168:171], a[36:39], v[208:223]\n"
"ds_read_b64_tr_b16 a[188:189], v19 offset:49152\n"
"ds_read_b64_tr_b16 a[190:191], v19 offset:49664\n"
"v_mul_f32 v154, v106, v154\n"
"v_fmac_f32 v154, v187, v146\n"
"v_mul_f32 v146, v186, v147\n"
"v_fmac_f32 v146, v187, v155\n"
"v_mfma_f32_16x16x32_bf16 a[248:251], a[252:255], a[172:175], a[248:251]\n"
"s_waitcnt lgkmcnt(4)\n"
"ds_read_b64_tr_b16 a[160:161], v8 offset:49152\n"
"ds_read_b64_tr_b16 a[162:163], v8 offset:49664\n"
"v_mfma_f32_32x32x16_bf16 v[224:239], a[176:179], a[36:39], v[224:239]\n"
"ds_read_b64_tr_b16 a[164:165], v20 offset:49152\n"
"ds_read_b64_tr_b16 a[166:167], v20 offset:49664\n"
"buffer_load_dwordx4 v37, s[24:27], s66 offen offset:3072 lds\n"
"v_mul_f32 v155, v106, v155\n"
"v_fmac_f32 v155, v187, v147\n"
"v_mfma_f32_32x32x16_bf16 v[240:255], a[180:183], a[36:39], v[240:255]\n"
"s_waitcnt lgkmcnt(4)\n"
"ds_read_b64_tr_b16 a[168:169], v21 offset:49152\n"
"ds_read_b64_tr_b16 a[170:171], v21 offset:49664\n"
"v_mul_f32 v147, v186, v148\n"
"v_fmac_f32 v147, v187, v156\n"
"v_mul_f32 v156, v106, v156\n"
"v_fmac_f32 v156, v187, v148\n"
"v_mfma_f32_32x32x16_bf16 v[192:207], a[184:187], a[40:43], v[192:207]\n"
"ds_read_b64_tr_b16 a[172:173], v18 offset:53248\n"
"ds_read_b64_tr_b16 a[174:175], v18 offset:53760\n"
"v_mul_f32 v148, v186, v149\n"
"v_fmac_f32 v148, v187, v157\n"
"v_mul_f32 v157, v106, v157\n"
"v_fmac_f32 v157, v187, v149\n"
"v_mfma_f32_32x32x16_bf16 v[208:223], a[188:191], a[40:43], v[208:223]\n"
"s_waitcnt lgkmcnt(4)\n"
"ds_read_b64_tr_b16 a[176:177], v19 offset:53248\n"
"ds_read_b64_tr_b16 a[178:179], v19 offset:53760\n"
"v_mul_f32 v149, v186, v150\n"
"v_fmac_f32 v149, v187, v158\n"
"v_mul_f32 v158, v106, v158\n"
"v_fmac_f32 v158, v187, v150\n"
"v_mfma_f32_16x16x32_bf16 a[248:251], a[252:255], a[160:163], a[248:251]\n"
"ds_read_b64_tr_b16 a[180:181], v8 offset:53248\n"
"ds_read_b64_tr_b16 a[182:183], v8 offset:53760\n"
"v_mfma_f32_32x32x16_bf16 v[224:239], a[164:167], a[40:43], v[224:239]\n"
"s_waitcnt lgkmcnt(4)\n"
"ds_read_b64_tr_b16 a[184:185], v20 offset:53248\n"
"ds_read_b64_tr_b16 a[186:187], v20 offset:53760\n"
"v_mul_f32 v150, v186, v151\n"
"v_fmac_f32 v150, v187, v159\n"
"v_mul_f32 v159, v106, v159\n"
"v_fmac_f32 v159, v187, v151\n"
"v_mfma_f32_32x32x16_bf16 v[240:255], a[168:171], a[40:43], v[240:255]\n"
"s_waitcnt vmcnt(16)\n"
"s_barrier\n"
"ds_read_b64_tr_b16 a[188:189], v21 offset:53248\n"
"ds_read_b64_tr_b16 a[190:191], v21 offset:53760\n"
"s_mul_i32 s66, s75, 1536\n"
"s_add_u32 m0, s57, 20480\n"
"s_nop 0\n"
"buffer_load_dwordx4 v34, s[20:23], s66 offen offset:0 lds\n"
"v_mfma_f32_32x32x16_bf16 v[192:207], a[172:175], a[44:47], v[192:207]\n"
"s_waitcnt lgkmcnt(4)\n"
"ds_read_b64_tr_b16 a[160:161], v22 offset:0\n"
"ds_read_b64_tr_b16 a[162:163], v22 offset:512\n"
"v_sub_f32 v106, 0, v190\n"
"v_mul_f32 v151, v190, v160\n"
"v_fmac_f32 v151, v191, v168\n"
"v_mul_f32 v168, v106, v168\n"
"v_mfma_f32_32x32x16_bf16 v[208:223], a[176:179], a[44:47], v[208:223]\n"
"ds_read_b64_tr_b16 a[164:165], v23 offset:0\n"
"ds_read_b64_tr_b16 a[166:167], v23 offset:512\n"
"buffer_load_dwordx4 v35, s[20:23], s66 offen offset:1024 lds\n"
"v_fmac_f32 v168, v191, v160\n"
"v_mul_f32 v160, v190, v161\n"
"v_mfma_f32_16x16x32_bf16 a[248:251], a[252:255], a[180:183], a[248:251]\n"
"s_waitcnt lgkmcnt(4)\n"
"ds_read_b64_tr_b16 a[168:169], v24 offset:0\n"
"ds_read_b64_tr_b16 a[170:171], v24 offset:512\n"
"v_mfma_f32_32x32x16_bf16 v[224:239], a[184:187], a[44:47], v[224:239]\n"
"ds_read_b64_tr_b16 a[172:173], v25 offset:0\n"
"ds_read_b64_tr_b16 a[174:175], v25 offset:512\n"
"buffer_load_dwordx4 v36, s[24:27], s66 offen offset:2048 lds\n"
"v_fmac_f32 v160, v191, v169\n"
"v_mul_f32 v169, v106, v169\n"
"v_mfma_f32_32x32x16_bf16 v[240:255], a[188:191], a[44:47], v[240:255]\n"
"s_waitcnt lgkmcnt(4)\n"
"ds_read_b64_tr_b16 a[176:177], v22 offset:4096\n"
"ds_read_b64_tr_b16 a[178:179], v22 offset:4608\n"
"v_accvgpr_read_b32 v82, a248\n"
"v_cmp_gt_u32 s[0:1], 32, v2\n"
"v_and_b32 v84, 31, v2\n"
"v_lshlrev_b32 v84, 4, v84\n"
"v_mfma_f32_32x32x16_bf16 v[192:207], a[160:163], a[48:51], v[192:207]\n"
"ds_read_b64_tr_b16 a[180:181], v23 offset:4096\n"
"ds_read_b64_tr_b16 a[182:183], v23 offset:4608\n"
"buffer_load_dwordx4 v37, s[24:27], s66 offen offset:3072 lds\n"
"s_lshl_b32 s2, s52, 10\n"
"v_add_u32 v85, s2, v84\n"
"v_mfma_f32_32x32x16_bf16 v[208:223], a[164:167], a[48:51], v[208:223]\n"
"s_waitcnt lgkmcnt(4)\n"
"ds_read_b64_tr_b16 a[184:185], v24 offset:4096\n"
"ds_read_b64_tr_b16 a[186:187], v24 offset:4608\n"
"v_mov_b32 v84, 0x70000000\n"
"v_cvt_pk_bf16_f32 v83, v82, v82\n"
"v_cndmask_b32 v84, v84, v85, s[0:1]\n"
"v_mfma_f32_32x32x16_bf16 v[224:239], a[168:171], a[48:51], v[224:239]\n"
"ds_read_b64_tr_b16 a[188:189], v25 offset:4096\n"
"ds_read_b64_tr_b16 a[190:191], v25 offset:4608\n"
"v_and_b32 v83, s100, v83\n"
"s_lshl_b32 s3, s54, 2\n"
"s_add_u32 s3, s3, s55\n"
"s_mul_i32 s3, s3, 10240\n"
"v_mfma_f32_32x32x16_bf16 v[240:255], a[172:175], a[48:51], v[240:255]\n"
"s_waitcnt lgkmcnt(4)\n"
"ds_read_b64_tr_b16 a[160:161], v22 offset:8192\n"
"ds_read_b64_tr_b16 a[162:163], v22 offset:8704\n"
"s_add_u32 s3, s3, 0x1681400\n"
"v_sub_f32 v82, v82, v83\n"
"v_cvt_pk_bf16_f32 v82, v83, v82\n"
"v_mov_b32 v83, s56\n"
"v_mfma_f32_32x32x16_bf16 v[192:207], a[176:179], a[52:55], v[192:207]\n"
"ds_read_b64_tr_b16 a[164:165], v23 offset:8192\n"
"ds_read_b64_tr_b16 a[166:167], v23 offset:8704\n"
"s_lshr_b32 s2, s101, 1\n"
"s_lshl_b32 s2, s2, 9\n"
"s_and_b32 s7, s101, 1\n"
"s_lshl_b32 s7, s7, 3\n"
"v_mfma_f32_32x32x16_bf16 v[208:223], a[180:183], a[52:55], v[208:223]\n"
"s_waitcnt lgkmcnt(4)\n"
"ds_read_b64_tr_b16 a[168:169], v24 offset:8192\n"
"ds_read_b64_tr_b16 a[170:171], v24 offset:8704\n"
"s_add_u32 s2, s2, s7\n"
"s_add_u32 s2, s2, s3\n"
"s_cmp_lt_u32 s101, 4\n"
"s_cselect_b32 s2, s2, 0x60000000\n"
"v_mfma_f32_32x32x16_bf16 v[224:239], a[184:187], a[52:55], v[224:239]\n"
"ds_read_b64_tr_b16 a[172:173], v25 offset:8192\n"
"ds_read_b64_tr_b16 a[174:175], v25 offset:8704\n"
"buffer_store_dwordx2 v[82:83], v84, s[40:43], s2 offen sc1\n"
"v_fmac_f32 v169, v191, v161\n"
"v_mfma_f32_32x32x16_bf16 v[240:255], a[188:191], a[52:55], v[240:255]\n"
"s_waitcnt lgkmcnt(4)\n"
"ds_read_b64_tr_b16 a[176:177], v22 offset:12288\n"
"ds_read_b64_tr_b16 a[178:179], v22 offset:12800\n"
"v_mul_f32 v161, v190, v162\n"
"v_fmac_f32 v161, v191, v170\n"
"v_mul_f32 v170, v106, v170\n"
"v_fmac_f32 v170, v191, v162\n"
"v_mfma_f32_32x32x16_bf16 v[192:207], a[160:163], a[56:59], v[192:207]\n"
"ds_read_b64_tr_b16 a[180:181], v23 offset:12288\n"
"ds_read_b64_tr_b16 a[182:183], v23 offset:12800\n"
"v_mul_f32 v162, v190, v163\n"
"v_fmac_f32 v162, v191, v171\n"
"v_mul_f32 v171, v106, v171\n"
"v_fmac_f32 v171, v191, v163\n"
"v_mfma_f32_32x32x16_bf16 v[208:223], a[164:167], a[56:59], v[208:223]\n"
"s_waitcnt lgkmcnt(4)\n"
"ds_read_b64_tr_b16 a[184:185], v24 offset:12288\n"
"ds_read_b64_tr_b16 a[186:187], v24 offset:12800\n"
"v_mul_f32 v163, v190, v164\n"
"v_fmac_f32 v163, v191, v172\n"
"v_mul_f32 v172, v106, v172\n"
"v_fmac_f32 v172, v191, v164\n"
"v_mfma_f32_32x32x16_bf16 v[224:239], a[168:171], a[56:59], v[224:239]\n"
"s_waitcnt vmcnt(17)\n"
"s_barrier\n"
"ds_read_b64_tr_b16 a[188:189], v25 offset:12288\n"
"ds_read_b64_tr_b16 a[190:191], v25 offset:12800\n"
"s_mul_i32 s66, s76, 1536\n"
"s_add_u32 m0, s57, 40960\n"
"s_nop 0\n"
"buffer_load_dwordx4 v34, s[20:23], s66 offen offset:0 lds\n"
"s_waitcnt vmcnt(10)\n"
"v_xor_b32 v91, v62, v91\n"
"v_xor_b32 v93, v62, v93\n"
"s_waitcnt vmcnt(9)\n"
"v_xor_b32 v95, v63, v95\n"
"v_xor_b32 v97, v63, v97\n"
"s_waitcnt vmcnt(8)\n"
"v_xor_b32 v99, v64, v99\n"
"v_xor_b32 v101, v64, v101\n"
"s_waitcnt vmcnt(7)\n"
"v_xor_b32 v103, v65, v103\n"
"v_xor_b32 v105, v65, v105\n"
"v_or3_b32 v91, v91, v93, v95\n"
"v_or3_b32 v97, v97, v99, v101\n"
"v_or3_b32 v91, v91, v103, v105\n"
"v_or_b32 v91, v91, v97\n"
"v_cmp_ne_u32 vcc, 0, v91\n"
"s_nop 0\n"
"s_cbranch_vccnz LaggB_slow%=\n"
"LaggB_ok%=:\n"
"v_mfma_f32_32x32x16_bf16 v[240:255], a[172:175], a[56:59], v[240:255]\n"
"s_waitcnt lgkmcnt(4)\n"
"ds_read_b64_tr_b16 a[160:161], v22 offset:40960\n"
"ds_read_b64_tr_b16 a[162:163], v22 offset:41472\n"
"buffer_load_dwordx4 v35, s[20:23], s66 offen offset:1024 lds\n"
"ds_read_b64 v[60:61], v75\n"
"ds_read_b64 v[62:63], v75 offset:8\n"
"ds_read_b64 v[64:65], v75 offset:16\n"
"v_mov_b32 v91, v92\n"
"v_mov_b32 v95, v96\n"
"v_mov_b32 v99, v100\n"
"v_mov_b32 v103, v104\n"
"v_fmac_f32 v98, v102, v68\n"
"v_fmac_f32 v99, v102, v69\n"
"v_fma_f32 v98, -v103, v69, v98\n"
"v_fmac_f32 v99, v103, v68\n"
"v_fmac_f32 v94, v98, v68\n"
"v_fmac_f32 v95, v98, v69\n"
"v_fma_f32 v94, -v99, v69, v94\n"
"v_fmac_f32 v95, v99, v68\n"
"v_fmac_f32 v90, v94, v68\n"
"v_fmac_f32 v91, v94, v69\n"
"v_fma_f32 v90, -v95, v69, v90\n"
"v_fmac_f32 v91, v95, v68\n"
"v_mov_b32 v92, v90\n"
"v_mov_b32 v93, v91\n"
"s_nop 0\n"
"v_permlane32_swap_b32 v90, v92\n"
"v_mfma_f32_32x32x16_bf16 v[192:207], a[176:179], a[60:63], v[192:207]\n"
"ds_read_b64_tr_b16 a[164:165], v23 offset:40960\n"
"ds_read_b64_tr_b16 a[166:167], v23 offset:41472\n"
"buffer_load_dwordx4 v36, s[24:27], s66 offen offset:2048 lds\n"
"v_permlane32_swap_b32 v91, v93\n"
"v_fmac_f32 v90, v70, v92\n"
"v_fmac_f32 v91, v70, v93\n"
"v_fma_f32 v90, -v71, v93, v90\n"
"v_fmac_f32 v91, v71, v92\n"
"v_mov_b32 v92, v90\n"
"v_mov_b32 v93, v91\n"
"s_nop 0\n"
"v_permlane16_swap_b32 v90, v92\n"
"v_permlane16_swap_b32 v91, v93\n"
"v_fmac_f32 v90, v72, v92\n"
"v_fmac_f32 v91, v72, v93\n"
"v_fma_f32 v90, -v73, v93, v90\n"
"v_fmac_f32 v91, v73, v92\n"
"v_mov_b32 v58, v86\n"
"v_mov_b32 v59, v87\n"
"v_fmac_f32 v58, v110, v90\n"
"v_fmac_f32 v59, v110, v91\n"
"v_fma_f32 v58, -v111, v91, v58\n"
"v_fmac_f32 v59, v111, v90\n"
"s_waitcnt lgkmcnt(4)\n"
"v_fmac_f32 v60, v58, v66\n"
"v_fmac_f32 v61, v58, v67\n"
"v_fma_f32 v60, -v59, v67, v60\n"
"v_mfma_f32_32x32x16_bf16 v[208:223], a[180:183], a[60:63], v[208:223]\n"
"ds_read_b64_tr_b16 a[168:169], v24 offset:40960\n"
"ds_read_b64_tr_b16 a[170:171], v24 offset:41472\n"
"buffer_load_dwordx4 v37, s[24:27], s66 offen offset:3072 lds\n"
"v_fmac_f32 v61, v59, v66\n"
"s_waitcnt lgkmcnt(5)\n"
"v_fmac_f32 v62, v60, v66\n"
"v_fmac_f32 v63, v60, v67\n"
"v_fma_f32 v62, -v61, v67, v62\n"
"v_fmac_f32 v63, v61, v66\n"
"s_waitcnt lgkmcnt(4)\n"
"v_fmac_f32 v64, v62, v66\n"
"v_fmac_f32 v65, v62, v67\n"
"v_fma_f32 v64, -v63, v67, v64\n"
"v_fmac_f32 v65, v63, v66\n"
"v_mfma_f32_32x32x16_bf16 v[224:239], a[184:187], a[60:63], v[224:239]\n"
"ds_read_b64_tr_b16 a[172:173], v25 offset:40960\n"
"ds_read_b64_tr_b16 a[174:175], v25 offset:41472\n"
"ds_write_b128 v75, v[58:61] offset:0\n"
"ds_write_b128 v75, v[62:65] offset:16\n"
"s_lshl_b32 s60, s63, 11\n"
"s_add_u32 s78, s60, s65\n"
"s_add_u32 s79, s78, 4096\n"
"s_waitcnt lgkmcnt(0)\n"
"s_barrier\n"
"v_mfma_f32_32x32x16_bf16 v[240:255], a[188:191], a[60:63], v[240:255]\n"
"ds_read_b64_tr_b16 a[176:177], v22 offset:45056\n"
"ds_read_b64_tr_b16 a[178:179], v22 offset:45568\n"
"v_mul_f32 v164, v190, v165\n"
"v_fmac_f32 v164, v191, v173\n"
"v_mul_f32 v173, v106, v173\n"
"v_fmac_f32 v173, v191, v165\n"
"v_mfma_f32_32x32x16_bf16 v[192:207], a[160:163], a[64:67], v[192:207]\n"
"ds_read_b64_tr_b16 a[180:181], v23 offset:45056\n"
"ds_read_b64_tr_b16 a[182:183], v23 offset:45568\n"
"v_lshrrev_b32 v89, 5, v2\n"
"v_mul_u32_u24 v88, 1280, v88\n"
"v_lshl_add_u32 v88, v89, 4, v88\n"
"s_lshl_b32 s12, s53, 2\n"
"v_mfma_f32_32x32x16_bf16 v[208:223], a[164:167], a[64:67], v[208:223]\n"
"ds_read_b64_tr_b16 a[184:185], v24 offset:45056\n"
"ds_read_b64_tr_b16 a[186:187], v24 offset:45568\n"
"s_add_u32 s12, s12, s52\n"
"s_mul_i32 s12, s12, 40960\n"
"s_mul_i32 s13, s55, 320\n"
"s_add_u32 s12, s12, s13\n"
"v_mfma_f32_32x32x16_bf16 v[224:239], a[168:171], a[64:67], v[224:239]\n"
"ds_read_b64_tr_b16 a[188:189], v25 offset:45056\n"
"ds_read_b64_tr_b16 a[190:191], v25 offset:45568\n"
"s_add_u32 s13, s12, 256\n"
"s_lshl_b32 s14, s54, 6\n"
"s_add_u32 s12, s12, s14\n"
"s_cmp_eq_u32 s52, s54\n"
"s_cselect_b32 s13, s13, 0x70000000\n"
"v_mfma_f32_32x32x16_bf16 v[240:255], a[172:175], a[64:67], v[240:255]\n"
"s_waitcnt lgkmcnt(4)\n"
"ds_read_b64_tr_b16 a[160:161], v22 offset:49152\n"
"ds_read_b64_tr_b16 a[162:163], v22 offset:49664\n"
"buffer_load_dwordx4 a[240:243], v88, s[8:11], s12 offen offset:0\n"
"v_mul_f32 v165, v190, v166\n"
"v_fmac_f32 v165, v191, v174\n"
"v_mfma_f32_32x32x16_bf16 v[192:207], a[176:179], a[68:71], v[192:207]\n"
"ds_read_b64_tr_b16 a[164:165], v23 offset:49152\n"
"ds_read_b64_tr_b16 a[166:167], v23 offset:49664\n"
"v_mul_f32 v174, v106, v174\n"
"v_fmac_f32 v174, v191, v166\n"
"v_mul_f32 v166, v190, v167\n"
"v_fmac_f32 v166, v191, v175\n"
"v_mfma_f32_32x32x16_bf16 v[208:223], a[180:183], a[68:71], v[208:223]\n"
"s_waitcnt lgkmcnt(4)\n"
"ds_read_b64_tr_b16 a[168:169], v24 offset:49152\n"
"ds_read_b64_tr_b16 a[170:171], v24 offset:49664\n"
"v_mul_f32 v175, v106, v175\n"
"v_fmac_f32 v175, v191, v167\n"
"v_mfma_f32_32x32x16_bf16 v[224:239], a[184:187], a[68:71], v[224:239]\n"
"ds_read_b64_tr_b16 a[172:173], v25 offset:49152\n"
"ds_read_b64_tr_b16 a[174:175], v25 offset:49664\n"
"ds_read_b64 v[90:91], v74 offset:0\n"
"ds_read_b64 v[92:93], v74 offset:2304\n"
"ds_read_b64 v[94:95], v74 offset:4608\n"
"ds_read_b64 v[96:97], v74 offset:6912\n"
"v_mfma_f32_32x32x16_bf16 v[240:255], a[188:191], a[68:71], v[240:255]\n"
"s_waitcnt lgkmcnt(8)\n"
"ds_read_b64_tr_b16 a[176:177], v22 offset:53248\n"
"ds_read_b64_tr_b16 a[178:179], v22 offset:53760\n"
"v_mfma_f32_32x32x16_bf16 v[192:207], a[160:163], a[72:75], v[192:207]\n"
"ds_read_b64_tr_b16 a[180:181], v23 offset:53248\n"
"ds_read_b64_tr_b16 a[182:183], v23 offset:53760\n"
"v_mfma_f32_32x32x16_bf16 v[208:223], a[164:167], a[72:75], v[208:223]\n"
"s_waitcnt lgkmcnt(8)\n"
"ds_read_b64_tr_b16 a[184:185], v24 offset:53248\n"
"ds_read_b64_tr_b16 a[186:187], v24 offset:53760\n"
"buffer_load_dwordx4 a[244:247], v88, s[8:11], s12 offen offset:32\n"
"s_waitcnt lgkmcnt(9)\n"
"v_mul_f32 v98, v178, v90\n"
"v_mul_f32 v99, v179, v90\n"
"v_sub_f32 v100, 0, v177\n"
"v_fmac_f32 v98, v179, v91\n"
"v_fma_f32 v99, -v178, v91, v99\n"
"v_fmac_f32 v53, v176, v98\n"
"v_fmac_f32 v120, v176, v99\n"
"v_fmac_f32 v53, v177, v99\n"
"v_fmac_f32 v120, v100, v98\n"
"v_fmac_f32 v112, v176, v53\n"
"v_fmac_f32 v121, v176, v120\n"
"v_fmac_f32 v112, v177, v120\n"
"v_fmac_f32 v121, v100, v53\n"
"v_fmac_f32 v113, v176, v112\n"
"v_fmac_f32 v122, v176, v121\n"
"v_fmac_f32 v113, v177, v121\n"
"v_fmac_f32 v122, v100, v112\n"
"v_fmac_f32 v114, v176, v113\n"
"v_fmac_f32 v123, v176, v122\n"
"v_fmac_f32 v114, v177, v122\n"
"v_fmac_f32 v123, v100, v113\n"
"v_fmac_f32 v115, v176, v114\n"
"v_fmac_f32 v124, v176, v123\n"
"v_fmac_f32 v115, v177, v123\n"
"v_fmac_f32 v124, v100, v114\n"
"v_fmac_f32 v116, v176, v115\n"
"v_fmac_f32 v125, v176, v124\n"
"v_fmac_f32 v116, v177, v124\n"
"v_fmac_f32 v125, v100, v115\n"
"v_fmac_f32 v117, v176, v116\n"
"v_fmac_f32 v126, v176, v125\n"
"v_fmac_f32 v117, v177, v125\n"
"v_mfma_f32_32x32x16_bf16 v[224:239], a[168:171], a[72:75], v[224:239]\n"
"s_barrier\n"
"ds_read_b64_tr_b16 a[188:189], v25 offset:53248\n"
"ds_read_b64_tr_b16 a[190:191], v25 offset:53760\n"
"s_mul_i32 s66, s77, 1536\n"
"s_add_u32 m0, s57, 61440\n"
"s_nop 0\n"
"buffer_load_dwordx4 v34, s[20:23], s66 offen offset:0 lds\n"
"v_fmac_f32 v126, v100, v116\n"
"v_fmac_f32 v118, v176, v117\n"
"v_fmac_f32 v127, v176, v126\n"
"v_fmac_f32 v118, v177, v126\n"
"v_fmac_f32 v127, v100, v117\n"
"v_cvt_pk_bf16_f32 v104, v53, v112\n"
"v_cvt_pk_bf16_f32 v105, v113, v114\n"
"v_cvt_pk_bf16_f32 v106, v115, v116\n"
"v_cvt_pk_bf16_f32 v107, v117, v118\n"
"v_cvt_pk_bf16_f32 v108, v120, v121\n"
"v_cvt_pk_bf16_f32 v109, v122, v123\n"
"v_cvt_pk_bf16_f32 v110, v124, v125\n"
"v_cvt_pk_bf16_f32 v111, v126, v127\n"
"v_mfma_f32_32x32x16_bf16 v[240:255], a[172:175], a[72:75], v[240:255]\n"
"s_waitcnt lgkmcnt(4)\n"
"ds_read_b64_tr_b16 a[160:161], v22 offset:20480\n"
"ds_read_b64_tr_b16 a[162:163], v22 offset:20992\n"
"buffer_load_dwordx4 v35, s[20:23], s66 offen offset:1024 lds\n"
"ds_write_b128 v45, v[104:107]\n"
"ds_read_b64_tr_b16 a[192:193], v81\n"
"ds_read_b64_tr_b16 a[194:195], v81 offset:320\n"
"ds_write_b128 v45, v[108:111]\n"
"ds_read_b64_tr_b16 a[196:197], v81\n"
"ds_read_b64_tr_b16 a[198:199], v81 offset:320\n"
"v_mul_f32 v98, v182, v92\n"
"v_mul_f32 v99, v183, v92\n"
"v_sub_f32 v100, 0, v181\n"
"v_fmac_f32 v98, v183, v93\n"
"v_fma_f32 v99, -v182, v93, v99\n"
"v_fmac_f32 v119, v180, v98\n"
"v_fmac_f32 v136, v180, v99\n"
"v_fmac_f32 v119, v181, v99\n"
"v_fmac_f32 v136, v100, v98\n"
"v_fmac_f32 v128, v180, v119\n"
"v_fmac_f32 v137, v180, v136\n"
"v_fmac_f32 v128, v181, v136\n"
"v_fmac_f32 v137, v100, v119\n"
"v_fmac_f32 v129, v180, v128\n"
"v_fmac_f32 v138, v180, v137\n"
"v_fmac_f32 v129, v181, v137\n"
"v_fmac_f32 v138, v100, v128\n"
"v_fmac_f32 v130, v180, v129\n"
"v_fmac_f32 v139, v180, v138\n"
"v_fmac_f32 v130, v181, v138\n"
"v_fmac_f32 v139, v100, v129\n"
"v_fmac_f32 v131, v180, v130\n"
"v_fmac_f32 v140, v180, v139\n"
"v_fmac_f32 v131, v181, v139\n"
"v_fmac_f32 v140, v100, v130\n"
"v_fmac_f32 v132, v180, v131\n"
"v_mfma_f32_32x32x16_bf16 v[192:207], a[176:179], a[76:79], v[192:207]\n"
"ds_read_b64_tr_b16 a[164:165], v23 offset:20480\n"
"ds_read_b64_tr_b16 a[166:167], v23 offset:20992\n"
"s_waitcnt lgkmcnt(5)\n"
"buffer_store_dwordx4 a[192:195], v44, s[40:43], s78 offen offset:0 sc1\n"
"buffer_load_dwordx4 v36, s[24:27], s66 offen offset:2048 lds\n"
"v_fmac_f32 v141, v180, v140\n"
"v_fmac_f32 v132, v181, v140\n"
"v_fmac_f32 v141, v100, v131\n"
"v_fmac_f32 v133, v180, v132\n"
"v_fmac_f32 v142, v180, v141\n"
"v_fmac_f32 v133, v181, v141\n"
"v_fmac_f32 v142, v100, v132\n"
"v_fmac_f32 v134, v180, v133\n"
"v_fmac_f32 v143, v180, v142\n"
"v_fmac_f32 v134, v181, v142\n"
"v_fmac_f32 v143, v100, v133\n"
"v_cvt_pk_bf16_f32 v104, v119, v128\n"
"v_cvt_pk_bf16_f32 v105, v129, v130\n"
"v_cvt_pk_bf16_f32 v106, v131, v132\n"
"v_cvt_pk_bf16_f32 v107, v133, v134\n"
"v_cvt_pk_bf16_f32 v108, v136, v137\n"
"v_cvt_pk_bf16_f32 v109, v138, v139\n"
"v_cvt_pk_bf16_f32 v110, v140, v141\n"
"v_cvt_pk_bf16_f32 v111, v142, v143\n"
"v_mfma_f32_32x32x16_bf16 v[208:223], a[180:183], a[76:79], v[208:223]\n"
"ds_read_b64_tr_b16 a[168:169], v24 offset:20480\n"
"ds_read_b64_tr_b16 a[170:171], v24 offset:20992\n"
"s_waitcnt lgkmcnt(4)\n"
"buffer_store_dwordx4 a[196:199], v44, s[40:43], s78 offen offset:1024 sc1\n"
"buffer_load_dwordx4 v37, s[24:27], s66 offen offset:3072 lds\n"
"ds_write_b128 v45, v[104:107]\n"
"ds_read_b64_tr_b16 a[200:201], v81\n"
"ds_read_b64_tr_b16 a[202:203], v81 offset:320\n"
"ds_write_b128 v45, v[108:111]\n"
"ds_read_b64_tr_b16 a[204:205], v81\n"
"ds_read_b64_tr_b16 a[206:207], v81 offset:320\n"
"v_mul_f32 v98, v186, v94\n"
"v_mul_f32 v99, v187, v94\n"
"v_sub_f32 v100, 0, v185\n"
"v_fmac_f32 v98, v187, v95\n"
"v_fma_f32 v99, -v186, v95, v99\n"
"v_fmac_f32 v135, v184, v98\n"
"v_fmac_f32 v152, v184, v99\n"
"v_fmac_f32 v135, v185, v99\n"
"v_fmac_f32 v152, v100, v98\n"
"v_fmac_f32 v144, v184, v135\n"
"v_fmac_f32 v153, v184, v152\n"
"v_fmac_f32 v144, v185, v152\n"
"v_fmac_f32 v153, v100, v135\n"
"v_fmac_f32 v145, v184, v144\n"
"v_fmac_f32 v154, v184, v153\n"
"v_fmac_f32 v145, v185, v153\n"
"v_fmac_f32 v154, v100, v144\n"
"v_fmac_f32 v146, v184, v145\n"
"v_fmac_f32 v155, v184, v154\n"
"v_fmac_f32 v146, v185, v154\n"
"v_fmac_f32 v155, v100, v145\n"
"v_fmac_f32 v147, v184, v146\n"
"v_fmac_f32 v156, v184, v155\n"
"v_mfma_f32_32x32x16_bf16 v[224:239], a[184:187], a[76:79], v[224:239]\n"
"ds_read_b64_tr_b16 a[172:173], v25 offset:20480\n"
"ds_read_b64_tr_b16 a[174:175], v25 offset:20992\n"
"s_waitcnt lgkmcnt(5)\n"
"buffer_store_dwordx4 a[200:203], v44, s[40:43], s78 offen offset:2048 sc1\n"
"v_fmac_f32 v147, v185, v155\n"
"v_fmac_f32 v156, v100, v146\n"
"v_fmac_f32 v148, v184, v147\n"
"v_fmac_f32 v157, v184, v156\n"
"v_fmac_f32 v148, v185, v156\n"
"v_fmac_f32 v157, v100, v147\n"
"v_fmac_f32 v149, v184, v148\n"
"v_fmac_f32 v158, v184, v157\n"
"v_fmac_f32 v149, v185, v157\n"
"v_fmac_f32 v158, v100, v148\n"
"v_fmac_f32 v150, v184, v149\n"
"v_fmac_f32 v159, v184, v158\n"
"v_fmac_f32 v150, v185, v158\n"
"v_fmac_f32 v159, v100, v149\n"
"v_cvt_pk_bf16_f32 v104, v135, v144\n"
"v_cvt_pk_bf16_f32 v105, v145, v146\n"
"v_cvt_pk_bf16_f32 v106, v147, v148\n"
"v_cvt_pk_bf16_f32 v107, v149, v150\n"
"v_cvt_pk_bf16_f32 v108, v152, v153\n"
"v_cvt_pk_bf16_f32 v109, v154, v155\n"
"v_cvt_pk_bf16_f32 v110, v156, v157\n"
"v_cvt_pk_bf16_f32 v111, v158, v159\n"
"v_mfma_f32_32x32x16_bf16 v[240:255], a[188:191], a[76:79], v[240:255]\n"
"ds_read_b64_tr_b16 a[176:177], v22 offset:24576\n"
"ds_read_b64_tr_b16 a[178:179], v22 offset:25088\n"
"s_waitcnt lgkmcnt(4)\n"
"buffer_store_dwordx4 a[204:207], v44, s[40:43], s78 offen offset:3072 sc1\n"
"ds_write_b128 v45, v[104:107]\n"
"ds_read_b64_tr_b16 a[208:209], v81\n"
"ds_read_b64_tr_b16 a[210:211], v81 offset:320\n"
"ds_write_b128 v45, v[108:111]\n"
"ds_read_b64_tr_b16 a[212:213], v81\n"
"ds_read_b64_tr_b16 a[214:215], v81 offset:320\n"
"v_mul_f32 v98, v190, v96\n"
"v_mul_f32 v99, v191, v96\n"
"v_sub_f32 v100, 0, v189\n"
"v_fmac_f32 v98, v191, v97\n"
"v_fma_f32 v99, -v190, v97, v99\n"
"v_fmac_f32 v151, v188, v98\n"
"v_fmac_f32 v168, v188, v99\n"
"v_fmac_f32 v151, v189, v99\n"
"v_fmac_f32 v168, v100, v98\n"
"v_fmac_f32 v160, v188, v151\n"
"v_fmac_f32 v169, v188, v168\n"
"v_fmac_f32 v160, v189, v168\n"
"v_fmac_f32 v169, v100, v151\n"
"v_fmac_f32 v161, v188, v160\n"
"v_fmac_f32 v170, v188, v169\n"
"v_fmac_f32 v161, v189, v169\n"
"v_fmac_f32 v170, v100, v160\n"
"v_fmac_f32 v162, v188, v161\n"
"v_fmac_f32 v171, v188, v170\n"
"v_fmac_f32 v162, v189, v170\n"
"v_fmac_f32 v171, v100, v161\n"
"v_fmac_f32 v163, v188, v162\n"
"v_fmac_f32 v172, v188, v171\n"
"v_fmac_f32 v163, v189, v171\n"
"v_fmac_f32 v172, v100, v162\n"
"v_mfma_f32_32x32x16_bf16 v[192:207], a[160:163], a[80:83], v[192:207]\n"
"ds_read_b64_tr_b16 a[180:181], v23 offset:24576\n"
"ds_read_b64_tr_b16 a[182:183], v23 offset:25088\n"
"s_waitcnt lgkmcnt(5)\n"
"buffer_store_dwordx4 a[208:211], v44, s[40:43], s79 offen offset:0 sc1\n"
"s_lshl_b32 s3, s54, 2\n"
"v_fmac_f32 v164, v188, v163\n"
"v_fmac_f32 v173, v188, v172\n"
"v_fmac_f32 v164, v189, v172\n"
"v_fmac_f32 v173, v100, v163\n"
"v_fmac_f32 v165, v188, v164\n"
"v_fmac_f32 v174, v188, v173\n"
"v_fmac_f32 v165, v189, v173\n"
"v_fmac_f32 v174, v100, v164\n"
"v_fmac_f32 v166, v188, v165\n"
"v_fmac_f32 v175, v188, v174\n"
"v_fmac_f32 v166, v189, v174\n"
"v_fmac_f32 v175, v100, v165\n"
"v_cvt_pk_bf16_f32 v104, v151, v160\n"
"v_cvt_pk_bf16_f32 v105, v161, v162\n"
"v_cvt_pk_bf16_f32 v106, v163, v164\n"
"v_cvt_pk_bf16_f32 v107, v165, v166\n"
"v_cvt_pk_bf16_f32 v108, v168, v169\n"
"v_cvt_pk_bf16_f32 v109, v170, v171\n"
"v_cvt_pk_bf16_f32 v110, v172, v173\n"
"v_cvt_pk_bf16_f32 v111, v174, v175\n"
"v_mfma_f32_32x32x16_bf16 v[208:223], a[164:167], a[80:83], v[208:223]\n"
"ds_read_b64_tr_b16 a[184:185], v24 offset:24576\n"
"ds_read_b64_tr_b16 a[186:187], v24 offset:25088\n"
"s_waitcnt lgkmcnt(4)\n"
"buffer_store_dwordx4 a[212:215], v44, s[40:43], s79 offen offset:1024 sc1\n"
"ds_write_b128 v45, v[104:107]\n"
"ds_read_b64_tr_b16 a[216:217], v81\n"
"ds_read_b64_tr_b16 a[218:219], v81 offset:320\n"
"ds_write_b128 v45, v[108:111]\n"
"ds_read_b64_tr_b16 a[220:221], v81\n"
"ds_read_b64_tr_b16 a[222:223], v81 offset:320\n"
"v_mfma_f32_32x32x16_bf16 v[224:239], a[168:171], a[80:83], v[224:239]\n"
"ds_read_b64_tr_b16 a[188:189], v25 offset:24576\n"
"ds_read_b64_tr_b16 a[190:191], v25 offset:25088\n"
"s_add_u32 s3, s3, s55\n"
"s_mul_i32 s3, s3, 10240\n"
"s_add_u32 s3, s3, 0x1681400\n"
"s_add_u32 s2, s3, 4096\n"
"v_mfma_f32_32x32x16_bf16 v[240:255], a[172:175], a[80:83], v[240:255]\n"
"s_waitcnt lgkmcnt(10)\n"
"ds_read_b64_tr_b16 a[160:161], v22 offset:28672\n"
"ds_read_b64_tr_b16 a[162:163], v22 offset:29184\n"
"v_mfma_f32_32x32x16_bf16 v[192:207], a[176:179], a[84:87], v[192:207]\n"
"ds_read_b64_tr_b16 a[164:165], v23 offset:28672\n"
"ds_read_b64_tr_b16 a[166:167], v23 offset:29184\n"
"buffer_load_dwordx4 a[224:227], v44, s[40:43], s3 offen offset:0 sc1\n"
"v_mfma_f32_32x32x16_bf16 v[208:223], a[180:183], a[84:87], v[208:223]\n"
"s_waitcnt lgkmcnt(4)\n"
"ds_read_b64_tr_b16 a[168:169], v24 offset:28672\n"
"ds_read_b64_tr_b16 a[170:171], v24 offset:29184\n"
"v_mfma_f32_32x32x16_bf16 v[224:239], a[184:187], a[84:87], v[224:239]\n"
"ds_read_b64_tr_b16 a[172:173], v25 offset:28672\n"
"ds_read_b64_tr_b16 a[174:175], v25 offset:29184\n"
"buffer_store_dwordx4 a[216:219], v44, s[40:43], s79 offen offset:2048 sc1\n"
"v_mfma_f32_32x32x16_bf16 v[240:255], a[188:191], a[84:87], v[240:255]\n"
"s_waitcnt lgkmcnt(4)\n"
"ds_read_b64_tr_b16 a[176:177], v22 offset:32768\n"
"ds_read_b64_tr_b16 a[178:179], v22 offset:33280\n"
"v_mfma_f32_32x32x16_bf16 v[192:207], a[160:163], a[88:91], v[192:207]\n"
"ds_read_b64_tr_b16 a[180:181], v23 offset:32768\n"
"ds_read_b64_tr_b16 a[182:183], v23 offset:33280\n"
"v_mfma_f32_32x32x16_bf16 v[208:223], a[164:167], a[88:91], v[208:223]\n"
"s_waitcnt lgkmcnt(4)\n"
"ds_read_b64_tr_b16 a[184:185], v24 offset:32768\n"
"ds_read_b64_tr_b16 a[186:187], v24 offset:33280\n"
"buffer_store_dwordx4 a[220:223], v44, s[40:43], s79 offen offset:3072 sc1\n"
"v_mfma_f32_32x32x16_bf16 v[224:239], a[168:171], a[88:91], v[224:239]\n"
"s_waitcnt vmcnt(24)\n"
"s_barrier\n"
"ds_read_b64_tr_b16 a[188:189], v25 offset:32768\n"
"ds_read_b64_tr_b16 a[190:191], v25 offset:33280\n"
"s_mov_b32 s66, s63\n"
"s_lshl_b32 s66, s66, 5\n"
"s_add_u32 m0, s59, 102400\n"
"v_mfma_f32_32x32x16_bf16 v[240:255], a[172:175], a[88:91], v[240:255]\n"
"s_waitcnt lgkmcnt(4)\n"
"ds_read_b64_tr_b16 a[160:161], v18 offset:0\n"
"ds_read_b64_tr_b16 a[162:163], v18 offset:512\n"
"v_mfma_f32_32x32x16_bf16 v[192:207], a[176:179], a[92:95], v[192:207]\n"
"ds_read_b64_tr_b16 a[164:165], v19 offset:0\n"
"ds_read_b64_tr_b16 a[166:167], v19 offset:512\n"
"v_mfma_f32_32x32x16_bf16 v[208:223], a[180:183], a[92:95], v[208:223]\n"
"s_waitcnt lgkmcnt(4)\n"
"ds_read_b64_tr_b16 a[168:169], v20 offset:0\n"
"ds_read_b64_tr_b16 a[170:171], v20 offset:512\n"
"buffer_load_dwordx4 a[228:231], v44, s[40:43], s3 offen offset:1024 sc1\n"
"v_mfma_f32_32x32x16_bf16 v[224:239], a[184:187], a[92:95], v[224:239]\n"
"ds_read_b64_tr_b16 a[172:173], v21 offset:0\n"
"ds_read_b64_tr_b16 a[174:175], v21 offset:512\n"
"v_mfma_f32_32x32x16_bf16 v[240:255], a[188:191], a[92:95], v[240:255]\n"
"s_waitcnt lgkmcnt(4)\n"
"ds_read_b64_tr_b16 a[176:177], v18 offset:4096\n"
"ds_read_b64_tr_b16 a[178:179], v18 offset:4608\n"
"buffer_load_dwordx4 a[232:235], v44, s[40:43], s3 offen offset:2048 sc1\n"
"v_mfma_f32_32x32x16_bf16 v[192:207], a[160:163], a[96:99], v[192:207]\n"
"ds_read_b64_tr_b16 a[180:181], v19 offset:4096\n"
"ds_read_b64_tr_b16 a[182:183], v19 offset:4608\n"
"buffer_load_dwordx4 a[236:239], v44, s[40:43], s3 offen offset:3072 sc1\n"
"v_mfma_f32_32x32x16_bf16 v[208:223], a[164:167], a[96:99], v[208:223]\n"
"s_waitcnt lgkmcnt(4)\n"
"ds_read_b64_tr_b16 a[184:185], v20 offset:4096\n"
"ds_read_b64_tr_b16 a[186:187], v20 offset:4608\n"
"v_mfma_f32_32x32x16_bf16 v[224:239], a[168:171], a[96:99], v[224:239]\n"
"ds_read_b64_tr_b16 a[188:189], v21 offset:4096\n"
"ds_read_b64_tr_b16 a[190:191], v21 offset:4608\n"
"buffer_load_dwordx4 a[248:251], v88, s[8:11], s13 offen offset:0\n"
"v_mfma_f32_32x32x16_bf16 v[240:255], a[172:175], a[96:99], v[240:255]\n"
"s_waitcnt lgkmcnt(4)\n"
"ds_read_b64_tr_b16 a[160:161], v18 offset:8192\n"
"ds_read_b64_tr_b16 a[162:163], v18 offset:8704\n"
"v_mfma_f32_32x32x16_bf16 v[192:207], a[176:179], a[100:103], v[192:207]\n"
"ds_read_b64_tr_b16 a[164:165], v19 offset:8192\n"
"ds_read_b64_tr_b16 a[166:167], v19 offset:8704\n"
"v_mfma_f32_32x32x16_bf16 v[208:223], a[180:183], a[100:103], v[208:223]\n"
"s_waitcnt lgkmcnt(4)\n"
"ds_read_b64_tr_b16 a[168:169], v20 offset:8192\n"
"ds_read_b64_tr_b16 a[170:171], v20 offset:8704\n"
"v_mfma_f32_32x32x16_bf16 v[224:239], a[184:187], a[100:103], v[224:239]\n"
"ds_read_b64_tr_b16 a[172:173], v21 offset:8192\n"
"ds_read_b64_tr_b16 a[174:175], v21 offset:8704\n"
"buffer_load_dwordx4 a[252:255], v88, s[8:11], s13 offen offset:32\n"
"v_mfma_f32_32x32x16_bf16 v[240:255], a[188:191], a[100:103], v[240:255]\n"
"s_waitcnt lgkmcnt(4)\n"
"ds_read_b64_tr_b16 a[176:177], v18 offset:12288\n"
"ds_read_b64_tr_b16 a[178:179], v18 offset:12800\n"
"v_mfma_f32_32x32x16_bf16 v[192:207], a[160:163], a[104:107], v[192:207]\n"
"ds_read_b64_tr_b16 a[180:181], v19 offset:12288\n"
"ds_read_b64_tr_b16 a[182:183], v19 offset:12800\n"
"v_mfma_f32_32x32x16_bf16 v[208:223], a[164:167], a[104:107], v[208:223]\n"
"s_waitcnt lgkmcnt(4)\n"
"ds_read_b64_tr_b16 a[184:185], v20 offset:12288\n"
"ds_read_b64_tr_b16 a[186:187], v20 offset:12800\n"
"buffer_load_dwordx4 v30, s[32:35], s66 offen offset:0 lds\n"
"v_mfma_f32_32x32x16_bf16 v[224:239], a[168:171], a[104:107], v[224:239]\n"
"s_waitcnt vmcnt(26)\n"
"s_waitcnt vmcnt(21)\n"
"s_waitcnt vmcnt(13)\n"
"s_barrier\n"
"ds_read_b64_tr_b16 a[188:189], v21 offset:12288\n"
"ds_read_b64_tr_b16 a[190:191], v21 offset:12800\n"
"v_mfma_f32_32x32x16_bf16 v[240:255], a[172:175], a[104:107], v[240:255]\n"
"s_waitcnt lgkmcnt(4)\n"
"ds_read_b64_tr_b16 a[160:161], v18 offset:20480\n"
"ds_read_b64_tr_b16 a[162:163], v18 offset:20992\n"
"buffer_load_dwordx4 v31, s[32:35], s66 offen offset:1024 lds\n"
"v_mfma_f32_32x32x16_bf16 v[192:207], a[176:179], a[108:111], v[192:207]\n"
"ds_read_b64_tr_b16 a[164:165], v18 offset:24576\n"
"ds_read_b64_tr_b16 a[166:167], v18 offset:25088\n"
"v_mfma_f32_32x32x16_bf16 v[208:223], a[180:183], a[108:111], v[208:223]\n"
"s_waitcnt lgkmcnt(4)\n"
"ds_read_b64_tr_b16 a[168:169], v18 offset:28672\n"
"ds_read_b64_tr_b16 a[170:171], v18 offset:29184\n"
"buffer_load_dwordx4 v32, s[32:35], s66 offen offset:2048 lds\n"
"v_mfma_f32_32x32x16_bf16 v[224:239], a[184:187], a[108:111], v[224:239]\n"
"ds_read_b64_tr_b16 a[172:173], v18 offset:32768\n"
"ds_read_b64_tr_b16 a[174:175], v18 offset:33280\n"
"v_mfma_f32_32x32x16_bf16 v[240:255], a[188:191], a[108:111], v[240:255]\n"
"s_waitcnt lgkmcnt(4)\n"
"ds_read_b64_tr_b16 a[176:177], v18 offset:40960\n"
"ds_read_b64_tr_b16 a[178:179], v18 offset:41472\n"
"buffer_load_dwordx4 v33, s[32:35], s66 offen offset:3072 lds\n"
"s_add_u32 m0, s59, 106496\n"
"v_mfma_f32_32x32x16_bf16 v[192:207], a[160:163], a[112:115], v[192:207]\n"
"ds_read_b64_tr_b16 a[180:181], v18 offset:45056\n"
"ds_read_b64_tr_b16 a[182:183], v18 offset:45568\n"
"v_mfma_f32_32x32x16_bf16 v[192:207], a[164:167], a[116:119], v[192:207]\n"
"s_waitcnt lgkmcnt(4)\n"
"ds_read_b64_tr_b16 a[184:185], v18 offset:49152\n"
"ds_read_b64_tr_b16 a[186:187], v18 offset:49664\n"
"buffer_load_dwordx4 v38, s[32:35], s66 offen lds\n"
"s_mov_b32 s66, s63\n"
"s_lshl_b32 s66, s66, 5\n"
"s_add_u32 m0, s59, 81920\n"
"v_mfma_f32_32x32x16_bf16 v[192:207], a[168:171], a[120:123], v[192:207]\n"
"ds_read_b64_tr_b16 a[188:189], v18 offset:53248\n"
"ds_read_b64_tr_b16 a[190:191], v18 offset:53760\n"
"v_mfma_f32_32x32x16_bf16 v[192:207], a[172:175], a[124:127], v[192:207]\n"
"s_waitcnt lgkmcnt(4)\n"
"ds_read_b64_tr_b16 a[160:161], v22 offset:0\n"
"ds_read_b64_tr_b16 a[162:163], v22 offset:512\n"
"buffer_load_dwordx4 v30, s[36:39], s66 offen offset:0 lds\n"
"v_mfma_f32_32x32x16_bf16 v[192:207], a[176:179], a[128:131], v[192:207]\n"
"ds_read_b64_tr_b16 a[164:165], v22 offset:4096\n"
"ds_read_b64_tr_b16 a[166:167], v22 offset:4608\n"
"v_mfma_f32_32x32x16_bf16 v[192:207], a[180:183], a[132:135], v[192:207]\n"
"s_waitcnt lgkmcnt(4)\n"
"ds_read_b64_tr_b16 a[168:169], v22 offset:8192\n"
"ds_read_b64_tr_b16 a[170:171], v22 offset:8704\n"
"buffer_load_dwordx4 v31, s[36:39], s66 offen offset:1024 lds\n"
"v_mfma_f32_32x32x16_bf16 v[192:207], a[184:187], a[136:139], v[192:207]\n"
"v_mfma_f32_32x32x16_bf16 v[192:207], a[188:191], a[140:143], v[192:207]\n"
"s_waitcnt lgkmcnt(2)\n"
"ds_read_b64_tr_b16 a[172:173], v22 offset:12288\n"
"ds_read_b64_tr_b16 a[174:175], v22 offset:12800\n"
"buffer_load_dwordx4 v32, s[36:39], s66 offen offset:2048 lds\n"
"v_mfma_f32_32x32x16_bf16 v[192:207], a[160:163], a[144:147], v[192:207]\n"
"ds_read_b64_tr_b16 a[176:177], v19 offset:20480\n"
"ds_read_b64_tr_b16 a[178:179], v19 offset:20992\n"
"v_mfma_f32_32x32x16_bf16 v[192:207], a[164:167], a[148:151], v[192:207]\n"
"s_waitcnt vmcnt(15) lgkmcnt(4)\n"
"ds_read_b64_tr_b16 a[180:181], v19 offset:24576\n"
"ds_read_b64_tr_b16 a[182:183], v19 offset:25088\n"
"buffer_load_dwordx4 v33, s[36:39], s66 offen offset:3072 lds\n"
"s_add_u32 m0, s59, 86016\n"
"v_mfma_f32_32x32x16_bf16 v[192:207], a[168:171], a[152:155], v[192:207]\n"
"ds_read_b64_tr_b16 a[184:185], v19 offset:28672\n"
"ds_read_b64_tr_b16 a[186:187], v19 offset:29184\n"
"v_accvgpr_read_b32 v83, a225\n"
"v_accvgpr_read_b32 v84, a227\n"
"v_xor_b32 v83, s56, v83\n"
"v_xor_b32 v84, s56, v84\n"
"v_or_b32 v82, v83, v84\n"
"s_waitcnt vmcnt(13)\n"
"v_accvgpr_read_b32 v83, a229\n"
"v_accvgpr_read_b32 v84, a231\n"
"v_xor_b32 v83, s56, v83\n"
"v_xor_b32 v84, s56, v84\n"
"v_or3_b32 v82, v82, v83, v84\n"
"s_waitcnt vmcnt(12)\n"
"v_accvgpr_read_b32 v83, a233\n"
"v_accvgpr_read_b32 v84, a235\n"
"v_xor_b32 v83, s56, v83\n"
"v_xor_b32 v84, s56, v84\n"
"v_or3_b32 v82, v82, v83, v84\n"
"s_waitcnt vmcnt(11)\n"
"v_accvgpr_read_b32 v83, a237\n"
"v_accvgpr_read_b32 v84, a239\n"
"v_xor_b32 v83, s56, v83\n"
"v_xor_b32 v84, s56, v84\n"
"v_or3_b32 v82, v82, v83, v84\n"
"v_cmp_ne_u32 s[0:1], 0, v82\n"
"s_nop 0\n"
"s_cmp_lg_u64 s[0:1], 0\n"
"s_cbranch_scc1 Lcs1B_slow%=\n"
"Lcs1B_ok%=:\n"
"v_accvgpr_write_b32 a225, 0\n"
"v_accvgpr_write_b32 a227, 0\n"
"v_accvgpr_write_b32 a229, 0\n"
"v_accvgpr_write_b32 a231, 0\n"
"v_accvgpr_write_b32 a233, 0\n"
"v_accvgpr_write_b32 a235, 0\n"
"v_accvgpr_write_b32 a237, 0\n"
"v_accvgpr_write_b32 a239, 0\n"
"v_mfma_f32_32x32x16_bf16 v[192:207], a[224:227], v[54:57], v[192:207]\n"
"s_waitcnt lgkmcnt(4)\n"
"ds_read_b64_tr_b16 a[188:189], v19 offset:32768\n"
"ds_read_b64_tr_b16 a[190:191], v19 offset:33280\n"
"buffer_load_dwordx4 v38, s[36:39], s66 offen lds\n"
"v_mfma_f32_32x32x16_bf16 v[192:207], a[172:175], a[156:159], v[192:207]\n"
"ds_read_b64_tr_b16 a[160:161], v19 offset:40960\n"
"ds_read_b64_tr_b16 a[162:163], v19 offset:41472\n"
"v_mfma_f32_32x32x16_bf16 v[208:223], a[176:179], a[112:115], v[208:223]\n"
"s_waitcnt lgkmcnt(4)\n"
"ds_read_b64_tr_b16 a[164:165], v19 offset:45056\n"
"ds_read_b64_tr_b16 a[166:167], v19 offset:45568\n"
"v_mfma_f32_32x32x16_bf16 v[208:223], a[180:183], a[116:119], v[208:223]\n"
"ds_read_b64_tr_b16 a[168:169], v19 offset:49152\n"
"ds_read_b64_tr_b16 a[170:171], v19 offset:49664\n"
"s_nop 2\n"
"v_mul_f32 v82, v192, v192\n"
"v_mul_f32 v83, v193, v193\n"
"v_fma_f32 v82, v51, v82, v50\n"
"v_fma_f32 v83, v51, v83, v50\n"
"v_mfma_f32_32x32x16_bf16 v[208:223], a[184:187], a[120:123], v[208:223]\n"
"s_waitcnt lgkmcnt(4)\n"
"ds_read_b64_tr_b16 a[172:173], v19 offset:53248\n"
"ds_read_b64_tr_b16 a[174:175], v19 offset:53760\n"
"v_mul_f32 v82, v192, v82\n"
"v_mul_f32 v83, v193, v83\n"
"v_exp_f32 v82, v82\n"
"v_mfma_f32_32x32x16_bf16 v[208:223], a[188:191], a[124:127], v[208:223]\n"
"ds_read_b64_tr_b16 a[176:177], v23 offset:0\n"
"ds_read_b64_tr_b16 a[178:179], v23 offset:512\n"
"buffer_store_dword v7, v4, s[40:43], s4 offen offset:128 sc1\n"
"v_mul_f32 v200, v192, v200\n"
"v_mfma_f32_32x32x16_bf16 v[208:223], a[160:163], a[128:131], v[208:223]\n"
"s_waitcnt lgkmcnt(4)\n"
"ds_read_b64_tr_b16 a[180:181], v23 offset:4096\n"
"ds_read_b64_tr_b16 a[182:183], v23 offset:4608\n"
"v_exp_f32 v83, v83\n"
"v_mul_f32 v201, v193, v201\n"
"v_fma_f32 v82, v82, v52, v52\n"
"v_mfma_f32_32x32x16_bf16 v[208:223], a[164:167], a[132:135], v[208:223]\n"
"ds_read_b64_tr_b16 a[184:185], v23 offset:8192\n"
"ds_read_b64_tr_b16 a[186:187], v23 offset:8704\n"
"v_fma_f32 v83, v83, v52, v52\n"
"v_rcp_f32 v82, v82\n"
"v_mfma_f32_32x32x16_bf16 v[208:223], a[168:171], a[136:139], v[208:223]\n"
"s_waitcnt lgkmcnt(4)\n"
"v_rcp_f32 v83, v83\n"
"v_mul_f32 v200, v200, v82\n"
"v_mul_f32 v201, v201, v83\n"
"v_cvt_pk_bf16_f32 v192, v200, v201\n"
"v_mul_f32 v84, v194, v194\n"
"v_mfma_f32_32x32x16_bf16 v[208:223], a[172:175], a[140:143], v[208:223]\n"
"ds_read_b64_tr_b16 a[188:189], v23 offset:12288\n"
"ds_read_b64_tr_b16 a[190:191], v23 offset:12800\n"
"v_mul_f32 v85, v195, v195\n"
"v_fma_f32 v84, v51, v84, v50\n"
"v_fma_f32 v85, v51, v85, v50\n"
"v_mul_f32 v84, v194, v84\n"
"v_mfma_f32_32x32x16_bf16 v[208:223], a[176:179], a[144:147], v[208:223]\n"
"s_waitcnt lgkmcnt(2)\n"
"ds_read_b64_tr_b16 a[160:161], v20 offset:20480\n"
"ds_read_b64_tr_b16 a[162:163], v20 offset:20992\n"
"v_mul_f32 v85, v195, v85\n"
"v_exp_f32 v84, v84\n"
"v_mul_f32 v202, v194, v202\n"
"v_mfma_f32_32x32x16_bf16 v[208:223], a[180:183], a[148:151], v[208:223]\n"
"ds_read_b64_tr_b16 a[164:165], v20 offset:24576\n"
"ds_read_b64_tr_b16 a[166:167], v20 offset:25088\n"
"v_exp_f32 v85, v85\n"
"v_mul_f32 v203, v195, v203\n"
"v_fma_f32 v84, v84, v52, v52\n"
"v_mfma_f32_32x32x16_bf16 v[208:223], a[184:187], a[152:155], v[208:223]\n"
"s_waitcnt lgkmcnt(4)\n"
"ds_read_b64_tr_b16 a[168:169], v20 offset:28672\n"
"ds_read_b64_tr_b16 a[170:171], v20 offset:29184\n"
"v_fma_f32 v85, v85, v52, v52\n"
"v_rcp_f32 v84, v84\n"
"v_mfma_f32_32x32x16_bf16 v[208:223], a[228:231], v[54:57], v[208:223]\n"
"ds_read_b64_tr_b16 a[172:173], v20 offset:32768\n"
"ds_read_b64_tr_b16 a[174:175], v20 offset:33280\n"
"v_rcp_f32 v85, v85\n"
"v_mul_f32 v202, v202, v84\n"
"v_mul_f32 v203, v203, v85\n"
"v_mfma_f32_32x32x16_bf16 v[208:223], a[188:191], a[156:159], v[208:223]\n"
"s_waitcnt lgkmcnt(4)\n"
"ds_read_b64_tr_b16 a[176:177], v20 offset:40960\n"
"ds_read_b64_tr_b16 a[178:179], v20 offset:41472\n"
"v_cvt_pk_bf16_f32 v193, v202, v203\n"
"v_mul_f32 v82, v196, v196\n"
"v_mul_f32 v83, v197, v197\n"
"v_fma_f32 v82, v51, v82, v50\n"
"v_mfma_f32_32x32x16_bf16 v[224:239], a[160:163], a[112:115], v[224:239]\n"
"ds_read_b64_tr_b16 a[180:181], v20 offset:45056\n"
"ds_read_b64_tr_b16 a[182:183], v20 offset:45568\n"
"v_fma_f32 v83, v51, v83, v50\n"
"v_mul_f32 v82, v196, v82\n"
"v_mul_f32 v83, v197, v83\n"
"v_mfma_f32_32x32x16_bf16 v[224:239], a[164:167], a[116:119], v[224:239]\n"
"s_waitcnt lgkmcnt(4)\n"
"ds_read_b64_tr_b16 a[184:185], v20 offset:49152\n"
"ds_read_b64_tr_b16 a[186:187], v20 offset:49664\n"
"v_exp_f32 v82, v82\n"
"v_mul_f32 v204, v196, v204\n"
"v_mfma_f32_32x32x16_bf16 v[224:239], a[168:171], a[120:123], v[224:239]\n"
"ds_read_b64_tr_b16 a[188:189], v20 offset:53248\n"
"ds_read_b64_tr_b16 a[190:191], v20 offset:53760\n"
"v_exp_f32 v83, v83\n"
"v_mul_f32 v205, v197, v205\n"
"v_fma_f32 v82, v82, v52, v52\n"
"v_mfma_f32_32x32x16_bf16 v[224:239], a[172:175], a[124:127], v[224:239]\n"
"s_waitcnt lgkmcnt(4)\n"
"ds_read_b64_tr_b16 a[160:161], v24 offset:0\n"
"ds_read_b64_tr_b16 a[162:163], v24 offset:512\n"
"v_fma_f32 v83, v83, v52, v52\n"
"v_rcp_f32 v82, v82\n"
"v_mfma_f32_32x32x16_bf16 v[224:239], a[176:179], a[128:131], v[224:239]\n"
"ds_read_b64_tr_b16 a[164:165], v24 offset:4096\n"
"ds_read_b64_tr_b16 a[166:167], v24 offset:4608\n"
"v_rcp_f32 v83, v83\n"
"v_mul_f32 v204, v204, v82\n"
"v_mul_f32 v205, v205, v83\n"
"v_mfma_f32_32x32x16_bf16 v[224:239], a[180:183], a[132:135], v[224:239]\n"
"s_waitcnt lgkmcnt(4)\n"
"ds_read_b64_tr_b16 a[168:169], v24 offset:8192\n"
"ds_read_b64_tr_b16 a[170:171], v24 offset:8704\n"
"v_cvt_pk_bf16_f32 v194, v204, v205\n"
"v_mul_f32 v84, v198, v198\n"
"v_mul_f32 v85, v199, v199\n"
"v_fma_f32 v84, v51, v84, v50\n"
"v_mfma_f32_32x32x16_bf16 v[224:239], a[184:187], a[136:139], v[224:239]\n"
"v_fma_f32 v85, v51, v85, v50\n"
"v_mul_f32 v84, v198, v84\n"
"v_mul_f32 v85, v199, v85\n"
"v_exp_f32 v84, v84\n"
"v_mul_f32 v206, v198, v206\n"
"v_mfma_f32_32x32x16_bf16 v[224:239], a[188:191], a[140:143], v[224:239]\n"
"s_waitcnt lgkmcnt(2)\n"
"ds_read_b64_tr_b16 a[172:173], v24 offset:12288\n"
"ds_read_b64_tr_b16 a[174:175], v24 offset:12800\n"
"v_exp_f32 v85, v85\n"
"v_mul_f32 v207, v199, v207\n"
"v_fma_f32 v84, v84, v52, v52\n"
"v_mfma_f32_32x32x16_bf16 v[224:239], a[160:163], a[144:147], v[224:239]\n"
"ds_read_b64_tr_b16 a[176:177], v21 offset:20480\n"
"ds_read_b64_tr_b16 a[178:179], v21 offset:20992\n"
"v_fma_f32 v85, v85, v52, v52\n"
"v_rcp_f32 v84, v84\n"
"v_mfma_f32_32x32x16_bf16 v[224:239], a[164:167], a[148:151], v[224:239]\n"
"s_waitcnt lgkmcnt(4)\n"
"ds_read_b64_tr_b16 a[180:181], v21 offset:24576\n"
"ds_read_b64_tr_b16 a[182:183], v21 offset:25088\n"
"v_rcp_f32 v85, v85\n"
"v_mul_f32 v206, v206, v84\n"
"v_mul_f32 v207, v207, v85\n"
"v_mfma_f32_32x32x16_bf16 v[224:239], a[168:171], a[152:155], v[224:239]\n"
"ds_read_b64_tr_b16 a[184:185], v21 offset:28672\n"
"ds_read_b64_tr_b16 a[186:187], v21 offset:29184\n"
"v_cvt_pk_bf16_f32 v195, v206, v207\n"
"v_mul_f32 v82, v208, v208\n"
"v_mul_f32 v83, v209, v209\n"
"v_fma_f32 v82, v51, v82, v50\n"
"v_mfma_f32_32x32x16_bf16 v[224:239], a[232:235], v[54:57], v[224:239]\n"
"s_waitcnt lgkmcnt(4)\n"
"ds_read_b64_tr_b16 a[188:189], v21 offset:32768\n"
"ds_read_b64_tr_b16 a[190:191], v21 offset:33280\n"
"buffer_store_dwordx4 v[192:195], v44, s[40:43], s67 offen offset:0 sc1\n"
"v_fma_f32 v83, v51, v83, v50\n"
"v_mfma_f32_32x32x16_bf16 v[224:239], a[172:175], a[156:159], v[224:239]\n"
"ds_read_b64_tr_b16 a[160:161], v21 offset:40960\n"
"ds_read_b64_tr_b16 a[162:163], v21 offset:41472\n"
"v_mul_f32 v82, v208, v82\n"
"v_mul_f32 v83, v209, v83\n"
"v_exp_f32 v82, v82\n"
"v_mfma_f32_32x32x16_bf16 v[240:255], a[176:179], a[112:115], v[240:255]\n"
"s_waitcnt lgkmcnt(4)\n"
"ds_read_b64_tr_b16 a[164:165], v21 offset:45056\n"
"ds_read_b64_tr_b16 a[166:167], v21 offset:45568\n"
"v_mul_f32 v216, v208, v216\n"
"v_exp_f32 v83, v83\n"
"v_mul_f32 v217, v209, v217\n"
"v_mfma_f32_32x32x16_bf16 v[240:255], a[180:183], a[116:119], v[240:255]\n"
"ds_read_b64_tr_b16 a[168:169], v21 offset:49152\n"
"ds_read_b64_tr_b16 a[170:171], v21 offset:49664\n"
"v_fma_f32 v82, v82, v52, v52\n"
"v_fma_f32 v83, v83, v52, v52\n"
"v_rcp_f32 v82, v82\n"
"v_mfma_f32_32x32x16_bf16 v[240:255], a[184:187], a[120:123], v[240:255]\n"
"s_waitcnt lgkmcnt(4)\n"
"ds_read_b64_tr_b16 a[172:173], v21 offset:53248\n"
"ds_read_b64_tr_b16 a[174:175], v21 offset:53760\n"
"v_rcp_f32 v83, v83\n"
"v_mul_f32 v216, v216, v82\n"
"v_mul_f32 v217, v217, v83\n"
"v_mfma_f32_32x32x16_bf16 v[240:255], a[188:191], a[124:127], v[240:255]\n"
"ds_read_b64_tr_b16 a[176:177], v25 offset:0\n"
"ds_read_b64_tr_b16 a[178:179], v25 offset:512\n"
"v_cvt_pk_bf16_f32 v196, v216, v217\n"
"v_mul_f32 v84, v210, v210\n"
"v_mul_f32 v85, v211, v211\n"
"v_fma_f32 v84, v51, v84, v50\n"
"v_mfma_f32_32x32x16_bf16 v[240:255], a[160:163], a[128:131], v[240:255]\n"
"s_waitcnt lgkmcnt(4)\n"
"ds_read_b64_tr_b16 a[180:181], v25 offset:4096\n"
"ds_read_b64_tr_b16 a[182:183], v25 offset:4608\n"
"v_fma_f32 v85, v51, v85, v50\n"
"v_mul_f32 v84, v210, v84\n"
"v_mul_f32 v85, v211, v85\n"
"v_mfma_f32_32x32x16_bf16 v[240:255], a[164:167], a[132:135], v[240:255]\n"
"ds_read_b64_tr_b16 a[184:185], v25 offset:8192\n"
"ds_read_b64_tr_b16 a[186:187], v25 offset:8704\n"
"v_exp_f32 v84, v84\n"
"v_mul_f32 v218, v210, v218\n"
"v_mfma_f32_32x32x16_bf16 v[240:255], a[168:171], a[136:139], v[240:255]\n"
"s_waitcnt lgkmcnt(4)\n"
"v_exp_f32 v85, v85\n"
"v_mul_f32 v219, v211, v219\n"
"v_fma_f32 v84, v84, v52, v52\n"
"v_fma_f32 v85, v85, v52, v52\n"
"v_mfma_f32_32x32x16_bf16 v[240:255], a[172:175], a[140:143], v[240:255]\n"
"s_waitcnt vmcnt(7)\n"
"s_barrier\n"
"ds_read_b64_tr_b16 a[188:189], v25 offset:12288\n"
"ds_read_b64_tr_b16 a[190:191], v25 offset:12800\n"
"s_lshl_b32 s66, s6, 5\n"
"s_and_b32 s66, s66, 0xffffff80\n"
"s_add_u32 m0, s59, 0\n"
"v_mfma_f32_32x32x16_bf16 v[240:255], a[176:179], a[144:147], v[240:255]\n"
"s_waitcnt lgkmcnt(2)\n"
"ds_read_b128 a[160:163], v14 offset:40960\n"
"buffer_load_dwordx4 v30, s[32:35], s66 offen offset:0 lds\n"
"v_rcp_f32 v84, v84\n"
"v_mfma_f32_32x32x16_bf16 v[240:255], a[180:183], a[148:151], v[240:255]\n"
"ds_read_b128 a[164:167], v14 offset:45056\n"
"v_rcp_f32 v85, v85\n"
"v_mul_f32 v218, v218, v84\n"
"v_mul_f32 v219, v219, v85\n"
"v_cvt_pk_bf16_f32 v197, v218, v219\n"
"v_mfma_f32_32x32x16_bf16 v[240:255], a[184:187], a[152:155], v[240:255]\n"
"s_waitcnt lgkmcnt(2)\n"
"ds_read_b128 a[168:171], v14 offset:49152\n"
"buffer_load_dwordx4 v31, s[32:35], s66 offen offset:1024 lds\n"
"v_mul_f32 v82, v212, v212\n"
"v_mul_f32 v83, v213, v213\n"
"v_fma_f32 v82, v51, v82, v50\n"
"v_mfma_f32_32x32x16_bf16 v[240:255], a[236:239], v[54:57], v[240:255]\n"
"ds_read_b128 a[172:175], v14 offset:53248\n"
"v_fma_f32 v83, v51, v83, v50\n"
"v_mul_f32 v82, v212, v82\n"
"v_mul_f32 v83, v213, v83\n"
"v_exp_f32 v82, v82\n"
"v_mfma_f32_32x32x16_bf16 v[240:255], a[188:191], a[156:159], v[240:255]\n"
"s_waitcnt lgkmcnt(2)\n"
"ds_read_b128 a[176:179], v14 offset:57344\n"
"buffer_load_dwordx4 v32, s[32:35], s66 offen offset:2048 lds\n"
"v_mul_f32 v220, v212, v220\n"
"v_exp_f32 v83, v83\n"
"v_mfma_f32_32x32x16_bf16 v[112:127], a[192:195], a[160:163], 0\n"
"ds_read_b128 a[180:183], v15 offset:40960\n"
"v_mul_f32 v221, v213, v221\n"
"v_fma_f32 v82, v82, v52, v52\n"
"v_fma_f32 v83, v83, v52, v52\n"
"v_rcp_f32 v82, v82\n"
"v_rcp_f32 v83, v83\n"
"v_mul_f32 v220, v220, v82\n"
"v_mul_f32 v221, v221, v83\n"
"v_cvt_pk_bf16_f32 v198, v220, v221\n"
"v_mul_f32 v84, v214, v214\n"
"v_mfma_f32_32x32x16_bf16 v[128:143], a[192:195], a[164:167], 0\n"
"s_waitcnt lgkmcnt(2)\n"
"ds_read_b128 a[184:187], v15 offset:45056\n"
"buffer_load_dwordx4 v33, s[32:35], s66 offen offset:3072 lds\n"
"s_add_u32 m0, s59, 4096\n"
"v_mul_f32 v85, v215, v215\n"
"v_fma_f32 v84, v51, v84, v50\n"
"v_fma_f32 v85, v51, v85, v50\n"
"v_mul_f32 v84, v214, v84\n"
"v_mul_f32 v85, v215, v85\n"
"v_exp_f32 v84, v84\n"
"v_mul_f32 v222, v214, v222\n"
"v_mfma_f32_32x32x16_bf16 v[144:159], a[192:195], a[168:171], 0\n"
"ds_read_b128 a[188:191], v15 offset:49152\n"
"buffer_load_dwordx4 v38, s[32:35], s66 offen lds\n"
"s_lshl_b32 s66, s6, 5\n"
"s_and_b32 s66, s66, 0xffffff80\n"
"s_add_u32 m0, s59, 20480\n"
"v_exp_f32 v85, v85\n"
"v_mul_f32 v223, v215, v223\n"
"v_fma_f32 v84, v84, v52, v52\n"
"v_fma_f32 v85, v85, v52, v52\n"
"v_mfma_f32_32x32x16_bf16 v[160:175], a[192:195], a[172:175], 0\n"
"s_waitcnt lgkmcnt(2)\n"
"ds_read_b128 a[160:163], v15 offset:53248\n"
"buffer_load_dwordx4 v30, s[36:39], s66 offen offset:0 lds\n"
"v_rcp_f32 v84, v84\n"
"v_rcp_f32 v85, v85\n"
"v_mul_f32 v222, v222, v84\n"
"v_mul_f32 v223, v223, v85\n"
"v_cvt_pk_bf16_f32 v199, v222, v223\n"
"v_mul_f32 v82, v224, v224\n"
"v_mul_f32 v83, v225, v225\n"
"v_mfma_f32_32x32x16_bf16 v[176:191], a[192:195], a[176:179], 0\n"
"ds_read_b128 a[164:167], v15 offset:57344\n"
"buffer_store_dwordx4 v[196:199], v44, s[40:43], s67 offen offset:1024 sc1\n"
"v_fma_f32 v82, v51, v82, v50\n"
"v_fma_f32 v83, v51, v83, v50\n"
"v_mul_f32 v82, v224, v82\n"
"v_mul_f32 v83, v225, v83\n"
"v_exp_f32 v82, v82\n"
"v_mul_f32 v232, v224, v232\n"
"v_mfma_f32_32x32x16_bf16 v[112:127], a[200:203], a[180:183], v[112:127]\n"
"s_waitcnt lgkmcnt(2)\n"
"ds_read_b128 a[168:171], v16 offset:40960\n"
"v_exp_f32 v83, v83\n"
"v_mul_f32 v233, v225, v233\n"
"v_fma_f32 v82, v82, v52, v52\n"
"v_fma_f32 v83, v83, v52, v52\n"
"v_rcp_f32 v82, v82\n"
"v_rcp_f32 v83, v83\n"
"v_mul_f32 v232, v232, v82\n"
"v_mul_f32 v233, v233, v83\n"
"v_mfma_f32_32x32x16_bf16 v[128:143], a[200:203], a[184:187], v[128:143]\n"
"ds_read_b128 a[172:175], v16 offset:45056\n"
"buffer_load_dwordx4 v31, s[36:39], s66 offen offset:1024 lds\n"
"v_cvt_pk_bf16_f32 v200, v232, v233\n"
"v_mul_f32 v84, v226, v226\n"
"v_mul_f32 v85, v227, v227\n"
"v_fma_f32 v84, v51, v84, v50\n"
"v_fma_f32 v85, v51, v85, v50\n"
"v_mul_f32 v84, v226, v84\n"
"v_mul_f32 v85, v227, v85\n"
"v_exp_f32 v84, v84\n"
"v_mfma_f32_32x32x16_bf16 v[144:159], a[200:203], a[188:191], v[144:159]\n"
"s_waitcnt lgkmcnt(2)\n"
"ds_read_b128 a[176:179], v16 offset:49152\n"
"buffer_load_dwordx4 v32, s[36:39], s66 offen offset:2048 lds\n"
"v_mul_f32 v234, v226, v234\n"
"v_exp_f32 v85, v85\n"
"v_mul_f32 v235, v227, v235\n"
"v_fma_f32 v84, v84, v52, v52\n"
"v_fma_f32 v85, v85, v52, v52\n"
"v_rcp_f32 v84, v84\n"
"v_mfma_f32_32x32x16_bf16 v[160:175], a[200:203], a[160:163], v[160:175]\n"
"ds_read_b128 a[180:183], v16 offset:53248\n"
"v_rcp_f32 v85, v85\n"
"v_mul_f32 v234, v234, v84\n"
"v_mul_f32 v235, v235, v85\n"
"v_cvt_pk_bf16_f32 v201, v234, v235\n"
"v_mul_f32 v82, v228, v228\n"
"v_mul_f32 v83, v229, v229\n"
"v_fma_f32 v82, v51, v82, v50\n"
"v_fma_f32 v83, v51, v83, v50\n"
"v_mul_f32 v82, v228, v82\n"
"v_mul_f32 v83, v229, v83\n"
"v_mfma_f32_32x32x16_bf16 v[176:191], a[200:203], a[164:167], v[176:191]\n"
"s_waitcnt lgkmcnt(2)\n"
"ds_read_b128 a[184:187], v16 offset:57344\n"
"buffer_load_dwordx4 v33, s[36:39], s66 offen offset:3072 lds\n"
"s_add_u32 m0, s59, 24576\n"
"v_exp_f32 v82, v82\n"
"v_mul_f32 v236, v228, v236\n"
"v_exp_f32 v83, v83\n"
"v_mul_f32 v237, v229, v237\n"
"v_fma_f32 v82, v82, v52, v52\n"
"v_fma_f32 v83, v83, v52, v52\n"
"v_mfma_f32_32x32x16_bf16 v[112:127], a[208:211], a[168:171], v[112:127]\n"
"ds_read_b128 a[188:191], v17 offset:40960\n"
"s_lshl_b32 s7, 1, s55\n"
"s_andn2_b32 s7, 15, s7\n"
"buffer_load_dwordx4 v38, s[36:39], s66 offen lds\n"
"v_mov_b32 v3, 0\n"
"v_rcp_f32 v82, v82\n"
"v_rcp_f32 v83, v83\n"
"v_mul_f32 v236, v236, v82\n"
"v_mul_f32 v237, v237, v83\n"
"v_mfma_f32_32x32x16_bf16 v[128:143], a[208:211], a[172:175], v[128:143]\n"
"s_waitcnt lgkmcnt(2)\n"
"ds_read_b128 a[160:163], v17 offset:45056\n"
"buffer_atomic_add v3, v1, s[40:43], s4 offen offset:128 sc0 sc1\n"
"v_cvt_pk_bf16_f32 v202, v236, v237\n"
"v_mul_f32 v84, v230, v230\n"
"v_mul_f32 v85, v231, v231\n"
"v_fma_f32 v84, v51, v84, v50\n"
"v_fma_f32 v85, v51, v85, v50\n"
"v_mul_f32 v84, v230, v84\n"
"v_mul_f32 v85, v231, v85\n"
"v_exp_f32 v84, v84\n"
"v_mfma_f32_32x32x16_bf16 v[144:159], a[208:211], a[176:179], v[144:159]\n"
"ds_read_b128 a[164:167], v17 offset:49152\n"
"v_mul_f32 v238, v230, v238\n"
"v_exp_f32 v85, v85\n"
"v_mul_f32 v239, v231, v239\n"
"v_fma_f32 v84, v84, v52, v52\n"
"v_fma_f32 v85, v85, v52, v52\n"
"v_rcp_f32 v84, v84\n"
"v_rcp_f32 v85, v85\n"
"v_mul_f32 v238, v238, v84\n"
"v_mfma_f32_32x32x16_bf16 v[160:175], a[208:211], a[180:183], v[160:175]\n"
"s_waitcnt lgkmcnt(2)\n"
"ds_read_b128 a[168:171], v17 offset:53248\n"
"v_mul_f32 v239, v239, v85\n"
"v_cvt_pk_bf16_f32 v203, v238, v239\n"
"v_mul_f32 v82, v240, v240\n"
"v_mul_f32 v83, v241, v241\n"
"v_fma_f32 v82, v51, v82, v50\n"
"v_fma_f32 v83, v51, v83, v50\n"
"v_mul_f32 v82, v240, v82\n"
"v_mul_f32 v83, v241, v83\n"
"v_exp_f32 v82, v82\n"
"v_mul_f32 v248, v240, v248\n"
"v_mfma_f32_32x32x16_bf16 v[176:191], a[208:211], a[184:187], v[176:191]\n"
"s_waitcnt vmcnt(14)\n"
"s_barrier\n"
"ds_read_b128 a[172:175], v17 offset:57344\n"
"buffer_store_dwordx4 v[200:203], v44, s[40:43], s67 offen offset:2048 sc1\n"
"s_xor_b32 s66, s55, 1\n"
"v_exp_f32 v83, v83\n"
"v_mul_f32 v249, v241, v249\n"
"v_fma_f32 v82, v82, v52, v52\n"
"v_fma_f32 v83, v83, v52, v52\n"
"v_mfma_f32_32x32x16_bf16 v[112:127], a[216:219], a[188:191], v[112:127]\n"
"s_waitcnt lgkmcnt(2)\n"
"ds_read_b128 a[176:179], v14 offset:20480\n"
"s_lshl_b32 s66, s66, 2\n"
"s_add_u32 s66, s66, s5\n"
"s_lshl_b32 s66, s66, 5\n"
"s_add_u32 m0, s59, 40960\n"
"v_rcp_f32 v82, v82\n"
"v_rcp_f32 v83, v83\n"
"v_mul_f32 v248, v248, v82\n"
"v_mul_f32 v249, v249, v83\n"
"v_cvt_pk_bf16_f32 v204, v248, v249\n"
"v_mfma_f32_32x32x16_bf16 v[128:143], a[216:219], a[160:163], v[128:143]\n"
"ds_read_b128 a[180:183], v14 offset:24576\n"
"buffer_load_dwordx4 v30, s[32:35], s66 offen offset:0 lds\n"
"v_mul_f32 v84, v242, v242\n"
"v_mul_f32 v85, v243, v243\n"
"v_fma_f32 v84, v51, v84, v50\n"
"v_fma_f32 v85, v51, v85, v50\n"
"v_mul_f32 v84, v242, v84\n"
"v_mul_f32 v85, v243, v85\n"
"v_exp_f32 v84, v84\n"
"v_mul_f32 v250, v242, v250\n"
"v_mfma_f32_32x32x16_bf16 v[144:159], a[216:219], a[164:167], v[144:159]\n"
"s_waitcnt lgkmcnt(2)\n"
"ds_read_b128 a[184:187], v14 offset:28672\n"
"buffer_load_dwordx4 v31, s[32:35], s66 offen offset:1024 lds\n"
"v_exp_f32 v85, v85\n"
"v_mul_f32 v251, v243, v251\n"
"v_fma_f32 v84, v84, v52, v52\n"
"v_fma_f32 v85, v85, v52, v52\n"
"v_rcp_f32 v84, v84\n"
"v_rcp_f32 v85, v85\n"
"v_mfma_f32_32x32x16_bf16 v[160:175], a[216:219], a[168:171], v[160:175]\n"
"ds_read_b128 a[188:191], v14 offset:32768\n"
"buffer_load_dwordx4 v32, s[32:35], s66 offen offset:2048 lds\n"
"v_mul_f32 v250, v250, v84\n"
"v_mul_f32 v251, v251, v85\n"
"v_cvt_pk_bf16_f32 v205, v250, v251\n"
"v_mul_f32 v82, v244, v244\n"
"v_mul_f32 v83, v245, v245\n"
"v_fma_f32 v82, v51, v82, v50\n"
"v_fma_f32 v83, v51, v83, v50\n"
"v_mul_f32 v82, v244, v82\n"
"v_mul_f32 v83, v245, v83\n"
"v_mfma_f32_32x32x16_bf16 v[176:191], a[216:219], a[172:175], v[176:191]\n"
"s_waitcnt lgkmcnt(2)\n"
"ds_read_b128 a[160:163], v14 offset:36864\n"
"buffer_load_dwordx4 v33, s[32:35], s66 offen offset:3072 lds\n"
"s_add_u32 m0, s59, 45056\n"
"v_exp_f32 v82, v82\n"
"v_mul_f32 v252, v244, v252\n"
"v_exp_f32 v83, v83\n"
"v_mul_f32 v253, v245, v253\n"
"v_fma_f32 v82, v82, v52, v52\n"
"v_fma_f32 v83, v83, v52, v52\n"
"v_mfma_f32_32x32x16_bf16 v[112:127], a[196:199], a[176:179], v[112:127]\n"
"ds_read_b128 a[164:167], v15 offset:20480\n"
"buffer_load_dwordx4 v38, s[32:35], s66 offen lds\n"
"s_xor_b32 s66, s55, 1\n"
"s_lshl_b32 s66, s66, 2\n"
"s_add_u32 s66, s66, s5\n"
"s_lshl_b32 s66, s66, 5\n"
"v_rcp_f32 v82, v82\n"
"v_rcp_f32 v83, v83\n"
"v_mul_f32 v252, v252, v82\n"
"v_mfma_f32_32x32x16_bf16 v[128:143], a[196:199], a[180:183], v[128:143]\n"
"s_waitcnt lgkmcnt(2)\n"
"ds_read_b128 a[168:171], v15 offset:24576\n"
"s_add_u32 m0, s59, 61440\n"
"s_nop 0\n"
"buffer_load_dwordx4 v30, s[36:39], s66 offen offset:0 lds\n"
"s_waitcnt vmcnt(7)\n"
"v_cmp_ne_u32 s[0:1], s56, v3\n"
"s_nop 0\n"
"s_and_b32 s0, s0, s7\n"
"s_cbranch_scc1 Lh1B_slow%=\n"
"Lh1B_ok%=:\n"
"v_mul_f32 v253, v253, v83\n"
"v_cvt_pk_bf16_f32 v206, v252, v253\n"
"v_mul_f32 v84, v246, v246\n"
"v_mul_f32 v85, v247, v247\n"
"v_fma_f32 v84, v51, v84, v50\n"
"v_mfma_f32_32x32x16_bf16 v[144:159], a[196:199], a[184:187], v[144:159]\n"
"ds_read_b128 a[172:175], v15 offset:28672\n"
"s_mov_b32 s2, s6\n"
"s_lshl_b32 s2, s2, 11\n"
"s_add_u32 s2, s2, s65\n"
"s_add_u32 s3, s2, 4096\n"
"buffer_load_dwordx4 a[0:3], v44, s[40:43], s2 offen offset:0 sc1\n"
"v_fma_f32 v85, v51, v85, v50\n"
"v_mul_f32 v84, v246, v84\n"
"v_mul_f32 v85, v247, v85\n"
"v_exp_f32 v84, v84\n"
"v_mfma_f32_32x32x16_bf16 v[160:175], a[196:199], a[188:191], v[160:175]\n"
"s_waitcnt lgkmcnt(2)\n"
"ds_read_b128 a[176:179], v15 offset:32768\n"
"buffer_load_dwordx4 a[4:7], v44, s[40:43], s2 offen offset:2048 sc1\n"
"s_mov_b32 s2, s6\n"
"s_lshl_b32 s2, s2, 11\n"
"s_add_u32 s2, s2, s65\n"
"s_add_u32 s3, s2, 4096\n"
"v_mul_f32 v254, v246, v254\n"
"v_exp_f32 v85, v85\n"
"v_mul_f32 v255, v247, v255\n"
"v_fma_f32 v84, v84, v52, v52\n"
"v_mfma_f32_32x32x16_bf16 v[176:191], a[196:199], a[160:163], v[176:191]\n"
"ds_read_b128 a[180:183], v15 offset:36864\n"
"buffer_load_dwordx4 a[8:11], v44, s[40:43], s2 offen offset:1024 sc1\n"
"v_fma_f32 v85, v85, v52, v52\n"
"v_rcp_f32 v84, v84\n"
"v_rcp_f32 v85, v85\n"
"v_mul_f32 v254, v254, v84\n"
"v_mul_f32 v255, v255, v85\n"
"v_cvt_pk_bf16_f32 v207, v254, v255\n"
"v_mfma_f32_32x32x16_bf16 v[112:127], a[204:207], a[164:167], v[112:127]\n"
"s_waitcnt lgkmcnt(2)\n"
"ds_read_b128 a[184:187], v16 offset:20480\n"
"v_mfma_f32_32x32x16_bf16 v[128:143], a[204:207], a[168:171], v[128:143]\n"
"ds_read_b128 a[188:191], v16 offset:24576\n"
"buffer_store_dwordx4 v[204:207], v44, s[40:43], s67 offen offset:3072 sc1\n"
"v_mfma_f32_32x32x16_bf16 v[144:159], a[204:207], a[172:175], v[144:159]\n"
"s_waitcnt lgkmcnt(2)\n"
"ds_read_b128 a[160:163], v16 offset:28672\n"
"v_mfma_f32_32x32x16_bf16 v[160:175], a[204:207], a[176:179], v[160:175]\n"
"ds_read_b128 a[164:167], v16 offset:32768\n"
"v_mfma_f32_32x32x16_bf16 v[176:191], a[204:207], a[180:183], v[176:191]\n"
"s_waitcnt lgkmcnt(2)\n"
"ds_read_b128 a[168:171], v16 offset:36864\n"
"buffer_load_dwordx4 a[12:15], v44, s[40:43], s2 offen offset:3072 sc1\n"
"s_xor_b32 s2, s55, 1\n"
"s_lshl_b32 s2, s2, 2\n"
"s_add_u32 s2, s2, s5\n"
"s_lshl_b32 s2, s2, 11\n"
"v_mfma_f32_32x32x16_bf16 v[112:127], a[212:215], a[184:187], v[112:127]\n"
"ds_read_b128 a[172:175], v17 offset:20480\n"
"s_add_u32 s2, s2, s65\n"
"s_add_u32 s3, s2, 4096\n"
"v_mfma_f32_32x32x16_bf16 v[128:143], a[212:215], a[188:191], v[128:143]\n"
"s_waitcnt lgkmcnt(2)\n"
"ds_read_b128 a[176:179], v17 offset:24576\n"
"buffer_load_dwordx4 a[16:19], v44, s[40:43], s2 offen offset:0 sc1\n"
"v_mfma_f32_32x32x16_bf16 v[144:159], a[212:215], a[160:163], v[144:159]\n"
"ds_read_b128 a[180:183], v17 offset:28672\n"
"v_mfma_f32_32x32x16_bf16 v[160:175], a[212:215], a[164:167], v[160:175]\n"
"s_waitcnt lgkmcnt(2)\n"
"ds_read_b128 a[184:187], v17 offset:32768\n"
"buffer_load_dwordx4 a[20:23], v44, s[40:43], s2 offen offset:2048 sc1\n"
"v_mfma_f32_32x32x16_bf16 v[176:191], a[212:215], a[168:171], v[176:191]\n"
"s_barrier\n"
"ds_read_b128 a[188:191], v17 offset:36864\n"
"v_mfma_f32_32x32x16_bf16 v[112:127], a[220:223], a[172:175], v[112:127]\n"
"s_waitcnt lgkmcnt(2)\n"
"ds_read_b64_tr_b16 a[160:161], v80 offset:0\n"
"ds_read_b64_tr_b16 a[162:163], v80 offset:1280\n"
"buffer_load_dwordx4 a[24:27], v44, s[40:43], s3 offen offset:0 sc1\n"
"v_mfma_f32_32x32x16_bf16 v[128:143], a[220:223], a[176:179], v[128:143]\n"
"ds_read_b64_tr_b16 a[164:165], v80 offset:64\n"
"ds_read_b64_tr_b16 a[166:167], v80 offset:1344\n"
"v_mfma_f32_32x32x16_bf16 v[144:159], a[220:223], a[180:183], v[144:159]\n"
"s_waitcnt lgkmcnt(4)\n"
"ds_read_b64_tr_b16 a[168:169], v80 offset:128\n"
"ds_read_b64_tr_b16 a[170:171], v80 offset:1408\n"
"buffer_load_dwordx4 a[28:31], v44, s[40:43], s3 offen offset:2048 sc1\n"
"s_xor_b32 s2, s55, 1\n"
"s_lshl_b32 s2, s2, 2\n"
"s_add_u32 s2, s2, s5\n"
"v_mfma_f32_32x32x16_bf16 v[160:175], a[220:223], a[184:187], v[160:175]\n"
"ds_read_b64_tr_b16 a[172:173], v80 offset:192\n"
"ds_read_b64_tr_b16 a[174:175], v80 offset:1472\n"
"s_lshl_b32 s2, s2, 11\n"
"s_add_u32 s2, s2, s65\n"
"s_add_u32 s3, s2, 4096\n"
"buffer_load_dwordx4 a[32:35], v44, s[40:43], s2 offen offset:1024 sc1\n"
"v_mfma_f32_32x32x16_bf16 v[176:191], a[220:223], a[188:191], v[176:191]\n"
"s_waitcnt lgkmcnt(4)\n"
"ds_read_b64_tr_b16 a[176:177], v80 offset:256\n"
"ds_read_b64_tr_b16 a[178:179], v80 offset:1536\n"
"v_mfma_f32_32x32x16_bf16 v[112:127], v[192:195], a[160:163], v[112:127]\n"
"ds_read_b64_tr_b16 a[180:181], v80 offset:5120\n"
"ds_read_b64_tr_b16 a[182:183], v80 offset:6400\n"
"buffer_load_dwordx4 a[36:39], v44, s[40:43], s2 offen offset:3072 sc1\n"
"s_lshl_b32 s60, s53, 4\n"
"s_add_u32 s60, s60, s55\n"
"v_mfma_f32_32x32x16_bf16 v[128:143], v[192:195], a[164:167], v[128:143]\n"
"s_waitcnt lgkmcnt(4)\n"
"ds_read_b64_tr_b16 a[184:185], v80 offset:5184\n"
"ds_read_b64_tr_b16 a[186:187], v80 offset:6464\n"
"s_mul_i32 s60, s60, 40960\n"
"s_add_u32 s60, s60, 0xc00000\n"
"s_mul_i32 s61, s54, 10240\n"
"v_mfma_f32_32x32x16_bf16 v[144:159], v[192:195], a[168:171], v[144:159]\n"
"ds_read_b64_tr_b16 a[188:189], v80 offset:5248\n"
"ds_read_b64_tr_b16 a[190:191], v80 offset:6528\n"
"buffer_load_dwordx4 a[40:43], v44, s[40:43], s3 offen offset:1024 sc1\n"
"s_add_u32 s61, s61, s60\n"
"s_lshl_b32 s67, s52, 11\n"
"v_mfma_f32_32x32x16_bf16 v[160:175], v[192:195], a[172:175], v[160:175]\n"
"s_waitcnt lgkmcnt(4)\n"
"ds_read_b64_tr_b16 a[160:161], v80 offset:5312\n"
"ds_read_b64_tr_b16 a[162:163], v80 offset:6592\n"
"s_mul_i32 s62, s54, 163840\n"
"s_add_u32 s60, s60, s62\n"
"s_add_u32 s62, s54, 1\n"
"s_and_b32 s62, s62, 3\n"
"v_mfma_f32_32x32x16_bf16 v[176:191], v[192:195], a[176:179], v[176:191]\n"
"ds_read_b64_tr_b16 a[164:165], v80 offset:5376\n"
"ds_read_b64_tr_b16 a[166:167], v80 offset:6656\n"
"buffer_load_dwordx4 a[44:47], v44, s[40:43], s3 offen offset:3072 sc1\n"
"s_mul_i32 s12, s62, 163840\n"
"s_add_u32 s12, s12, s61\n"
"v_mfma_f32_32x32x16_bf16 v[112:127], v[196:199], a[180:183], v[112:127]\n"
"s_waitcnt lgkmcnt(4)\n"
"ds_read_b64_tr_b16 a[168:169], v80 offset:10240\n"
"ds_read_b64_tr_b16 a[170:171], v80 offset:11520\n"
"s_add_u32 s12, s12, s67\n"
"s_mul_i32 s62, s62, 10240\n"
"s_add_u32 s62, s62, s60\n"
"s_add_u32 s16, s62, s67\n"
"v_mfma_f32_32x32x16_bf16 v[128:143], v[196:199], a[184:187], v[128:143]\n"
"ds_read_b64_tr_b16 a[172:173], v80 offset:10304\n"
"ds_read_b64_tr_b16 a[174:175], v80 offset:11584\n"
"buffer_load_dwordx4 v31, s[36:39], s66 offen offset:1024 lds\n"
"s_add_u32 s62, s62, 8192\n"
"s_cmp_eq_u32 s52, s54\n"
"s_cselect_b32 s19, s62, 0x70000000\n"
"v_mfma_f32_32x32x16_bf16 v[144:159], v[196:199], a[188:191], v[144:159]\n"
"s_waitcnt lgkmcnt(4)\n"
"ds_read_b64_tr_b16 a[176:177], v80 offset:10368\n"
"ds_read_b64_tr_b16 a[178:179], v80 offset:11648\n"
"s_add_u32 s62, s54, 2\n"
"s_and_b32 s62, s62, 3\n"
"s_mul_i32 s13, s62, 163840\n"
"s_add_u32 s13, s13, s61\n"
"v_mfma_f32_32x32x16_bf16 v[160:175], v[196:199], a[160:163], v[160:175]\n"
"ds_read_b64_tr_b16 a[180:181], v80 offset:10432\n"
"ds_read_b64_tr_b16 a[182:183], v80 offset:11712\n"
"buffer_load_dwordx4 v32, s[36:39], s66 offen offset:2048 lds\n"
"s_add_u32 s13, s13, s67\n"
"s_mul_i32 s62, s62, 10240\n"
"v_mfma_f32_32x32x16_bf16 v[176:191], v[196:199], a[164:167], v[176:191]\n"
"s_waitcnt lgkmcnt(4)\n"
"ds_read_b64_tr_b16 a[184:185], v80 offset:10496\n"
"ds_read_b64_tr_b16 a[186:187], v80 offset:11776\n"
"s_add_u32 s62, s62, s60\n"
"s_add_u32 s17, s62, s67\n"
"s_add_u32 s62, s62, 8192\n"
"s_cmp_eq_u32 s52, s54\n"
"s_cselect_b32 s20, s62, 0x70000000\n"
"v_mfma_f32_32x32x16_bf16 v[112:127], v[200:203], a[168:171], v[112:127]\n"
"ds_read_b64_tr_b16 a[188:189], v80 offset:15360\n"
"ds_read_b64_tr_b16 a[190:191], v80 offset:16640\n"
"buffer_load_dwordx4 v33, s[36:39], s66 offen offset:3072 lds\n"
"s_add_u32 m0, s59, 65536\n"
"s_add_u32 s62, s54, 3\n"
"v_mfma_f32_32x32x16_bf16 v[128:143], v[200:203], a[172:175], v[128:143]\n"
"s_waitcnt lgkmcnt(4)\n"
"ds_read_b64_tr_b16 a[160:161], v80 offset:15424\n"
"ds_read_b64_tr_b16 a[162:163], v80 offset:16704\n"
"s_and_b32 s62, s62, 3\n"
"s_mul_i32 s14, s62, 163840\n"
"s_add_u32 s14, s14, s61\n"
"s_add_u32 s14, s14, s67\n"
"v_mfma_f32_32x32x16_bf16 v[144:159], v[200:203], a[176:179], v[144:159]\n"
"ds_read_b64_tr_b16 a[164:165], v80 offset:15488\n"
"ds_read_b64_tr_b16 a[166:167], v80 offset:16768\n"
"buffer_load_dwordx4 v38, s[36:39], s66 offen lds\n"
"s_mul_i32 s66, s54, 6\n"
"s_add_u32 s66, s66, 0\n"
"s_mul_i32 s66, s66, 81920\n"
"v_mfma_f32_32x32x16_bf16 v[160:175], v[200:203], a[180:183], v[160:175]\n"
"s_waitcnt lgkmcnt(4)\n"
"ds_read_b64_tr_b16 a[168:169], v80 offset:15552\n"
"ds_read_b64_tr_b16 a[170:171], v80 offset:16832\n"
"s_add_u32 m0, s59, 102400\n"
"s_mul_i32 s62, s62, 10240\n"
"s_add_u32 s62, s62, s60\n"
"s_add_u32 s18, s62, s67\n"
"v_mfma_f32_32x32x16_bf16 v[176:191], v[200:203], a[184:187], v[176:191]\n"
"s_barrier\n"
"ds_read_b64_tr_b16 a[172:173], v80 offset:15616\n"
"ds_read_b64_tr_b16 a[174:175], v80 offset:16896\n"
"buffer_load_dwordx4 v39, s[28:31], s66 offen offset:0 lds\n"
"v_mfma_f32_32x32x16_bf16 v[112:127], v[204:207], a[188:191], v[112:127]\n"
"s_waitcnt lgkmcnt(4)\n"
"ds_read_b128 a[176:179], v28 offset:0\n"
"s_add_u32 s62, s62, 8192\n"
"s_cmp_eq_u32 s52, s54\n"
"s_cselect_b32 s21, s62, 0x70000000\n"
"s_mul_i32 s62, s52, 163840\n"
"s_add_u32 s62, s62, s61\n"
"v_mfma_f32_32x32x16_bf16 v[128:143], v[204:207], a[160:163], v[128:143]\n"
"ds_read_b128 a[180:183], v28 offset:4096\n"
"buffer_load_dwordx4 v40, s[28:31], s66 offen offset:1024 lds\n"
"s_add_u32 s62, s62, 8192\n"
"s_cmp_eq_u32 s52, s54\n"
"s_cselect_b32 s15, 0x70000000, s62\n"
"v_mfma_f32_32x32x16_bf16 v[144:159], v[204:207], a[164:167], v[144:159]\n"
"s_waitcnt lgkmcnt(2)\n"
"ds_read_b128 a[184:187], v28 offset:8192\n"
"s_lshl_b32 s24, s53, 2\n"
"s_add_u32 s24, s24, s55\n"
"s_lshl_b32 s24, s24, 2\n"
"s_add_u32 s24, s24, s52\n"
"s_lshl_b32 s24, s24, 7\n"
"v_mfma_f32_32x32x16_bf16 v[160:175], v[204:207], a[168:171], v[160:175]\n"
"ds_read_b128 a[188:191], v28 offset:12288\n"
"buffer_load_dwordx4 v41, s[28:31], s66 offen offset:2048 lds\n"
"s_add_u32 s24, s24, 0x1798000\n"
"s_mov_b32 s26, 0x3f80\n"
"s_mov_b32 s27, 0x3f800000\n"
"v_mfma_f32_32x32x16_bf16 v[176:191], v[204:207], a[172:175], v[176:191]\n"
"s_waitcnt vmcnt(19) lgkmcnt(2)\n"
"ds_read_b128 a[160:163], v28 offset:16384\n"
"v_and_b32 v92, 31, v2\n"
"v_lshrrev_b32 v93, 5, v2\n"
"v_lshlrev_b32 v96, 1, v92\n"
"s_lshl_b32 s60, s53, 2\n"
"s_add_u32 s60, s60, s52\n"
"v_mfma_f32_32x32x16_bf16 v[112:127], a[0:3], a[176:179], v[112:127]\n"
"ds_read_b128 a[164:167], v29 offset:0\n"
"s_mul_i32 s60, s60, 40960\n"
"s_mul_i32 s61, s55, 320\n"
"s_lshl_b32 s62, s54, 6\n"
"s_add_u32 s62, s62, s61\n"
"s_add_u32 s61, s61, 256\n"
"buffer_load_dwordx4 v42, s[28:31], s66 offen offset:3072 lds\n"
"v_mfma_f32_32x32x16_bf16 v[128:143], a[0:3], a[180:183], v[128:143]\n"
"s_waitcnt lgkmcnt(2)\n"
"ds_read_b128 a[168:171], v29 offset:4096\n"
"s_add_u32 s22, s60, s62\n"
"s_add_u32 s23, s60, s61\n"
"s_cmp_eq_u32 s52, s54\n"
"s_cselect_b32 s61, s61, 0x70000000\n"
"s_add_u32 m0, s59, 106496\n"
"v_mfma_f32_32x32x16_bf16 v[144:159], a[0:3], a[184:187], v[144:159]\n"
"ds_read_b128 a[172:175], v29 offset:8192\n"
"buffer_load_ushort v90, v96, s[48:51], s62 offen\n"
"buffer_load_ushort v91, v96, s[48:51], s61 offen\n"
"s_lshl_b32 s22, s22, 1\n"
"v_mfma_f32_32x32x16_bf16 v[160:175], a[0:3], a[188:191], v[160:175]\n"
"s_waitcnt vmcnt(21) lgkmcnt(2)\n"
"ds_read_b128 a[176:179], v29 offset:12288\n"
"s_lshl_b32 s23, s23, 1\n"
"v_mul_u32_u24 v95, 20480, v93\n"
"v_lshl_add_u32 v95, v92, 2, v95\n"
"v_lshlrev_b32 v93, 3, v93\n"
"v_sub_u32 v93, v92, v93\n"
"v_mfma_f32_32x32x16_bf16 v[176:191], a[0:3], a[160:163], v[176:191]\n"
"s_barrier\n"
"ds_read_b128 a[180:183], v29 offset:16384\n"
"buffer_load_dwordx4 v43, s[28:31], s66 offen lds\n"
"s_mul_i32 s66, s54, 6\n"
"s_add_u32 s66, s66, 2\n"
"v_mfma_f32_32x32x16_bf16 v[112:127], a[4:7], a[164:167], v[112:127]\n"
"s_waitcnt lgkmcnt(2)\n"
"ds_read_b128 a[184:187], v28 offset:20480\n"
"s_mul_i32 s66, s66, 81920\n"
"s_add_u32 m0, s59, 81920\n"
"v_ashrrev_i32 v93, 1, v93\n"
"v_and_b32 v92, 1, v92\n"
"v_lshlrev_b32 v92, 4, v92\n"
"v_mfma_f32_32x32x16_bf16 v[128:143], a[4:7], a[168:171], v[128:143]\n"
"ds_read_b128 a[188:191], v28 offset:24576\n"
"buffer_load_dwordx4 v39, s[28:31], s66 offen offset:0 lds\n"
"v_lshlrev_b32 v94, v92, s26\n"
"v_accvgpr_read_b32 v97, a240\n"
"v_lshlrev_b32 v96, 16, v97\n"
"v_mfma_f32_32x32x16_bf16 v[144:159], a[4:7], a[172:175], v[144:159]\n"
"s_waitcnt lgkmcnt(2)\n"
"ds_read_b128 a[160:163], v28 offset:28672\n"
"s_waitcnt vmcnt(21)\n"
"v_and_b32 v97, s100, v97\n"
"v_fma_f32 v96, v96, v5, -v6\n"
"v_fma_f32 v97, v97, v5, -v6\n"
"v_cvt_pk_bf16_f32 v224, v96, v97\n"
"v_mfma_f32_32x32x16_bf16 v[160:175], a[4:7], a[176:179], v[160:175]\n"
"ds_read_b128 a[164:167], v28 offset:32768\n"
"buffer_store_dword v7, v4, s[40:43], s4 offen offset:256 sc1\n"
"v_accvgpr_read_b32 v99, a241\n"
"v_lshlrev_b32 v98, 16, v99\n"
"v_mfma_f32_32x32x16_bf16 v[176:191], a[4:7], a[180:183], v[176:191]\n"
"s_waitcnt lgkmcnt(2)\n"
"ds_read_b128 a[168:171], v28 offset:36864\n"
"buffer_load_dwordx4 v40, s[28:31], s66 offen offset:1024 lds\n"
"v_and_b32 v99, s100, v99\n"
"v_fma_f32 v98, v98, v5, -v6\n"
"v_fma_f32 v99, v99, v5, -v6\n"
"v_mfma_f32_32x32x16_bf16 v[112:127], a[8:11], a[184:187], v[112:127]\n"
"ds_read_b128 a[172:175], v29 offset:20480\n"
"v_cvt_pk_bf16_f32 v225, v98, v99\n"
"v_accvgpr_read_b32 v97, a242\n"
"v_lshlrev_b32 v96, 16, v97\n"
"v_and_b32 v97, s100, v97\n"
"v_fma_f32 v96, v96, v5, -v6\n"
"v_mfma_f32_32x32x16_bf16 v[128:143], a[8:11], a[188:191], v[128:143]\n"
"s_waitcnt lgkmcnt(2)\n"
"ds_read_b128 a[176:179], v29 offset:24576\n"
"v_fma_f32 v97, v97, v5, -v6\n"
"v_cvt_pk_bf16_f32 v226, v96, v97\n"
"v_accvgpr_read_b32 v99, a243\n"
"v_lshlrev_b32 v98, 16, v99\n"
"v_and_b32 v99, s100, v99\n"
"v_mfma_f32_32x32x16_bf16 v[144:159], a[8:11], a[160:163], v[144:159]\n"
"ds_read_b128 a[180:183], v29 offset:28672\n"
"buffer_load_dwordx4 v41, s[28:31], s66 offen offset:2048 lds\n"
"v_fma_f32 v98, v98, v5, -v6\n"
"v_fma_f32 v99, v99, v5, -v6\n"
"v_cvt_pk_bf16_f32 v227, v98, v99\n"
"v_mfma_f32_32x32x16_bf16 v[160:175], a[8:11], a[164:167], v[160:175]\n"
"s_waitcnt vmcnt(23) lgkmcnt(2)\n"
"ds_read_b128 a[184:187], v29 offset:32768\n"
"v_accvgpr_read_b32 v97, a244\n"
"v_lshlrev_b32 v96, 16, v97\n"
"v_and_b32 v97, s100, v97\n"
"v_fma_f32 v96, v96, v5, -v6\n"
"v_fma_f32 v97, v97, v5, -v6\n"
"v_mfma_f32_32x32x16_bf16 v[176:191], a[8:11], a[168:171], v[176:191]\n"
"s_barrier\n"
"ds_read_b128 a[188:191], v29 offset:36864\n"
"buffer_load_dwordx4 v42, s[28:31], s66 offen offset:3072 lds\n"
"s_add_u32 m0, s59, 86016\n"
"v_mfma_f32_32x32x16_bf16 v[112:127], a[12:15], a[172:175], v[112:127]\n"
"s_waitcnt lgkmcnt(2)\n"
"ds_read_b128 a[160:163], v10 offset:40960\n"
"v_cvt_pk_bf16_f32 v228, v96, v97\n"
"v_accvgpr_read_b32 v99, a245\n"
"v_lshlrev_b32 v98, 16, v99\n"
"v_and_b32 v99, s100, v99\n"
"v_fma_f32 v98, v98, v5, -v6\n"
"v_mfma_f32_32x32x16_bf16 v[128:143], a[12:15], a[176:179], v[128:143]\n"
"ds_read_b128 a[164:167], v10 offset:45056\n"
"s_lshl_b32 s7, 1, s55\n"
"s_andn2_b32 s7, 12, s7\n"
"buffer_load_dwordx4 v43, s[28:31], s66 offen lds\n"
"s_mul_i32 s66, s55, -2\n"
"s_add_u32 s66, s66, 1\n"
"v_mfma_f32_32x32x16_bf16 v[144:159], a[12:15], a[180:183], v[144:159]\n"
"s_waitcnt lgkmcnt(2)\n"
"ds_read_b128 a[168:171], v10 offset:49152\n"
"s_add_u32 s66, s66, s64\n"
"s_mul_i32 s66, s66, 81920\n"
"s_add_u32 m0, s59, 122880\n"
"v_mov_b32 v3, 0\n"
"v_fma_f32 v99, v99, v5, -v6\n"
"v_mfma_f32_32x32x16_bf16 v[160:175], a[12:15], a[184:187], v[160:175]\n"
"ds_read_b128 a[172:175], v10 offset:53248\n"
"buffer_atomic_add v3, v1, s[40:43], s4 offen offset:0 sc0 sc1\n"
"v_cvt_pk_bf16_f32 v229, v98, v99\n"
"v_accvgpr_read_b32 v97, a246\n"
"v_lshlrev_b32 v96, 16, v97\n"
"v_mfma_f32_32x32x16_bf16 v[176:191], a[12:15], a[188:191], v[176:191]\n"
"s_waitcnt vmcnt(25) lgkmcnt(2)\n"
"ds_read_b128 a[176:179], v10 offset:57344\n"
"buffer_load_dwordx4 v39, s[28:31], s66 offen offset:0 lds\n"
"v_and_b32 v97, s100, v97\n"
"v_fma_f32 v96, v96, v5, -v6\n"
"v_fma_f32 v97, v97, v5, -v6\n"
"v_mfma_f32_32x32x16_bf16 v[112:127], a[16:19], a[160:163], v[112:127]\n"
"ds_read_b128 a[180:183], v11 offset:40960\n"
"v_cvt_pk_bf16_f32 v230, v96, v97\n"
"v_accvgpr_read_b32 v99, a247\n"
"v_lshlrev_b32 v98, 16, v99\n"
"v_and_b32 v99, s100, v99\n"
"v_fma_f32 v98, v98, v5, -v6\n"
"v_mfma_f32_32x32x16_bf16 v[128:143], a[16:19], a[164:167], v[128:143]\n"
"s_waitcnt lgkmcnt(2)\n"
"ds_read_b128 a[184:187], v11 offset:45056\n"
"buffer_load_dwordx4 v40, s[28:31], s66 offen offset:1024 lds\n"
"v_fma_f32 v99, v99, v5, -v6\n"
"v_cvt_pk_bf16_f32 v231, v98, v99\n"
"v_accvgpr_read_b32 v97, a248\n"
"v_mfma_f32_32x32x16_bf16 v[144:159], a[16:19], a[168:171], v[144:159]\n"
"ds_read_b128 a[188:191], v11 offset:49152\n"
"v_lshlrev_b32 v96, 16, v97\n"
"v_and_b32 v97, s100, v97\n"
"v_fma_f32 v96, v96, v5, -v6\n"
"v_fma_f32 v97, v97, v5, -v6\n"
"v_cvt_pk_bf16_f32 v232, v96, v97\n"
"v_mfma_f32_32x32x16_bf16 v[160:175], a[16:19], a[172:175], v[160:175]\n"
"s_waitcnt vmcnt(26) lgkmcnt(2)\n"
"ds_read_b128 a[160:163], v11 offset:53248\n"
"buffer_load_dwordx4 v41, s[28:31], s66 offen offset:2048 lds\n"
"v_accvgpr_read_b32 v99, a249\n"
"v_lshlrev_b32 v98, 16, v99\n"
"v_and_b32 v99, s100, v99\n"
"v_mfma_f32_32x32x16_bf16 v[176:191], a[16:19], a[176:179], v[176:191]\n"
"ds_read_b128 a[164:167], v11 offset:57344\n"
"v_fma_f32 v98, v98, v5, -v6\n"
"v_fma_f32 v99, v99, v5, -v6\n"
"v_cvt_pk_bf16_f32 v233, v98, v99\n"
"v_accvgpr_read_b32 v97, a250\n"
"v_lshlrev_b32 v96, 16, v97\n"
"v_mfma_f32_32x32x16_bf16 v[112:127], a[20:23], a[180:183], v[112:127]\n"
"s_waitcnt lgkmcnt(2)\n"
"ds_read_b128 a[168:171], v12 offset:40960\n"
"v_and_b32 v97, s100, v97\n"
"v_fma_f32 v96, v96, v5, -v6\n"
"v_fma_f32 v97, v97, v5, -v6\n"
"v_cvt_pk_bf16_f32 v234, v96, v97\n"
"v_mfma_f32_32x32x16_bf16 v[128:143], a[20:23], a[184:187], v[128:143]\n"
"ds_read_b128 a[172:175], v12 offset:45056\n"
"buffer_load_dwordx4 v42, s[28:31], s66 offen offset:3072 lds\n"
"s_add_u32 m0, s59, 126976\n"
"v_accvgpr_read_b32 v99, a251\n"
"v_lshlrev_b32 v98, 16, v99\n"
"v_mfma_f32_32x32x16_bf16 v[144:159], a[20:23], a[188:191], v[144:159]\n"
"s_waitcnt lgkmcnt(2)\n"
"ds_read_b128 a[176:179], v12 offset:49152\n"
"v_and_b32 v99, s100, v99\n"
"v_fma_f32 v98, v98, v5, -v6\n"
"v_fma_f32 v99, v99, v5, -v6\n"
"v_cvt_pk_bf16_f32 v235, v98, v99\n"
"v_mfma_f32_32x32x16_bf16 v[160:175], a[20:23], a[160:163], v[160:175]\n"
"ds_read_b128 a[180:183], v12 offset:53248\n"
"buffer_load_dwordx4 v43, s[28:31], s66 offen lds\n"
"s_mul_i32 s66, s54, 6\n"
"s_add_u32 s66, s66, 1\n"
"s_mul_i32 s66, s66, 81920\n"
"s_add_u32 m0, s59, 0\n"
"v_mfma_f32_32x32x16_bf16 v[176:191], a[20:23], a[164:167], v[176:191]\n"
"s_waitcnt vmcnt(28) lgkmcnt(2)\n"
"ds_read_b128 a[184:187], v12 offset:57344\n"
"v_accvgpr_read_b32 v97, a252\n"
"v_lshlrev_b32 v96, 16, v97\n"
"v_and_b32 v97, s100, v97\n"
"v_fma_f32 v96, v96, v5, -v6\n"
"v_fma_f32 v97, v97, v5, -v6\n"
"v_mfma_f32_32x32x16_bf16 v[112:127], a[24:27], a[168:171], v[112:127]\n"
"ds_read_b128 a[188:191], v13 offset:40960\n"
"buffer_load_dwordx4 v39, s[28:31], s66 offen offset:0 lds\n"
"v_cvt_pk_bf16_f32 v236, v96, v97\n"
"v_accvgpr_read_b32 v99, a253\n"
"v_lshlrev_b32 v98, 16, v99\n"
"v_mfma_f32_32x32x16_bf16 v[128:143], a[24:27], a[172:175], v[128:143]\n"
"s_waitcnt lgkmcnt(2)\n"
"ds_read_b128 a[160:163], v13 offset:45056\n"
"v_and_b32 v99, s100, v99\n"
"v_fma_f32 v98, v98, v5, -v6\n"
"v_fma_f32 v99, v99, v5, -v6\n"
"v_cvt_pk_bf16_f32 v237, v98, v99\n"
"v_mfma_f32_32x32x16_bf16 v[144:159], a[24:27], a[176:179], v[144:159]\n"
"ds_read_b128 a[164:167], v13 offset:49152\n"
"buffer_load_dwordx4 v40, s[28:31], s66 offen offset:1024 lds\n"
"v_accvgpr_read_b32 v97, a254\n"
"v_lshlrev_b32 v96, 16, v97\n"
"v_and_b32 v97, s100, v97\n"
"v_mfma_f32_32x32x16_bf16 v[160:175], a[24:27], a[180:183], v[160:175]\n"
"s_waitcnt vmcnt(29) lgkmcnt(2)\n"
"ds_read_b128 a[168:171], v13 offset:53248\n"
"s_waitcnt vmcnt(7)\n"
"v_cmp_ne_u32 s[0:1], s56, v3\n"
"s_nop 0\n"
"s_and_b32 s0, s0, s7\n"
"s_cbranch_scc1 Lh0B_slow%=\n"
"Lh0B_ok%=:\n"
"s_mul_i32 s2, s54, 6\n"
"s_add_u32 s2, s2, 0\n"
"s_lshl_b32 s2, s2, 12\n"
"v_mfma_f32_32x32x16_bf16 v[176:191], a[24:27], a[184:187], v[176:191]\n"
"s_barrier\n"
"ds_read_b128 a[172:175], v13 offset:57344\n"
"s_add_u32 s2, s2, 81920\n"
"s_add_u32 s2, s2, s65\n"
"s_add_u32 s3, s2, 4096\n"
"buffer_load_dwordx4 a[48:51], v44, s[40:43], s2 offen offset:0 sc1\n"
"v_mfma_f32_32x32x16_bf16 v[112:127], a[28:31], a[188:191], v[112:127]\n"
"s_waitcnt lgkmcnt(2)\n"
"ds_read_b128 a[176:179], v14 offset:4096\n"
"v_fma_f32 v96, v96, v5, -v6\n"
"v_fma_f32 v97, v97, v5, -v6\n"
"v_cvt_pk_bf16_f32 v238, v96, v97\n"
"v_accvgpr_read_b32 v99, a255\n"
"v_mfma_f32_32x32x16_bf16 v[128:143], a[28:31], a[160:163], v[128:143]\n"
"ds_read_b128 a[180:183], v14 offset:8192\n"
"buffer_load_dwordx4 a[52:55], v44, s[40:43], s2 offen offset:1024 sc1\n"
"v_lshlrev_b32 v98, 16, v99\n"
"v_and_b32 v99, s100, v99\n"
"v_fma_f32 v98, v98, v5, -v6\n"
"v_mfma_f32_32x32x16_bf16 v[144:159], a[28:31], a[164:167], v[144:159]\n"
"s_waitcnt lgkmcnt(2)\n"
"ds_read_b128 a[184:187], v14 offset:12288\n"
"v_fma_f32 v99, v99, v5, -v6\n"
"v_cvt_pk_bf16_f32 v239, v98, v99\n"
"v_cmp_eq_u32 vcc, 0, v93\n"
"v_lshlrev_b32 v90, v92, v90\n"
"v_lshlrev_b32 v91, v92, v91\n"
"v_mfma_f32_32x32x16_bf16 v[160:175], a[28:31], a[168:171], v[160:175]\n"
"ds_read_b128 a[188:191], v14 offset:16384\n"
"buffer_load_dwordx4 a[56:59], v44, s[40:43], s2 offen offset:2048 sc1\n"
"v_cmp_eq_u32 s[78:79], 1, v93\n"
"v_cndmask_b32 v50, 0, v94, vcc\n"
"v_cndmask_b32 v58, 0, v90, vcc\n"
"v_mfma_f32_32x32x16_bf16 v[176:191], a[28:31], a[172:175], v[176:191]\n"
"s_waitcnt lgkmcnt(2)\n"
"ds_read_b128 a[160:163], v15 offset:4096\n"
"v_cndmask_b32 v66, 0, v91, vcc\n"
"v_cmp_eq_u32 vcc, 2, v93\n"
"v_cndmask_b32 v51, 0, v94, s[78:79]\n"
"v_cndmask_b32 v59, 0, v90, s[78:79]\n"
"v_cndmask_b32 v67, 0, v91, s[78:79]\n"
"v_mfma_f32_32x32x16_bf16 v[128:143], a[32:35], a[176:179], v[128:143]\n"
"ds_read_b128 a[164:167], v15 offset:8192\n"
"buffer_load_dwordx4 a[60:63], v44, s[40:43], s2 offen offset:3072 sc1\n"
"s_mul_i32 s2, s54, 6\n"
"s_add_u32 s2, s2, 2\n"
"s_lshl_b32 s2, s2, 12\n"
"s_add_u32 s2, s2, 81920\n"
"v_mfma_f32_32x32x16_bf16 v[144:159], a[32:35], a[180:183], v[144:159]\n"
"s_waitcnt lgkmcnt(2)\n"
"ds_read_b128 a[168:171], v15 offset:12288\n"
"s_add_u32 s2, s2, s65\n"
"s_add_u32 s3, s2, 4096\n"
"buffer_load_dwordx4 a[64:67], v44, s[40:43], s2 offen offset:0 sc1\n"
"v_cmp_eq_u32 s[78:79], 3, v93\n"
"v_mfma_f32_32x32x16_bf16 v[160:175], a[32:35], a[184:187], v[160:175]\n"
"ds_read_b128 a[172:175], v15 offset:16384\n"
"v_cndmask_b32 v52, 0, v94, vcc\n"
"v_cndmask_b32 v60, 0, v90, vcc\n"
"v_cndmask_b32 v68, 0, v91, vcc\n"
"v_cmp_eq_u32 vcc, 8, v93\n"
"v_cndmask_b32 v53, 0, v94, s[78:79]\n"
"v_mfma_f32_32x32x16_bf16 v[176:191], a[32:35], a[188:191], v[176:191]\n"
"s_waitcnt lgkmcnt(2)\n"
"ds_read_b128 a[176:179], v16 offset:4096\n"
"buffer_load_dwordx4 a[68:71], v44, s[40:43], s2 offen offset:1024 sc1\n"
"v_cndmask_b32 v61, 0, v90, s[78:79]\n"
"v_cndmask_b32 v69, 0, v91, s[78:79]\n"
"v_cmp_eq_u32 s[78:79], 9, v93\n"
"v_mfma_f32_32x32x16_bf16 v[128:143], a[36:39], a[160:163], v[128:143]\n"
"ds_read_b128 a[180:183], v16 offset:8192\n"
"v_cndmask_b32 v54, 0, v94, vcc\n"
"v_cndmask_b32 v62, 0, v90, vcc\n"
"v_cndmask_b32 v70, 0, v91, vcc\n"
"v_cmp_eq_u32 vcc, 10, v93\n"
"v_cndmask_b32 v55, 0, v94, s[78:79]\n"
"v_mfma_f32_32x32x16_bf16 v[144:159], a[36:39], a[164:167], v[144:159]\n"
"s_waitcnt lgkmcnt(2)\n"
"ds_read_b128 a[184:187], v16 offset:12288\n"
"buffer_load_dwordx4 a[72:75], v44, s[40:43], s2 offen offset:2048 sc1\n"
"v_cndmask_b32 v63, 0, v90, s[78:79]\n"
"v_cndmask_b32 v71, 0, v91, s[78:79]\n"
"v_cmp_eq_u32 s[78:79], 11, v93\n"
"v_mfma_f32_32x32x16_bf16 v[160:175], a[36:39], a[168:171], v[160:175]\n"
"ds_read_b128 a[188:191], v16 offset:16384\n"
"v_cndmask_b32 v56, 0, v94, vcc\n"
"v_cndmask_b32 v64, 0, v90, vcc\n"
"v_cndmask_b32 v72, 0, v91, vcc\n"
"v_cndmask_b32 v57, 0, v94, s[78:79]\n"
"v_cndmask_b32 v65, 0, v90, s[78:79]\n"
"v_mfma_f32_32x32x16_bf16 v[176:191], a[36:39], a[172:175], v[176:191]\n"
"s_waitcnt lgkmcnt(2)\n"
"ds_read_b128 a[160:163], v17 offset:4096\n"
"buffer_load_dwordx4 a[76:79], v44, s[40:43], s2 offen offset:3072 sc1\n"
"s_lshl_b32 s7, 1, s55\n"
"s_andn2_b32 s7, 15, s7\n"
"v_mov_b32 v3, 0\n"
"v_mfma_f32_32x32x16_bf16 v[128:143], a[40:43], a[176:179], v[128:143]\n"
"ds_read_b128 a[164:167], v17 offset:8192\n"
"v_cndmask_b32 v73, 0, v91, s[78:79]\n"
"v_mfma_f32_32x32x16_bf16 v[144:159], a[40:43], a[180:183], v[144:159]\n"
"s_waitcnt lgkmcnt(2)\n"
"ds_read_b128 a[168:171], v17 offset:12288\n"
"buffer_atomic_add v3, v1, s[40:43], s4 offen offset:256 sc0 sc1\n"
"v_mfma_f32_32x32x16_bf16 v[160:175], a[40:43], a[184:187], v[160:175]\n"
"s_barrier\n"
"ds_read_b128 a[172:175], v17 offset:16384\n"
"buffer_load_dwordx4 v41, s[28:31], s66 offen offset:2048 lds\n"
"v_mfma_f32_32x32x16_bf16 v[176:191], a[40:43], a[188:191], v[176:191]\n"
"s_waitcnt lgkmcnt(2)\n"
"ds_read_b64_tr_b16 a[176:177], v27 offset:41024\n"
"ds_read_b64_tr_b16 a[178:179], v27 offset:42304\n"
"v_mfma_f32_32x32x16_bf16 v[128:143], a[44:47], a[160:163], v[128:143]\n"
"ds_read_b64_tr_b16 a[180:181], v27 offset:41088\n"
"ds_read_b64_tr_b16 a[182:183], v27 offset:42368\n"
"buffer_load_dwordx4 v42, s[28:31], s66 offen offset:3072 lds\n"
"s_add_u32 m0, s59, 4096\n"
"v_mfma_f32_32x32x16_bf16 v[144:159], a[44:47], a[164:167], v[144:159]\n"
"s_waitcnt lgkmcnt(4)\n"
"ds_read_b64_tr_b16 a[184:185], v27 offset:41152\n"
"ds_read_b64_tr_b16 a[186:187], v27 offset:42432\n"
"v_mfma_f32_32x32x16_bf16 v[160:175], a[44:47], a[168:171], v[160:175]\n"
"ds_read_b64_tr_b16 a[188:189], v27 offset:41216\n"
"ds_read_b64_tr_b16 a[190:191], v27 offset:42496\n"
"buffer_load_dwordx4 v43, s[28:31], s66 offen lds\n"
"s_mul_i32 s66, s54, 6\n"
"s_add_u32 s66, s66, 3\n"
"s_mul_i32 s66, s66, 81920\n"
"v_mfma_f32_32x32x16_bf16 v[176:191], a[44:47], a[172:175], v[176:191]\n"
"s_waitcnt vmcnt(11) lgkmcnt(4)\n"
"ds_read_b64_tr_b16 a[160:161], v27 offset:46144\n"
"ds_read_b64_tr_b16 a[162:163], v27 offset:47424\n"
"s_add_u32 m0, s59, 20480\n"
"v_mfma_f32_32x32x16_bf16 v[128:143], a[48:51], a[176:179], v[128:143]\n"
"ds_read_b64_tr_b16 a[164:165], v27 offset:46208\n"
"ds_read_b64_tr_b16 a[166:167], v27 offset:47488\n"
"buffer_load_dwordx4 v39, s[28:31], s66 offen offset:0 lds\n"
"v_mfma_f32_32x32x16_bf16 v[144:159], a[48:51], a[180:183], v[144:159]\n"
"s_waitcnt lgkmcnt(4)\n"
"ds_read_b64_tr_b16 a[168:169], v27 offset:46272\n"
"ds_read_b64_tr_b16 a[170:171], v27 offset:47552\n"
"v_mfma_f32_32x32x16_bf16 v[160:175], a[48:51], a[184:187], v[160:175]\n"
"ds_read_b64_tr_b16 a[172:173], v27 offset:46336\n"
"ds_read_b64_tr_b16 a[174:175], v27 offset:47616\n"
"buffer_load_dwordx4 v40, s[28:31], s66 offen offset:1024 lds\n"
"v_mfma_f32_32x32x16_bf16 v[176:191], a[48:51], a[188:191], v[176:191]\n"
"s_waitcnt vmcnt(12) lgkmcnt(4)\n"
"ds_read_b64_tr_b16 a[176:177], v27 offset:51264\n"
"ds_read_b64_tr_b16 a[178:179], v27 offset:52544\n"
"v_mfma_f32_32x32x16_bf16 v[128:143], a[52:55], a[160:163], v[128:143]\n"
"ds_read_b64_tr_b16 a[180:181], v27 offset:51328\n"
"ds_read_b64_tr_b16 a[182:183], v27 offset:52608\n"
"v_mfma_f32_32x32x16_bf16 v[144:159], a[52:55], a[164:167], v[144:159]\n"
"s_waitcnt lgkmcnt(4)\n"
"ds_read_b64_tr_b16 a[184:185], v27 offset:51392\n"
"ds_read_b64_tr_b16 a[186:187], v27 offset:52672\n"
"buffer_load_dwordx4 v41, s[28:31], s66 offen offset:2048 lds\n"
"v_mfma_f32_32x32x16_bf16 v[160:175], a[52:55], a[168:171], v[160:175]\n"
"ds_read_b64_tr_b16 a[188:189], v27 offset:51456\n"
"ds_read_b64_tr_b16 a[190:191], v27 offset:52736\n"
"v_mfma_f32_32x32x16_bf16 v[176:191], a[52:55], a[172:175], v[176:191]\n"
"s_waitcnt vmcnt(12) lgkmcnt(4)\n"
"ds_read_b64_tr_b16 a[160:161], v27 offset:56384\n"
"ds_read_b64_tr_b16 a[162:163], v27 offset:57664\n"
"buffer_load_dwordx4 v42, s[28:31], s66 offen offset:3072 lds\n"
"s_add_u32 m0, s59, 24576\n"
"v_mfma_f32_32x32x16_bf16 v[128:143], a[56:59], a[176:179], v[128:143]\n"
"ds_read_b64_tr_b16 a[164:165], v27 offset:56448\n"
"ds_read_b64_tr_b16 a[166:167], v27 offset:57728\n"
"s_waitcnt vmcnt(7)\n"
"v_cmp_ne_u32 s[0:1], s56, v3\n"
"s_nop 0\n"
"s_and_b32 s0, s0, s7\n"
"s_cbranch_scc1 Lh2B_slow%=\n"
"Lh2B_ok%=:\n"
"s_mul_i32 s2, s55, -2\n"
"s_add_u32 s2, s2, 1\n"
"v_mfma_f32_32x32x16_bf16 v[144:159], a[56:59], a[180:183], v[144:159]\n"
"s_waitcnt lgkmcnt(4)\n"
"ds_read_b64_tr_b16 a[168:169], v27 offset:56512\n"
"ds_read_b64_tr_b16 a[170:171], v27 offset:57792\n"
"s_add_u32 s2, s2, s64\n"
"s_lshl_b32 s2, s2, 12\n"
"s_add_u32 s2, s2, 81920\n"
"s_add_u32 s2, s2, s65\n"
"s_add_u32 s3, s2, 4096\n"
"v_mfma_f32_32x32x16_bf16 v[160:175], a[56:59], a[184:187], v[160:175]\n"
"s_barrier\n"
"ds_read_b64_tr_b16 a[172:173], v27 offset:56576\n"
"ds_read_b64_tr_b16 a[174:175], v27 offset:57856\n"
"buffer_load_dwordx4 v43, s[28:31], s66 offen lds\n"
"v_mfma_f32_32x32x16_bf16 v[176:191], a[56:59], a[188:191], v[176:191]\n"
"s_waitcnt lgkmcnt(4)\n"
"ds_read_b64_tr_b16 a[176:177], v27 offset:20544\n"
"ds_read_b64_tr_b16 a[178:179], v27 offset:21824\n"
"v_mfma_f32_32x32x16_bf16 v[128:143], a[60:63], a[160:163], v[128:143]\n"
"ds_read_b64_tr_b16 a[180:181], v27 offset:20608\n"
"ds_read_b64_tr_b16 a[182:183], v27 offset:21888\n"
"buffer_load_dwordx4 a[80:83], v44, s[40:43], s2 offen offset:0 sc1\n"
"v_mfma_f32_32x32x16_bf16 v[144:159], a[60:63], a[164:167], v[144:159]\n"
"s_waitcnt lgkmcnt(4)\n"
"ds_read_b64_tr_b16 a[184:185], v27 offset:20672\n"
"ds_read_b64_tr_b16 a[186:187], v27 offset:21952\n"
"v_mfma_f32_32x32x16_bf16 v[160:175], a[60:63], a[168:171], v[160:175]\n"
"ds_read_b64_tr_b16 a[188:189], v27 offset:20736\n"
"ds_read_b64_tr_b16 a[190:191], v27 offset:22016\n"
"buffer_load_dwordx4 a[84:87], v44, s[40:43], s2 offen offset:1024 sc1\n"
"v_mfma_f32_32x32x16_bf16 v[176:191], a[60:63], a[172:175], v[176:191]\n"
"s_waitcnt lgkmcnt(4)\n"
"ds_read_b64_tr_b16 a[160:161], v27 offset:25664\n"
"ds_read_b64_tr_b16 a[162:163], v27 offset:26944\n"
"v_mfma_f32_32x32x16_bf16 v[128:143], a[64:67], a[176:179], v[128:143]\n"
"ds_read_b64_tr_b16 a[164:165], v27 offset:25728\n"
"ds_read_b64_tr_b16 a[166:167], v27 offset:27008\n"
"buffer_load_dwordx4 a[88:91], v44, s[40:43], s2 offen offset:2048 sc1\n"
"v_mfma_f32_32x32x16_bf16 v[144:159], a[64:67], a[180:183], v[144:159]\n"
"s_waitcnt lgkmcnt(4)\n"
"ds_read_b64_tr_b16 a[168:169], v27 offset:25792\n"
"ds_read_b64_tr_b16 a[170:171], v27 offset:27072\n"
"buffer_load_dwordx4 a[92:95], v44, s[40:43], s2 offen offset:3072 sc1\n"
"s_mul_i32 s2, s54, 6\n"
"s_add_u32 s2, s2, 1\n"
"s_lshl_b32 s2, s2, 12\n"
"v_mfma_f32_32x32x16_bf16 v[160:175], a[64:67], a[184:187], v[160:175]\n"
"ds_read_b64_tr_b16 a[172:173], v27 offset:25856\n"
"ds_read_b64_tr_b16 a[174:175], v27 offset:27136\n"
"s_add_u32 s2, s2, 81920\n"
"s_add_u32 s2, s2, s65\n"
"s_add_u32 s3, s2, 4096\n"
"v_mfma_f32_32x32x16_bf16 v[176:191], a[64:67], a[188:191], v[176:191]\n"
"s_waitcnt lgkmcnt(4)\n"
"ds_read_b64_tr_b16 a[176:177], v27 offset:30784\n"
"ds_read_b64_tr_b16 a[178:179], v27 offset:32064\n"
"buffer_load_dwordx4 a[96:99], v44, s[40:43], s2 offen offset:0 sc1\n"
"v_mfma_f32_32x32x16_bf16 v[128:143], a[68:71], a[160:163], v[128:143]\n"
"ds_read_b64_tr_b16 a[180:181], v27 offset:30848\n"
"ds_read_b64_tr_b16 a[182:183], v27 offset:32128\n"
"v_mfma_f32_32x32x16_bf16 v[144:159], a[68:71], a[164:167], v[144:159]\n"
"s_waitcnt lgkmcnt(4)\n"
"ds_read_b64_tr_b16 a[184:185], v27 offset:30912\n"
"ds_read_b64_tr_b16 a[186:187], v27 offset:32192\n"
"buffer_load_dwordx4 a[100:103], v44, s[40:43], s2 offen offset:1024 sc1\n"
"v_mfma_f32_32x32x16_bf16 v[160:175], a[68:71], a[168:171], v[160:175]\n"
"ds_read_b64_tr_b16 a[188:189], v27 offset:30976\n"
"ds_read_b64_tr_b16 a[190:191], v27 offset:32256\n"
"v_mfma_f32_32x32x16_bf16 v[176:191], a[68:71], a[172:175], v[176:191]\n"
"s_waitcnt lgkmcnt(4)\n"
"ds_read_b64_tr_b16 a[160:161], v27 offset:35904\n"
"ds_read_b64_tr_b16 a[162:163], v27 offset:37184\n"
"buffer_load_dwordx4 a[104:107], v44, s[40:43], s2 offen offset:2048 sc1\n"
"v_mfma_f32_32x32x16_bf16 v[128:143], a[72:75], a[176:179], v[128:143]\n"
"ds_read_b64_tr_b16 a[164:165], v27 offset:35968\n"
"ds_read_b64_tr_b16 a[166:167], v27 offset:37248\n"
"v_mfma_f32_32x32x16_bf16 v[144:159], a[72:75], a[180:183], v[144:159]\n"
"s_waitcnt lgkmcnt(4)\n"
"ds_read_b64_tr_b16 a[168:169], v27 offset:36032\n"
"ds_read_b64_tr_b16 a[170:171], v27 offset:37312\n"
"buffer_load_dwordx4 a[108:111], v44, s[40:43], s2 offen offset:3072 sc1\n"
"s_mul_i32 s2, s54, 6\n"
"s_add_u32 s2, s2, 3\n"
"s_lshl_b32 s2, s2, 12\n"
"v_mfma_f32_32x32x16_bf16 v[160:175], a[72:75], a[184:187], v[160:175]\n"
"s_barrier\n"
"ds_read_b64_tr_b16 a[172:173], v27 offset:36096\n"
"ds_read_b64_tr_b16 a[174:175], v27 offset:37376\n"
"s_add_u32 s2, s2, 81920\n"
"s_add_u32 s2, s2, s65\n"
"s_add_u32 s3, s2, 4096\n"
"v_mfma_f32_32x32x16_bf16 v[176:191], a[72:75], a[188:191], v[176:191]\n"
"s_waitcnt lgkmcnt(4)\n"
"ds_read_b64_tr_b16 a[176:177], v80 offset:64\n"
"ds_read_b64_tr_b16 a[178:179], v80 offset:1344\n"
"buffer_load_dwordx4 a[112:115], v44, s[40:43], s2 offen offset:0 sc1\n"
"v_mfma_f32_32x32x16_bf16 v[128:143], a[76:79], a[160:163], v[128:143]\n"
"ds_read_b64_tr_b16 a[180:181], v80 offset:128\n"
"ds_read_b64_tr_b16 a[182:183], v80 offset:1408\n"
"buffer_load_dwordx4 a[116:119], v44, s[40:43], s2 offen offset:1024 sc1\n"
"v_mfma_f32_32x32x16_bf16 v[144:159], a[76:79], a[164:167], v[144:159]\n"
"s_waitcnt lgkmcnt(4)\n"
"ds_read_b64_tr_b16 a[184:185], v80 offset:192\n"
"ds_read_b64_tr_b16 a[186:187], v80 offset:1472\n"
"v_mfma_f32_32x32x16_bf16 v[160:175], a[76:79], a[168:171], v[160:175]\n"
"ds_read_b64_tr_b16 a[188:189], v80 offset:256\n"
"ds_read_b64_tr_b16 a[190:191], v80 offset:1536\n"
"buffer_load_dwordx4 a[120:123], v44, s[40:43], s2 offen offset:2048 sc1\n"
"v_mfma_f32_32x32x16_bf16 v[176:191], a[76:79], a[172:175], v[176:191]\n"
"s_waitcnt vmcnt(10) lgkmcnt(4)\n"
"ds_read_b64_tr_b16 a[160:161], v80 offset:5184\n"
"ds_read_b64_tr_b16 a[162:163], v80 offset:6464\n"
"v_mfma_f32_32x32x16_bf16 v[128:143], a[80:83], a[176:179], v[128:143]\n"
"ds_read_b64_tr_b16 a[164:165], v80 offset:5248\n"
"ds_read_b64_tr_b16 a[166:167], v80 offset:6528\n"
"buffer_load_dwordx4 a[124:127], v44, s[40:43], s2 offen offset:3072 sc1\n"
"v_cmp_eq_u32 s[0:1], s54, v2\n"
"v_mov_b32 v4, 0x70000000\n"
"v_mfma_f32_32x32x16_bf16 v[144:159], a[80:83], a[180:183], v[144:159]\n"
"s_waitcnt lgkmcnt(4)\n"
"ds_read_b64_tr_b16 a[168:169], v80 offset:5312\n"
"ds_read_b64_tr_b16 a[170:171], v80 offset:6592\n"
"s_lshl_b32 s7, 1, s54\n"
"s_andn2_b32 s7, 15, s7\n"
"v_cndmask_b32 v4, v4, v1, s[0:1]\n"
"v_mfma_f32_32x32x16_bf16 v[160:175], a[80:83], a[184:187], v[160:175]\n"
"ds_read_b64_tr_b16 a[172:173], v80 offset:5376\n"
"ds_read_b64_tr_b16 a[174:175], v80 offset:6656\n"
"v_mfma_f32_32x32x16_bf16 v[176:191], a[80:83], a[188:191], v[176:191]\n"
"s_waitcnt vmcnt(10) lgkmcnt(4)\n"
"ds_read_b64_tr_b16 a[176:177], v80 offset:10304\n"
"ds_read_b64_tr_b16 a[178:179], v80 offset:11584\n"
"v_mfma_f32_32x32x16_bf16 v[128:143], a[84:87], a[160:163], v[128:143]\n"
"ds_read_b64_tr_b16 a[180:181], v80 offset:10368\n"
"ds_read_b64_tr_b16 a[182:183], v80 offset:11648\n"
"v_mfma_f32_32x32x16_bf16 v[144:159], a[84:87], a[164:167], v[144:159]\n"
"s_waitcnt lgkmcnt(4)\n"
"ds_read_b64_tr_b16 a[184:185], v80 offset:10432\n"
"ds_read_b64_tr_b16 a[186:187], v80 offset:11712\n"
"v_mfma_f32_32x32x16_bf16 v[160:175], a[84:87], a[168:171], v[160:175]\n"
"ds_read_b64_tr_b16 a[188:189], v80 offset:10496\n"
"ds_read_b64_tr_b16 a[190:191], v80 offset:11776\n"
"v_mfma_f32_32x32x16_bf16 v[176:191], a[84:87], a[172:175], v[176:191]\n"
"s_waitcnt vmcnt(9) lgkmcnt(4)\n"
"ds_read_b64_tr_b16 a[160:161], v80 offset:15424\n"
"ds_read_b64_tr_b16 a[162:163], v80 offset:16704\n"
"v_mfma_f32_32x32x16_bf16 v[128:143], a[88:91], a[176:179], v[128:143]\n"
"ds_read_b64_tr_b16 a[164:165], v80 offset:15488\n"
"ds_read_b64_tr_b16 a[166:167], v80 offset:16768\n"
"v_mfma_f32_32x32x16_bf16 v[144:159], a[88:91], a[180:183], v[144:159]\n"
"s_waitcnt lgkmcnt(4)\n"
"ds_read_b64_tr_b16 a[168:169], v80 offset:15552\n"
"ds_read_b64_tr_b16 a[170:171], v80 offset:16832\n"
"v_mfma_f32_32x32x16_bf16 v[160:175], a[88:91], a[184:187], v[160:175]\n"
"ds_read_b64_tr_b16 a[172:173], v80 offset:15616\n"
"ds_read_b64_tr_b16 a[174:175], v80 offset:16896\n"
"v_mfma_f32_32x32x16_bf16 v[176:191], a[88:91], a[188:191], v[176:191]\n"
"s_waitcnt vmcnt(8) lgkmcnt(4)\n"
"v_mfma_f32_32x32x16_bf16 v[128:143], a[92:95], a[160:163], v[128:143]\n"
"v_mfma_f32_32x32x16_bf16 v[144:159], a[92:95], a[164:167], v[144:159]\n"
"s_waitcnt lgkmcnt(0)\n"
"v_mfma_f32_32x32x16_bf16 v[160:175], a[92:95], a[168:171], v[160:175]\n"
"s_barrier\n"
"v_mfma_f32_32x32x16_bf16 v[176:191], a[92:95], a[172:175], v[176:191]\n"
"ds_read_b64_tr_b16 a[176:177], v26 offset:256\n"
"ds_read_b64_tr_b16 a[178:179], v26 offset:1536\n"
"v_mfma_f32_32x32x16_bf16 v[176:191], a[248:251], v[50:53], v[176:191]\n"
"ds_read_b64_tr_b16 a[180:181], v26 offset:5376\n"
"ds_read_b64_tr_b16 a[182:183], v26 offset:6656\n"
"v_mfma_f32_32x32x16_bf16 v[176:191], a[252:255], v[54:57], v[176:191]\n"
"ds_read_b64_tr_b16 a[184:185], v26 offset:10496\n"
"ds_read_b64_tr_b16 a[186:187], v26 offset:11776\n"
"v_mfma_f32_32x32x16_bf16 v[176:191], v[232:235], v[66:69], v[176:191]\n"
"ds_read_b64_tr_b16 a[188:189], v26 offset:15616\n"
"ds_read_b64_tr_b16 a[190:191], v26 offset:16896\n"
"v_mfma_f32_32x32x16_bf16 v[176:191], v[236:239], v[70:73], v[176:191]\n"
"s_waitcnt vmcnt(6) lgkmcnt(4)\n"
"ds_read_b64_tr_b16 a[160:161], v26 offset:20736\n"
"ds_read_b64_tr_b16 a[162:163], v26 offset:22016\n"
"v_mfma_f32_32x32x16_bf16 v[176:191], a[96:99], a[176:179], v[176:191]\n"
"ds_read_b64_tr_b16 a[164:165], v26 offset:25856\n"
"ds_read_b64_tr_b16 a[166:167], v26 offset:27136\n"
"v_mfma_f32_32x32x16_bf16 v[176:191], a[100:103], a[180:183], v[176:191]\n"
"s_waitcnt vmcnt(4) lgkmcnt(4)\n"
"ds_read_b64_tr_b16 a[168:169], v26 offset:30976\n"
"ds_read_b64_tr_b16 a[170:171], v26 offset:32256\n"
"v_mfma_f32_32x32x16_bf16 v[176:191], a[104:107], a[184:187], v[176:191]\n"
"ds_read_b64_tr_b16 a[172:173], v26 offset:36096\n"
"ds_read_b64_tr_b16 a[174:175], v26 offset:37376\n"
"v_mfma_f32_32x32x16_bf16 v[176:191], a[108:111], a[188:191], v[176:191]\n"
"s_waitcnt vmcnt(2) lgkmcnt(4)\n"
"ds_read_b64_tr_b16 a[176:177], v26 offset:64\n"
"ds_read_b64_tr_b16 a[178:179], v26 offset:1344\n"
"v_mfma_f32_32x32x16_bf16 v[176:191], a[112:115], a[160:163], v[176:191]\n"
"ds_read_b64_tr_b16 a[180:181], v26 offset:5184\n"
"ds_read_b64_tr_b16 a[182:183], v26 offset:6464\n"
"v_mfma_f32_32x32x16_bf16 v[176:191], a[116:119], a[164:167], v[176:191]\n"
"s_waitcnt vmcnt(0) lgkmcnt(4)\n"
"ds_read_b64_tr_b16 a[184:185], v26 offset:10304\n"
"ds_read_b64_tr_b16 a[186:187], v26 offset:11584\n"
"v_mfma_f32_32x32x16_bf16 v[176:191], a[120:123], a[168:171], v[176:191]\n"
"ds_read_b64_tr_b16 a[188:189], v26 offset:15424\n"
"ds_read_b64_tr_b16 a[190:191], v26 offset:16704\n"
"v_mfma_f32_32x32x16_bf16 v[176:191], a[124:127], a[172:175], v[176:191]\n"
"s_waitcnt lgkmcnt(4)\n"
"ds_read_b64_tr_b16 a[160:161], v26 offset:20544\n"
"ds_read_b64_tr_b16 a[162:163], v26 offset:21824\n"
"v_mfma_f32_32x32x16_bf16 v[128:143], a[96:99], a[176:179], v[128:143]\n"
"ds_read_b64_tr_b16 a[164:165], v26 offset:25664\n"
"ds_read_b64_tr_b16 a[166:167], v26 offset:26944\n"
"v_mfma_f32_32x32x16_bf16 v[128:143], a[100:103], a[180:183], v[128:143]\n"
"s_waitcnt lgkmcnt(4)\n"
"ds_read_b64_tr_b16 a[168:169], v26 offset:30784\n"
"ds_read_b64_tr_b16 a[170:171], v26 offset:32064\n"
"s_nop 1\n"
"v_cvt_pk_bf16_f32 v240, v176, v177\n"
"v_cvt_pk_bf16_f32 v241, v178, v179\n"
"v_cvt_pk_bf16_f32 v242, v180, v181\n"
"v_cvt_pk_bf16_f32 v243, v182, v183\n"
"v_mfma_f32_32x32x16_bf16 v[128:143], a[104:107], a[184:187], v[128:143]\n"
"ds_read_b64_tr_b16 a[172:173], v26 offset:35904\n"
"ds_read_b64_tr_b16 a[174:175], v26 offset:37184\n"
"buffer_store_dwordx4 v[240:243], v44, s[40:43], s15 offen offset:0 sc1\n"
"v_cvt_pk_bf16_f32 v244, v184, v185\n"
"v_cvt_pk_bf16_f32 v245, v186, v187\n"
"v_mfma_f32_32x32x16_bf16 v[128:143], a[108:111], a[188:191], v[128:143]\n"
"s_waitcnt lgkmcnt(4)\n"
"ds_read_b64_tr_b16 a[176:177], v26 offset:128\n"
"ds_read_b64_tr_b16 a[178:179], v26 offset:1408\n"
"v_cvt_pk_bf16_f32 v246, v188, v189\n"
"v_cvt_pk_bf16_f32 v247, v190, v191\n"
"v_mfma_f32_32x32x16_bf16 v[128:143], a[112:115], a[160:163], v[128:143]\n"
"ds_read_b64_tr_b16 a[180:181], v26 offset:5248\n"
"ds_read_b64_tr_b16 a[182:183], v26 offset:6528\n"
"v_mfma_f32_32x32x16_bf16 v[128:143], a[116:119], a[164:167], v[128:143]\n"
"s_waitcnt lgkmcnt(4)\n"
"ds_read_b64_tr_b16 a[184:185], v26 offset:10368\n"
"ds_read_b64_tr_b16 a[186:187], v26 offset:11648\n"
"buffer_store_dwordx4 v[244:247], v44, s[40:43], s15 offen offset:1024 sc1\n"
"v_mfma_f32_32x32x16_bf16 v[128:143], a[120:123], a[168:171], v[128:143]\n"
"ds_read_b64_tr_b16 a[188:189], v26 offset:15488\n"
"ds_read_b64_tr_b16 a[190:191], v26 offset:16768\n"
"v_mfma_f32_32x32x16_bf16 v[128:143], a[124:127], a[172:175], v[128:143]\n"
"s_waitcnt lgkmcnt(4)\n"
"ds_read_b64_tr_b16 a[160:161], v26 offset:20608\n"
"ds_read_b64_tr_b16 a[162:163], v26 offset:21888\n"
"v_mfma_f32_32x32x16_bf16 v[144:159], a[96:99], a[176:179], v[144:159]\n"
"ds_read_b64_tr_b16 a[164:165], v26 offset:25728\n"
"ds_read_b64_tr_b16 a[166:167], v26 offset:27008\n"
"v_mfma_f32_32x32x16_bf16 v[144:159], a[100:103], a[180:183], v[144:159]\n"
"s_waitcnt lgkmcnt(4)\n"
"ds_read_b64_tr_b16 a[168:169], v26 offset:30848\n"
"ds_read_b64_tr_b16 a[170:171], v26 offset:32128\n"
"s_nop 1\n"
"v_cvt_pk_bf16_f32 v128, v128, v129\n"
"v_cvt_pk_bf16_f32 v129, v130, v131\n"
"v_cvt_pk_bf16_f32 v130, v132, v133\n"
"v_cvt_pk_bf16_f32 v131, v134, v135\n"
"v_mfma_f32_32x32x16_bf16 v[144:159], a[104:107], a[184:187], v[144:159]\n"
"ds_read_b64_tr_b16 a[172:173], v26 offset:35968\n"
"ds_read_b64_tr_b16 a[174:175], v26 offset:37248\n"
"buffer_store_dwordx4 v[128:131], v44, s[40:43], s12 offen offset:0 sc1\n"
"v_cvt_pk_bf16_f32 v132, v136, v137\n"
"v_cvt_pk_bf16_f32 v133, v138, v139\n"
"v_mfma_f32_32x32x16_bf16 v[144:159], a[108:111], a[188:191], v[144:159]\n"
"s_waitcnt lgkmcnt(4)\n"
"ds_read_b64_tr_b16 a[176:177], v26 offset:192\n"
"ds_read_b64_tr_b16 a[178:179], v26 offset:1472\n"
"v_cvt_pk_bf16_f32 v134, v140, v141\n"
"v_cvt_pk_bf16_f32 v135, v142, v143\n"
"v_mfma_f32_32x32x16_bf16 v[144:159], a[112:115], a[160:163], v[144:159]\n"
"ds_read_b64_tr_b16 a[180:181], v26 offset:5312\n"
"ds_read_b64_tr_b16 a[182:183], v26 offset:6592\n"
"v_mfma_f32_32x32x16_bf16 v[144:159], a[116:119], a[164:167], v[144:159]\n"
"s_waitcnt lgkmcnt(4)\n"
"ds_read_b64_tr_b16 a[184:185], v26 offset:10432\n"
"ds_read_b64_tr_b16 a[186:187], v26 offset:11712\n"
"buffer_store_dwordx4 v[132:135], v44, s[40:43], s12 offen offset:1024 sc1\n"
"v_mfma_f32_32x32x16_bf16 v[144:159], a[120:123], a[168:171], v[144:159]\n"
"ds_read_b64_tr_b16 a[188:189], v26 offset:15552\n"
"ds_read_b64_tr_b16 a[190:191], v26 offset:16832\n"
"v_mfma_f32_32x32x16_bf16 v[144:159], a[124:127], a[172:175], v[144:159]\n"
"s_waitcnt lgkmcnt(4)\n"
"ds_read_b64_tr_b16 a[160:161], v26 offset:20672\n"
"ds_read_b64_tr_b16 a[162:163], v26 offset:21952\n"
"v_mfma_f32_32x32x16_bf16 v[160:175], a[96:99], a[176:179], v[160:175]\n"
"ds_read_b64_tr_b16 a[164:165], v26 offset:25792\n"
"ds_read_b64_tr_b16 a[166:167], v26 offset:27072\n"
"v_mfma_f32_32x32x16_bf16 v[160:175], a[100:103], a[180:183], v[160:175]\n"
"s_waitcnt lgkmcnt(4)\n"
"ds_read_b64_tr_b16 a[168:169], v26 offset:30912\n"
"ds_read_b64_tr_b16 a[170:171], v26 offset:32192\n"
"s_nop 1\n"
"v_cvt_pk_bf16_f32 v144, v144, v145\n"
"v_cvt_pk_bf16_f32 v145, v146, v147\n"
"v_cvt_pk_bf16_f32 v146, v148, v149\n"
"v_cvt_pk_bf16_f32 v147, v150, v151\n"
"v_mfma_f32_32x32x16_bf16 v[160:175], a[104:107], a[184:187], v[160:175]\n"
"ds_read_b64_tr_b16 a[172:173], v26 offset:36032\n"
"ds_read_b64_tr_b16 a[174:175], v26 offset:37312\n"
"buffer_store_dwordx4 v[144:147], v44, s[40:43], s13 offen offset:0 sc1\n"
"v_cvt_pk_bf16_f32 v148, v152, v153\n"
"v_cvt_pk_bf16_f32 v149, v154, v155\n"
"v_mfma_f32_32x32x16_bf16 v[160:175], a[108:111], a[188:191], v[160:175]\n"
"s_waitcnt lgkmcnt(4)\n"
"ds_read_b128 a[176:179], v14 offset:0\n"
"v_cvt_pk_bf16_f32 v150, v156, v157\n"
"v_cvt_pk_bf16_f32 v151, v158, v159\n"
"v_mfma_f32_32x32x16_bf16 v[160:175], a[112:115], a[160:163], v[160:175]\n"
"ds_read_b128 a[180:183], v15 offset:0\n"
"v_mfma_f32_32x32x16_bf16 v[160:175], a[116:119], a[164:167], v[160:175]\n"
"s_waitcnt lgkmcnt(2)\n"
"ds_read_b128 a[184:187], v16 offset:0\n"
"buffer_store_dwordx4 v[148:151], v44, s[40:43], s13 offen offset:1024 sc1\n"
"v_mfma_f32_32x32x16_bf16 v[160:175], a[120:123], a[168:171], v[160:175]\n"
"ds_read_b128 a[188:191], v17 offset:0\n"
"v_mfma_f32_32x32x16_bf16 v[160:175], a[124:127], a[172:175], v[160:175]\n"
"s_waitcnt lgkmcnt(2)\n"
"ds_read_b64_tr_b16 a[160:161], v27 offset:40960\n"
"ds_read_b64_tr_b16 a[162:163], v27 offset:42240\n"
"v_mfma_f32_32x32x16_bf16 v[112:127], a[32:35], a[176:179], v[112:127]\n"
"ds_read_b64_tr_b16 a[164:165], v27 offset:46080\n"
"ds_read_b64_tr_b16 a[166:167], v27 offset:47360\n"
"v_mfma_f32_32x32x16_bf16 v[112:127], a[36:39], a[180:183], v[112:127]\n"
"s_waitcnt lgkmcnt(4)\n"
"ds_read_b64_tr_b16 a[168:169], v27 offset:51200\n"
"ds_read_b64_tr_b16 a[170:171], v27 offset:52480\n"
"s_nop 1\n"
"v_cvt_pk_bf16_f32 v160, v160, v161\n"
"v_cvt_pk_bf16_f32 v161, v162, v163\n"
"v_cvt_pk_bf16_f32 v162, v164, v165\n"
"v_cvt_pk_bf16_f32 v163, v166, v167\n"
"v_mfma_f32_32x32x16_bf16 v[112:127], a[40:43], a[184:187], v[112:127]\n"
"ds_read_b64_tr_b16 a[172:173], v27 offset:56320\n"
"ds_read_b64_tr_b16 a[174:175], v27 offset:57600\n"
"buffer_store_dwordx4 v[160:163], v44, s[40:43], s14 offen offset:0 sc1\n"
"v_cvt_pk_bf16_f32 v164, v168, v169\n"
"v_cvt_pk_bf16_f32 v165, v170, v171\n"
"v_mfma_f32_32x32x16_bf16 v[112:127], a[44:47], a[188:191], v[112:127]\n"
"s_waitcnt lgkmcnt(4)\n"
"ds_read_b64_tr_b16 a[176:177], v27 offset:20480\n"
"ds_read_b64_tr_b16 a[178:179], v27 offset:21760\n"
"v_cvt_pk_bf16_f32 v166, v172, v173\n"
"v_cvt_pk_bf16_f32 v167, v174, v175\n"
"v_mfma_f32_32x32x16_bf16 v[112:127], a[48:51], a[160:163], v[112:127]\n"
"ds_read_b64_tr_b16 a[180:181], v27 offset:25600\n"
"ds_read_b64_tr_b16 a[182:183], v27 offset:26880\n"
"v_mfma_f32_32x32x16_bf16 v[112:127], a[52:55], a[164:167], v[112:127]\n"
"s_waitcnt lgkmcnt(4)\n"
"ds_read_b64_tr_b16 a[184:185], v27 offset:30720\n"
"ds_read_b64_tr_b16 a[186:187], v27 offset:32000\n"
"buffer_store_dwordx4 v[164:167], v44, s[40:43], s14 offen offset:1024 sc1\n"
"v_mfma_f32_32x32x16_bf16 v[112:127], a[56:59], a[168:171], v[112:127]\n"
"ds_read_b64_tr_b16 a[188:189], v27 offset:35840\n"
"ds_read_b64_tr_b16 a[190:191], v27 offset:37120\n"
"v_mfma_f32_32x32x16_bf16 v[112:127], a[60:63], a[172:175], v[112:127]\n"
"s_waitcnt lgkmcnt(4)\n"
"ds_read_b64_tr_b16 a[160:161], v80 offset:0\n"
"ds_read_b64_tr_b16 a[162:163], v80 offset:1280\n"
"v_mfma_f32_32x32x16_bf16 v[112:127], a[64:67], a[176:179], v[112:127]\n"
"ds_read_b64_tr_b16 a[164:165], v80 offset:5120\n"
"ds_read_b64_tr_b16 a[166:167], v80 offset:6400\n"
"v_mfma_f32_32x32x16_bf16 v[112:127], a[68:71], a[180:183], v[112:127]\n"
"s_waitcnt lgkmcnt(4)\n"
"ds_read_b64_tr_b16 a[168:169], v80 offset:10240\n"
"ds_read_b64_tr_b16 a[170:171], v80 offset:11520\n"
"v_mfma_f32_32x32x16_bf16 v[112:127], a[72:75], a[184:187], v[112:127]\n"
"ds_read_b64_tr_b16 a[172:173], v80 offset:15360\n"
"ds_read_b64_tr_b16 a[174:175], v80 offset:16640\n"
"v_mfma_f32_32x32x16_bf16 v[112:127], a[76:79], a[188:191], v[112:127]\n"
"s_waitcnt lgkmcnt(4)\n"
"ds_read_b64_tr_b16 a[176:177], v26 offset:0\n"
"ds_read_b64_tr_b16 a[178:179], v26 offset:1280\n"
"v_mfma_f32_32x32x16_bf16 v[112:127], a[80:83], a[160:163], v[112:127]\n"
"ds_read_b64_tr_b16 a[180:181], v26 offset:5120\n"
"ds_read_b64_tr_b16 a[182:183], v26 offset:6400\n"
"v_mfma_f32_32x32x16_bf16 v[112:127], a[84:87], a[164:167], v[112:127]\n"
"s_waitcnt lgkmcnt(4)\n"
"ds_read_b64_tr_b16 a[184:185], v26 offset:10240\n"
"ds_read_b64_tr_b16 a[186:187], v26 offset:11520\n"
"v_mfma_f32_32x32x16_bf16 v[112:127], a[88:91], a[168:171], v[112:127]\n"
"ds_read_b64_tr_b16 a[188:189], v26 offset:15360\n"
"ds_read_b64_tr_b16 a[190:191], v26 offset:16640\n"
"v_mfma_f32_32x32x16_bf16 v[112:127], a[92:95], a[172:175], v[112:127]\n"
"s_waitcnt lgkmcnt(4)\n"
"ds_read_b64_tr_b16 a[160:161], v26 offset:20480\n"
"ds_read_b64_tr_b16 a[162:163], v26 offset:21760\n"
"v_mfma_f32_32x32x16_bf16 v[112:127], a[96:99], a[176:179], v[112:127]\n"
"ds_read_b64_tr_b16 a[164:165], v26 offset:25600\n"
"ds_read_b64_tr_b16 a[166:167], v26 offset:26880\n"
"v_mfma_f32_32x32x16_bf16 v[112:127], a[100:103], a[180:183], v[112:127]\n"
"s_waitcnt lgkmcnt(4)\n"
"ds_read_b64_tr_b16 a[168:169], v26 offset:30720\n"
"ds_read_b64_tr_b16 a[170:171], v26 offset:32000\n"
"v_mfma_f32_32x32x16_bf16 v[112:127], a[104:107], a[184:187], v[112:127]\n"
"ds_read_b64_tr_b16 a[172:173], v26 offset:35840\n"
"ds_read_b64_tr_b16 a[174:175], v26 offset:37120\n"
"s_waitcnt vmcnt(0)\n"
"buffer_store_dword v7, v4, s[40:43], s24 offen sc1\n"
"v_mfma_f32_32x32x16_bf16 v[112:127], a[108:111], a[188:191], v[112:127]\n"
"s_waitcnt lgkmcnt(4)\n"
"v_mfma_f32_32x32x16_bf16 v[112:127], a[112:115], a[160:163], v[112:127]\n"
"v_mfma_f32_32x32x16_bf16 v[112:127], a[116:119], a[164:167], v[112:127]\n"
"s_waitcnt lgkmcnt(0)\n"
"v_mfma_f32_32x32x16_bf16 v[112:127], a[120:123], a[168:171], v[112:127]\n"
"v_mfma_f32_32x32x16_bf16 v[112:127], a[124:127], a[172:175], v[112:127]\n"
"v_mfma_f32_32x32x16_bf16 v[112:127], a[240:243], v[50:53], v[112:127]\n"
"v_mfma_f32_32x32x16_bf16 v[112:127], a[244:247], v[54:57], v[112:127]\n"
"v_mfma_f32_32x32x16_bf16 v[112:127], v[224:227], v[58:61], v[112:127]\n"
"v_mfma_f32_32x32x16_bf16 v[112:127], v[228:231], v[62:65], v[112:127]\n"
"s_waitcnt vmcnt(0)\n"
"v_mov_b32 v3, 0\n"
"buffer_atomic_add v3, v1, s[40:43], s24 offen offset:0 sc0 sc1\n"
"s_waitcnt vmcnt(0)\n"
"v_cmp_ne_u32 s[0:1], s56, v3\n"
"s_nop 0\n"
"s_and_b32 s0, s0, s7\n"
"s_cbranch_scc1 LppB_slow%=\n"
"LppB_ok%=:\n"
"s_cmp_lg_u32 s52, s54\n"
"s_cbranch_scc1 LrcvB%=\n"
"buffer_load_dwordx4 v[152:155], v44, s[40:43], s19 offen offset:0 sc1\n"
"buffer_load_dwordx4 v[156:159], v44, s[40:43], s19 offen offset:1024 sc1\n"
"buffer_load_dwordx4 v[160:163], v44, s[40:43], s20 offen offset:0 sc1\n"
"buffer_load_dwordx4 v[164:167], v44, s[40:43], s20 offen offset:1024 sc1\n"
"buffer_load_dwordx4 v[168:171], v44, s[40:43], s21 offen offset:0 sc1\n"
"buffer_load_dwordx4 v[172:175], v44, s[40:43], s21 offen offset:1024 sc1\n"
"LrcvB%=:\n"
"buffer_load_dwordx4 v[128:131], v44, s[40:43], s16 offen offset:0 sc1\n"
"buffer_load_dwordx4 v[132:135], v44, s[40:43], s16 offen offset:1024 sc1\n"
"buffer_load_dwordx4 v[136:139], v44, s[40:43], s17 offen offset:0 sc1\n"
"buffer_load_dwordx4 v[140:143], v44, s[40:43], s17 offen offset:1024 sc1\n"
"buffer_load_dwordx4 v[144:147], v44, s[40:43], s18 offen offset:0 sc1\n"
"buffer_load_dwordx4 v[148:151], v44, s[40:43], s18 offen offset:1024 sc1\n"
"s_lshl_b32 s60, s98, 2\n"
"s_add_u32 s60, s60, s52\n"
"s_mov_b32 s61, 0x1708000\n"
"s_cmp_eq_u32 s60, 240\n"
"s_cselect_b32 s60, s61, 0x70000000\n"
"buffer_store_dword v7, v4, s[40:43], s60 offen sc1\n"
"s_waitcnt vmcnt(6)\n"
"v_dot2c_f32_bf16 v112, s26, v128\n"
"v_dot2c_f32_bf16 v113, s27, v128\n"
"v_dot2c_f32_bf16 v114, s26, v129\n"
"v_dot2c_f32_bf16 v115, s27, v129\n"
"v_dot2c_f32_bf16 v116, s26, v130\n"
"v_dot2c_f32_bf16 v117, s27, v130\n"
"v_dot2c_f32_bf16 v118, s26, v131\n"
"v_dot2c_f32_bf16 v119, s27, v131\n"
"s_waitcnt vmcnt(5)\n"
"v_dot2c_f32_bf16 v120, s26, v132\n"
"v_dot2c_f32_bf16 v121, s27, v132\n"
"v_dot2c_f32_bf16 v122, s26, v133\n"
"v_dot2c_f32_bf16 v123, s27, v133\n"
"v_dot2c_f32_bf16 v124, s26, v134\n"
"v_dot2c_f32_bf16 v125, s27, v134\n"
"v_dot2c_f32_bf16 v126, s26, v135\n"
"v_dot2c_f32_bf16 v127, s27, v135\n"
"s_waitcnt vmcnt(4)\n"
"v_dot2c_f32_bf16 v112, s26, v136\n"
"v_dot2c_f32_bf16 v113, s27, v136\n"
"v_dot2c_f32_bf16 v114, s26, v137\n"
"v_dot2c_f32_bf16 v115, s27, v137\n"
"v_dot2c_f32_bf16 v116, s26, v138\n"
"v_dot2c_f32_bf16 v117, s27, v138\n"
"v_dot2c_f32_bf16 v118, s26, v139\n"
"v_dot2c_f32_bf16 v119, s27, v139\n"
"s_waitcnt vmcnt(3)\n"
"v_dot2c_f32_bf16 v120, s26, v140\n"
"v_dot2c_f32_bf16 v121, s27, v140\n"
"v_dot2c_f32_bf16 v122, s26, v141\n"
"v_dot2c_f32_bf16 v123, s27, v141\n"
"v_dot2c_f32_bf16 v124, s26, v142\n"
"v_dot2c_f32_bf16 v125, s27, v142\n"
"v_dot2c_f32_bf16 v126, s26, v143\n"
"v_dot2c_f32_bf16 v127, s27, v143\n"
"s_waitcnt vmcnt(2)\n"
"v_dot2c_f32_bf16 v112, s26, v144\n"
"v_dot2c_f32_bf16 v113, s27, v144\n"
"v_dot2c_f32_bf16 v114, s26, v145\n"
"v_dot2c_f32_bf16 v115, s27, v145\n"
"v_dot2c_f32_bf16 v116, s26, v146\n"
"v_dot2c_f32_bf16 v117, s27, v146\n"
"v_dot2c_f32_bf16 v118, s26, v147\n"
"v_dot2c_f32_bf16 v119, s27, v147\n"
"s_waitcnt vmcnt(1)\n"
"v_dot2c_f32_bf16 v120, s26, v148\n"
"v_dot2c_f32_bf16 v121, s27, v148\n"
"v_dot2c_f32_bf16 v122, s26, v149\n"
"v_dot2c_f32_bf16 v123, s27, v149\n"
"v_dot2c_f32_bf16 v124, s26, v150\n"
"v_dot2c_f32_bf16 v125, s27, v150\n"
"v_dot2c_f32_bf16 v126, s26, v151\n"
"v_dot2c_f32_bf16 v127, s27, v151\n"
"s_add_u32 s62, s22, 0\n"
"buffer_store_dword v112, v95, s[44:47], s62 offen offset:0 sc1\n"
"buffer_store_dword v113, v95, s[44:47], s62 offen offset:2560 sc1\n"
"s_add_u32 s62, s22, 5120\n"
"buffer_store_dword v114, v95, s[44:47], s62 offen offset:0 sc1\n"
"buffer_store_dword v115, v95, s[44:47], s62 offen offset:2560 sc1\n"
"s_add_u32 s62, s22, 10240\n"
"buffer_store_dword v116, v95, s[44:47], s62 offen offset:0 sc1\n"
"buffer_store_dword v117, v95, s[44:47], s62 offen offset:2560 sc1\n"
"s_add_u32 s62, s22, 15360\n"
"buffer_store_dword v118, v95, s[44:47], s62 offen offset:0 sc1\n"
"buffer_store_dword v119, v95, s[44:47], s62 offen offset:2560 sc1\n"
"s_add_u32 s62, s22, 40960\n"
"buffer_store_dword v120, v95, s[44:47], s62 offen offset:0 sc1\n"
"buffer_store_dword v121, v95, s[44:47], s62 offen offset:2560 sc1\n"
"s_add_u32 s62, s22, 46080\n"
"buffer_store_dword v122, v95, s[44:47], s62 offen offset:0 sc1\n"
"buffer_store_dword v123, v95, s[44:47], s62 offen offset:2560 sc1\n"
"s_add_u32 s62, s22, 51200\n"
"buffer_store_dword v124, v95, s[44:47], s62 offen offset:0 sc1\n"
"buffer_store_dword v125, v95, s[44:47], s62 offen offset:2560 sc1\n"
"s_add_u32 s62, s22, 56320\n"
"buffer_store_dword v126, v95, s[44:47], s62 offen offset:0 sc1\n"
"buffer_store_dword v127, v95, s[44:47], s62 offen offset:2560 sc1\n"
"s_cmp_lg_u32 s52, s54\n"
"s_cbranch_scc1 LfinB%=\n"
"v_dot2c_f32_bf16 v176, s26, v152\n"
"v_dot2c_f32_bf16 v177, s27, v152\n"
"v_dot2c_f32_bf16 v178, s26, v153\n"
"v_dot2c_f32_bf16 v179, s27, v153\n"
"v_dot2c_f32_bf16 v180, s26, v154\n"
"v_dot2c_f32_bf16 v181, s27, v154\n"
"v_dot2c_f32_bf16 v182, s26, v155\n"
"v_dot2c_f32_bf16 v183, s27, v155\n"
"v_dot2c_f32_bf16 v184, s26, v156\n"
"v_dot2c_f32_bf16 v185, s27, v156\n"
"v_dot2c_f32_bf16 v186, s26, v157\n"
"v_dot2c_f32_bf16 v187, s27, v157\n"
"v_dot2c_f32_bf16 v188, s26, v158\n"
"v_dot2c_f32_bf16 v189, s27, v158\n"
"v_dot2c_f32_bf16 v190, s26, v159\n"
"v_dot2c_f32_bf16 v191, s27, v159\n"
"v_dot2c_f32_bf16 v176, s26, v160\n"
"v_dot2c_f32_bf16 v177, s27, v160\n"
"v_dot2c_f32_bf16 v178, s26, v161\n"
"v_dot2c_f32_bf16 v179, s27, v161\n"
"v_dot2c_f32_bf16 v180, s26, v162\n"
"v_dot2c_f32_bf16 v181, s27, v162\n"
"v_dot2c_f32_bf16 v182, s26, v163\n"
"v_dot2c_f32_bf16 v183, s27, v163\n"
"v_dot2c_f32_bf16 v184, s26, v164\n"
"v_dot2c_f32_bf16 v185, s27, v164\n"
"v_dot2c_f32_bf16 v186, s26, v165\n"
"v_dot2c_f32_bf16 v187, s27, v165\n"
"v_dot2c_f32_bf16 v188, s26, v166\n"
"v_dot2c_f32_bf16 v189, s27, v166\n"
"v_dot2c_f32_bf16 v190, s26, v167\n"
"v_dot2c_f32_bf16 v191, s27, v167\n"
"v_dot2c_f32_bf16 v176, s26, v168\n"
"v_dot2c_f32_bf16 v177, s27, v168\n"
"v_dot2c_f32_bf16 v178, s26, v169\n"
"v_dot2c_f32_bf16 v179, s27, v169\n"
"v_dot2c_f32_bf16 v180, s26, v170\n"
"v_dot2c_f32_bf16 v181, s27, v170\n"
"v_dot2c_f32_bf16 v182, s26, v171\n"
"v_dot2c_f32_bf16 v183, s27, v171\n"
"v_dot2c_f32_bf16 v184, s26, v172\n"
"v_dot2c_f32_bf16 v185, s27, v172\n"
"v_dot2c_f32_bf16 v186, s26, v173\n"
"v_dot2c_f32_bf16 v187, s27, v173\n"
"v_dot2c_f32_bf16 v188, s26, v174\n"
"v_dot2c_f32_bf16 v189, s27, v174\n"
"v_dot2c_f32_bf16 v190, s26, v175\n"
"v_dot2c_f32_bf16 v191, s27, v175\n"
"s_add_u32 s62, s23, 0\n"
"buffer_store_dword v176, v95, s[44:47], s62 offen offset:0 sc1\n"
"buffer_store_dword v177, v95, s[44:47], s62 offen offset:2560 sc1\n"
"s_add_u32 s62, s23, 5120\n"
"buffer_store_dword v178, v95, s[44:47], s62 offen offset:0 sc1\n"
"buffer_store_dword v179, v95, s[44:47], s62 offen offset:2560 sc1\n"
"s_add_u32 s62, s23, 10240\n"
"buffer_store_dword v180, v95, s[44:47], s62 offen offset:0 sc1\n"
"buffer_store_dword v181, v95, s[44:47], s62 offen offset:2560 sc1\n"
"s_add_u32 s62, s23, 15360\n"
"buffer_store_dword v182, v95, s[44:47], s62 offen offset:0 sc1\n"
"buffer_store_dword v183, v95, s[44:47], s62 offen offset:2560 sc1\n"
"s_add_u32 s62, s23, 40960\n"
"buffer_store_dword v184, v95, s[44:47], s62 offen offset:0 sc1\n"
"buffer_store_dword v185, v95, s[44:47], s62 offen offset:2560 sc1\n"
"s_add_u32 s62, s23, 46080\n"
"buffer_store_dword v186, v95, s[44:47], s62 offen offset:0 sc1\n"
"buffer_store_dword v187, v95, s[44:47], s62 offen offset:2560 sc1\n"
"s_add_u32 s62, s23, 51200\n"
"buffer_store_dword v188, v95, s[44:47], s62 offen offset:0 sc1\n"
"buffer_store_dword v189, v95, s[44:47], s62 offen offset:2560 sc1\n"
"s_add_u32 s62, s23, 56320\n"
"buffer_store_dword v190, v95, s[44:47], s62 offen offset:0 sc1\n"
"buffer_store_dword v191, v95, s[44:47], s62 offen offset:2560 sc1\n"
"LfinB%=:\n"
"s_branch Lend%=\n"
"LstA_slow%=:\n"
"s_mov_b32 s99, 0\n"
"LstA_loop%=:\n"
"s_sleep 4\n"
"s_add_u32 s99, s99, 1\n"
"s_cmp_gt_u32 s99, 400000\n"
"s_cbranch_scc1 LstA_ok%=\n"
"buffer_load_dwordx2 v[58:59], v60, s[40:43], s7 offen sc1\n"
"s_waitcnt vmcnt(0)\n"
"v_cmp_ne_u32 s[0:1], s56, v59\n"
"s_nop 0\n"
"s_cmp_lg_u64 s[0:1], 0\n"
"s_cbranch_scc1 LstA_loop%=\n"
"s_branch LstA_ok%=\n"
"Lcs0A_slow%=:\n"
"s_mov_b32 s99, 0\n"
"Lcs0A_loop%=:\n"
"s_sleep 4\n"
"s_add_u32 s99, s99, 1\n"
"s_cmp_gt_u32 s99, 400000\n"
"s_cbranch_scc1 Lcs0A_ok%=\n"
"buffer_load_dwordx4 a[224:227], v44, s[40:43], s3 offen offset:0 sc1\n"
"buffer_load_dwordx4 a[228:231], v44, s[40:43], s3 offen offset:1024 sc1\n"
"buffer_load_dwordx4 a[232:235], v44, s[40:43], s3 offen offset:2048 sc1\n"
"buffer_load_dwordx4 a[236:239], v44, s[40:43], s3 offen offset:3072 sc1\n"
"buffer_load_dwordx4 a[240:243], v44, s[40:43], s2 offen offset:0 sc1\n"
"s_waitcnt vmcnt(4)\n"
"v_accvgpr_read_b32 v77, a225\n"
"v_accvgpr_read_b32 v78, a227\n"
"v_xor_b32 v77, s56, v77\n"
"v_xor_b32 v78, s56, v78\n"
"v_or_b32 v76, v77, v78\n"
"s_waitcnt vmcnt(3)\n"
"v_accvgpr_read_b32 v77, a229\n"
"v_accvgpr_read_b32 v78, a231\n"
"v_xor_b32 v77, s56, v77\n"
"v_xor_b32 v78, s56, v78\n"
"v_or3_b32 v76, v76, v77, v78\n"
"s_waitcnt vmcnt(2)\n"
"v_accvgpr_read_b32 v77, a233\n"
"v_accvgpr_read_b32 v78, a235\n"
"v_xor_b32 v77, s56, v77\n"
"v_xor_b32 v78, s56, v78\n"
"v_or3_b32 v76, v76, v77, v78\n"
"s_waitcnt vmcnt(1)\n"
"v_accvgpr_read_b32 v77, a237\n"
"v_accvgpr_read_b32 v78, a239\n"
"v_xor_b32 v77, s56, v77\n"
"v_xor_b32 v78, s56, v78\n"
"v_or3_b32 v76, v76, v77, v78\n"
"s_waitcnt vmcnt(0)\n"
"v_accvgpr_read_b32 v77, a241\n"
"v_accvgpr_read_b32 v78, a243\n"
"v_xor_b32 v77, s56, v77\n"
"v_xor_b32 v78, s56, v78\n"
"v_or3_b32 v76, v76, v77, v78\n"
"v_cmp_ne_u32 s[0:1], 0, v76\n"
"s_nop 0\n"
"s_cmp_lg_u64 s[0:1], 0\n"
"s_cbranch_scc1 Lcs0A_loop%=\n"
"s_branch Lcs0A_ok%=\n"
"Lcs1A_slow%=:\n"
"s_mov_b32 s99, 0\n"
"Lcs1A_loop%=:\n"
"s_sleep 4\n"
"s_add_u32 s99, s99, 1\n"
"s_cmp_gt_u32 s99, 400000\n"
"s_cbranch_scc1 Lcs1A_ok%=\n"
"buffer_load_dwordx4 a[224:227], v44, s[40:43], s3 offen offset:0 sc1\n"
"buffer_load_dwordx4 a[228:231], v44, s[40:43], s3 offen offset:1024 sc1\n"
"buffer_load_dwordx4 a[232:235], v44, s[40:43], s3 offen offset:2048 sc1\n"
"buffer_load_dwordx4 a[236:239], v44, s[40:43], s3 offen offset:3072 sc1\n"
"s_waitcnt vmcnt(3)\n"
"v_accvgpr_read_b32 v77, a225\n"
"v_accvgpr_read_b32 v78, a227\n"
"v_xor_b32 v77, s56, v77\n"
"v_xor_b32 v78, s56, v78\n"
"v_or_b32 v76, v77, v78\n"
"s_waitcnt vmcnt(2)\n"
"v_accvgpr_read_b32 v77, a229\n"
"v_accvgpr_read_b32 v78, a231\n"
"v_xor_b32 v77, s56, v77\n"
"v_xor_b32 v78, s56, v78\n"
"v_or3_b32 v76, v76, v77, v78\n"
"s_waitcnt vmcnt(1)\n"
"v_accvgpr_read_b32 v77, a233\n"
"v_accvgpr_read_b32 v78, a235\n"
"v_xor_b32 v77, s56, v77\n"
"v_xor_b32 v78, s56, v78\n"
"v_or3_b32 v76, v76, v77, v78\n"
"s_waitcnt vmcnt(0)\n"
"v_accvgpr_read_b32 v77, a237\n"
"v_accvgpr_read_b32 v78, a239\n"
"v_xor_b32 v77, s56, v77\n"
"v_xor_b32 v78, s56, v78\n"
"v_or3_b32 v76, v76, v77, v78\n"
"v_cmp_ne_u32 s[0:1], 0, v76\n"
"s_nop 0\n"
"s_cmp_lg_u64 s[0:1], 0\n"
"s_cbranch_scc1 Lcs1A_loop%=\n"
"s_branch Lcs1A_ok%=\n"
"LaggA_slow%=:\n"
"s_mov_b32 s99, 0\n"
"LaggA_loop%=:\n"
"s_sleep 4\n"
"s_add_u32 s99, s99, 1\n"
"s_cmp_gt_u32 s99, 400000\n"
"s_cbranch_scc1 LaggA_ok%=\n"
"buffer_load_dwordx4 v[90:93], v58, s[40:43], s61 offen sc1\n"
"buffer_load_dwordx4 v[94:97], v59, s[40:43], s61 offen sc1\n"
"buffer_load_dwordx4 v[98:101], v60, s[40:43], s61 offen sc1\n"
"buffer_load_dwordx4 v[102:105], v61, s[40:43], s61 offen sc1\n"
"s_waitcnt vmcnt(3)\n"
"v_xor_b32 v91, v62, v91\n"
"v_xor_b32 v93, v62, v93\n"
"s_waitcnt vmcnt(2)\n"
"v_xor_b32 v95, v63, v95\n"
"v_xor_b32 v97, v63, v97\n"
"s_waitcnt vmcnt(1)\n"
"v_xor_b32 v99, v64, v99\n"
"v_xor_b32 v101, v64, v101\n"
"s_waitcnt vmcnt(0)\n"
"v_xor_b32 v103, v65, v103\n"
"v_xor_b32 v105, v65, v105\n"
"v_or3_b32 v91, v91, v93, v95\n"
"v_or3_b32 v97, v97, v99, v101\n"
"v_or3_b32 v91, v91, v103, v105\n"
"v_or_b32 v91, v91, v97\n"
"v_cmp_ne_u32 vcc, 0, v91\n"
"s_nop 0\n"
"s_cbranch_vccnz LaggA_loop%=\n"
"s_branch LaggA_ok%=\n"
"Lh1A_slow%=:\n"
"s_mov_b32 s99, 0\n"
"Lh1A_loop%=:\n"
"s_sleep 4\n"
"s_add_u32 s99, s99, 1\n"
"s_cmp_gt_u32 s99, 400000\n"
"s_cbranch_scc1 Lh1A_ok%=\n"
"v_mov_b32 v3, 0\n"
"buffer_atomic_add v3, v1, s[40:43], s4 offen offset:128 sc0 sc1\n"
"s_waitcnt vmcnt(0)\n"
"v_cmp_ne_u32 s[0:1], s56, v3\n"
"s_nop 0\n"
"s_and_b32 s0, s0, s7\n"
"s_cbranch_scc1 Lh1A_loop%=\n"
"s_branch Lh1A_ok%=\n"
"Lh0A_slow%=:\n"
"s_mov_b32 s99, 0\n"
"Lh0A_loop%=:\n"
"s_sleep 4\n"
"s_add_u32 s99, s99, 1\n"
"s_cmp_gt_u32 s99, 400000\n"
"s_cbranch_scc1 Lh0A_ok%=\n"
"v_mov_b32 v3, 0\n"
"buffer_atomic_add v3, v1, s[40:43], s4 offen offset:0 sc0 sc1\n"
"s_waitcnt vmcnt(0)\n"
"v_cmp_ne_u32 s[0:1], s56, v3\n"
"s_nop 0\n"
"s_and_b32 s0, s0, s7\n"
"s_cbranch_scc1 Lh0A_loop%=\n"
"s_branch Lh0A_ok%=\n"
"Lh2A_slow%=:\n"
"s_mov_b32 s99, 0\n"
"Lh2A_loop%=:\n"
"s_sleep 4\n"
"s_add_u32 s99, s99, 1\n"
"s_cmp_gt_u32 s99, 400000\n"
"s_cbranch_scc1 Lh2A_ok%=\n"
"v_mov_b32 v3, 0\n"
"buffer_atomic_add v3, v1, s[40:43], s4 offen offset:256 sc0 sc1\n"
"s_waitcnt vmcnt(0)\n"
"v_cmp_ne_u32 s[0:1], s56, v3\n"
"s_nop 0\n"
"s_and_b32 s0, s0, s7\n"
"s_cbranch_scc1 Lh2A_loop%=\n"
"s_branch Lh2A_ok%=\n"
"LppA_slow%=:\n"
"s_mov_b32 s99, 0\n"
"LppA_loop%=:\n"
"s_sleep 4\n"
"s_add_u32 s99, s99, 1\n"
"s_cmp_gt_u32 s99, 400000\n"
"s_cbranch_scc1 LppA_ok%=\n"
"v_mov_b32 v3, 0\n"
"buffer_atomic_add v3, v1, s[40:43], s24 offen offset:0 sc0 sc1\n"
"s_waitcnt vmcnt(0)\n"
"v_cmp_ne_u32 s[0:1], s56, v3\n"
"s_nop 0\n"
"s_and_b32 s0, s0, s7\n"
"s_cbranch_scc1 LppA_loop%=\n"
"s_branch LppA_ok%=\n"
"LstB_slow%=:\n"
"s_mov_b32 s99, 0\n"
"LstB_loop%=:\n"
"s_sleep 4\n"
"s_add_u32 s99, s99, 1\n"
"s_cmp_gt_u32 s99, 400000\n"
"s_cbranch_scc1 LstB_ok%=\n"
"buffer_load_dwordx2 v[58:59], v60, s[40:43], s7 offen sc1\n"
"s_waitcnt vmcnt(0)\n"
"v_cmp_ne_u32 s[0:1], s56, v59\n"
"s_nop 0\n"
"s_cmp_lg_u64 s[0:1], 0\n"
"s_cbranch_scc1 LstB_loop%=\n"
"s_branch LstB_ok%=\n"
"Lcs0B_slow%=:\n"
"s_mov_b32 s99, 0\n"
"Lcs0B_loop%=:\n"
"s_sleep 4\n"
"s_add_u32 s99, s99, 1\n"
"s_cmp_gt_u32 s99, 400000\n"
"s_cbranch_scc1 Lcs0B_ok%=\n"
"buffer_load_dwordx4 a[224:227], v44, s[40:43], s3 offen offset:0 sc1\n"
"buffer_load_dwordx4 a[228:231], v44, s[40:43], s3 offen offset:1024 sc1\n"
"buffer_load_dwordx4 a[232:235], v44, s[40:43], s3 offen offset:2048 sc1\n"
"buffer_load_dwordx4 a[236:239], v44, s[40:43], s3 offen offset:3072 sc1\n"
"s_waitcnt vmcnt(3)\n"
"v_accvgpr_read_b32 v83, a225\n"
"v_accvgpr_read_b32 v84, a227\n"
"v_xor_b32 v83, s56, v83\n"
"v_xor_b32 v84, s56, v84\n"
"v_or_b32 v82, v83, v84\n"
"s_waitcnt vmcnt(2)\n"
"v_accvgpr_read_b32 v83, a229\n"
"v_accvgpr_read_b32 v84, a231\n"
"v_xor_b32 v83, s56, v83\n"
"v_xor_b32 v84, s56, v84\n"
"v_or3_b32 v82, v82, v83, v84\n"
"s_waitcnt vmcnt(1)\n"
"v_accvgpr_read_b32 v83, a233\n"
"v_accvgpr_read_b32 v84, a235\n"
"v_xor_b32 v83, s56, v83\n"
"v_xor_b32 v84, s56, v84\n"
"v_or3_b32 v82, v82, v83, v84\n"
"s_waitcnt vmcnt(0)\n"
"v_accvgpr_read_b32 v83, a237\n"
"v_accvgpr_read_b32 v84, a239\n"
"v_xor_b32 v83, s56, v83\n"
"v_xor_b32 v84, s56, v84\n"
"v_or3_b32 v82, v82, v83, v84\n"
"v_cmp_ne_u32 s[0:1], 0, v82\n"
"s_nop 0\n"
"s_cmp_lg_u64 s[0:1], 0\n"
"s_cbranch_scc1 Lcs0B_loop%=\n"
"s_branch Lcs0B_ok%=\n"
"Lcs1B_slow%=:\n"
"s_mov_b32 s99, 0\n"
"Lcs1B_loop%=:\n"
"s_sleep 4\n"
"s_add_u32 s99, s99, 1\n"
"s_cmp_gt_u32 s99, 400000\n"
"s_cbranch_scc1 Lcs1B_ok%=\n"
"buffer_load_dwordx4 a[224:227], v44, s[40:43], s3 offen offset:0 sc1\n"
"buffer_load_dwordx4 a[228:231], v44, s[40:43], s3 offen offset:1024 sc1\n"
"buffer_load_dwordx4 a[232:235], v44, s[40:43], s3 offen offset:2048 sc1\n"
"buffer_load_dwordx4 a[236:239], v44, s[40:43], s3 offen offset:3072 sc1\n"
"s_waitcnt vmcnt(3)\n"
"v_accvgpr_read_b32 v83, a225\n"
"v_accvgpr_read_b32 v84, a227\n"
"v_xor_b32 v83, s56, v83\n"
"v_xor_b32 v84, s56, v84\n"
"v_or_b32 v82, v83, v84\n"
"s_waitcnt vmcnt(2)\n"
"v_accvgpr_read_b32 v83, a229\n"
"v_accvgpr_read_b32 v84, a231\n"
"v_xor_b32 v83, s56, v83\n"
"v_xor_b32 v84, s56, v84\n"
"v_or3_b32 v82, v82, v83, v84\n"
"s_waitcnt vmcnt(1)\n"
"v_accvgpr_read_b32 v83, a233\n"
"v_accvgpr_read_b32 v84, a235\n"
"v_xor_b32 v83, s56, v83\n"
"v_xor_b32 v84, s56, v84\n"
"v_or3_b32 v82, v82, v83, v84\n"
"s_waitcnt vmcnt(0)\n"
"v_accvgpr_read_b32 v83, a237\n"
"v_accvgpr_read_b32 v84, a239\n"
"v_xor_b32 v83, s56, v83\n"
"v_xor_b32 v84, s56, v84\n"
"v_or3_b32 v82, v82, v83, v84\n"
"v_cmp_ne_u32 s[0:1], 0, v82\n"
"s_nop 0\n"
"s_cmp_lg_u64 s[0:1], 0\n"
"s_cbranch_scc1 Lcs1B_loop%=\n"
"s_branch Lcs1B_ok%=\n"
"LaggB_slow%=:\n"
"s_mov_b32 s99, 0\n"
"LaggB_loop%=:\n"
"s_sleep 4\n"
"s_add_u32 s99, s99, 1\n"
"s_cmp_gt_u32 s99, 400000\n"
"s_cbranch_scc1 LaggB_ok%=\n"
"buffer_load_dwordx4 v[90:93], v58, s[40:43], s61 offen sc1\n"
"buffer_load_dwordx4 v[94:97], v59, s[40:43], s61 offen sc1\n"
"buffer_load_dwordx4 v[98:101], v60, s[40:43], s61 offen sc1\n"
"buffer_load_dwordx4 v[102:105], v61, s[40:43], s61 offen sc1\n"
"s_waitcnt vmcnt(3)\n"
"v_xor_b32 v91, v62, v91\n"
"v_xor_b32 v93, v62, v93\n"
"s_waitcnt vmcnt(2)\n"
"v_xor_b32 v95, v63, v95\n"
"v_xor_b32 v97, v63, v97\n"
"s_waitcnt vmcnt(1)\n"
"v_xor_b32 v99, v64, v99\n"
"v_xor_b32 v101, v64, v101\n"
"s_waitcnt vmcnt(0)\n"
"v_xor_b32 v103, v65, v103\n"
"v_xor_b32 v105, v65, v105\n"
"v_or3_b32 v91, v91, v93, v95\n"
"v_or3_b32 v97, v97, v99, v101\n"
"v_or3_b32 v91, v91, v103, v105\n"
"v_or_b32 v91, v91, v97\n"
"v_cmp_ne_u32 vcc, 0, v91\n"
"s_nop 0\n"
"s_cbranch_vccnz LaggB_loop%=\n"
"s_branch LaggB_ok%=\n"
"Lh1B_slow%=:\n"
"s_mov_b32 s99, 0\n"
"Lh1B_loop%=:\n"
"s_sleep 4\n"
"s_add_u32 s99, s99, 1\n"
"s_cmp_gt_u32 s99, 400000\n"
"s_cbranch_scc1 Lh1B_ok%=\n"
"v_mov_b32 v3, 0\n"
"buffer_atomic_add v3, v1, s[40:43], s4 offen offset:128 sc0 sc1\n"
"s_waitcnt vmcnt(0)\n"
"v_cmp_ne_u32 s[0:1], s56, v3\n"
"s_nop 0\n"
"s_and_b32 s0, s0, s7\n"
"s_cbranch_scc1 Lh1B_loop%=\n"
"s_branch Lh1B_ok%=\n"
"Lh0B_slow%=:\n"
"s_mov_b32 s99, 0\n"
"Lh0B_loop%=:\n"
"s_sleep 4\n"
"s_add_u32 s99, s99, 1\n"
"s_cmp_gt_u32 s99, 400000\n"
"s_cbranch_scc1 Lh0B_ok%=\n"
"v_mov_b32 v3, 0\n"
"buffer_atomic_add v3, v1, s[40:43], s4 offen offset:0 sc0 sc1\n"
"s_waitcnt vmcnt(0)\n"
"v_cmp_ne_u32 s[0:1], s56, v3\n"
"s_nop 0\n"
"s_and_b32 s0, s0, s7\n"
"s_cbranch_scc1 Lh0B_loop%=\n"
"s_branch Lh0B_ok%=\n"
"Lh2B_slow%=:\n"
"s_mov_b32 s99, 0\n"
"Lh2B_loop%=:\n"
"s_sleep 4\n"
"s_add_u32 s99, s99, 1\n"
"s_cmp_gt_u32 s99, 400000\n"
"s_cbranch_scc1 Lh2B_ok%=\n"
"v_mov_b32 v3, 0\n"
"buffer_atomic_add v3, v1, s[40:43], s4 offen offset:256 sc0 sc1\n"
"s_waitcnt vmcnt(0)\n"
"v_cmp_ne_u32 s[0:1], s56, v3\n"
"s_nop 0\n"
"s_and_b32 s0, s0, s7\n"
"s_cbranch_scc1 Lh2B_loop%=\n"
"s_branch Lh2B_ok%=\n"
"LppB_slow%=:\n"
"s_mov_b32 s99, 0\n"
"LppB_loop%=:\n"
"s_sleep 4\n"
"s_add_u32 s99, s99, 1\n"
"s_cmp_gt_u32 s99, 400000\n"
"s_cbranch_scc1 LppB_ok%=\n"
"v_mov_b32 v3, 0\n"
"buffer_atomic_add v3, v1, s[40:43], s24 offen offset:0 sc0 sc1\n"
"s_waitcnt vmcnt(0)\n"
"v_cmp_ne_u32 s[0:1], s56, v3\n"
"s_nop 0\n"
"s_and_b32 s0, s0, s7\n"
"s_cbranch_scc1 LppB_loop%=\n"
"s_branch LppB_ok%=\n"
"Lend%=:\n"
  :: "s"(__builtin_amdgcn_kernarg_segment_ptr()), "s"(blockIdx.x), "v"(threadIdx.x), "s"(lb) : "v1","v2","v3","v4","v5","v6","v7","v8","v9","v10","v11","v12","v13","v14","v15","v16","v17","v18","v19","v20","v21","v22","v23","v24","v25","v26","v27","v28","v29","v30","v31","v32","v33","v34","v35","v36","v37","v38","v39","v40","v41","v42","v43","v44","v45","v46","v47","v48","v49","v50","v51","v52","v53","v54","v55","v56","v57","v58","v59","v60","v61","v62","v63","v64","v65","v66","v67","v68","v69","v70","v71","v72","v73","v74","v75","v76","v77","v78","v79","v80","v81","v82","v83","v84","v85","v86","v87","v88","v89","v90","v91","v92","v93","v94","v95","v96","v97","v98","v99","v100","v101","v102","v103","v104","v105","v106","v107","v108","v109","v110","v111","v112","v113","v114","v115","v116","v117","v118","v119","v120","v121","v122","v123","v124","v125","v126","v127","v128","v129","v130","v131","v132","v133","v134","v135","v136","v137","v138","v139","v140","v141","v142","v143","v144","v145","v146","v147","v148","v149","v150","v151","v152","v153","v154","v155","v156","v157","v158","v159","v160","v161","v162","v163","v164","v165","v166","v167","v168","v169","v170","v171","v172","v173","v174","v175","v176","v177","v178","v179","v180","v181","v182","v183","v184","v185","v186","v187","v188","v189","v190","v191","v192","v193","v194","v195","v196","v197","v198","v199","v200","v201","v202","v203","v204","v205","v206","v207","v208","v209","v210","v211","v212","v213","v214","v215","v216","v217","v218","v219","v220","v221","v222","v223","v224","v225","v226","v227","v228","v229","v230","v231","v232","v233","v234","v235","v236","v237","v238","v239","v240","v241","v242","v243","v244","v245","v246","v247","v248","v249","v250","v251","v252","v253","v254","v255","a0","a1","a2","a3","a4","a5","a6","a7","a8","a9","a10","a11","a12","a13","a14","a15","a16","a17","a18","a19","a20","a21","a22","a23","a24","a25","a26","a27","a28","a29","a30","a31","a32","a33","a34","a35","a36","a37","a38","a39","a40","a41","a42","a43","a44","a45","a46","a47","a48","a49","a50","a51","a52","a53","a54","a55","a56","a57","a58","a59","a60","a61","a62","a63","a64","a65","a66","a67","a68","a69","a70","a71","a72","a73","a74","a75","a76","a77","a78","a79","a80","a81","a82","a83","a84","a85","a86","a87","a88","a89","a90","a91","a92","a93","a94","a95","a96","a97","a98","a99","a100","a101","a102","a103","a104","a105","a106","a107","a108","a109","a110","a111","a112","a113","a114","a115","a116","a117","a118","a119","a120","a121","a122","a123","a124","a125","a126","a127","a128","a129","a130","a131","a132","a133","a134","a135","a136","a137","a138","a139","a140","a141","a142","a143","a144","a145","a146","a147","a148","a149","a150","a151","a152","a153","a154","a155","a156","a157","a158","a159","a160","a161","a162","a163","a164","a165","a166","a167","a168","a169","a170","a171","a172","a173","a174","a175","a176","a177","a178","a179","a180","a181","a182","a183","a184","a185","a186","a187","a188","a189","a190","a191","a192","a193","a194","a195","a196","a197","a198","a199","a200","a201","a202","a203","a204","a205","a206","a207","a208","a209","a210","a211","a212","a213","a214","a215","a216","a217","a218","a219","a220","a221","a222","a223","a224","a225","a226","a227","a228","a229","a230","a231","a232","a233","a234","a235","a236","a237","a238","a239","a240","a241","a242","a243","a244","a245","a246","a247","a248","a249","a250","a251","a252","a253","a254","a255","s8","s9","s10","s11","s12","s13","s14","s15","s16","s17","s18","s19","s20","s21","s22","s23","s24","s25","s26","s27","s28","s29","s30","s31","s32","s33","s34","s35","s36","s37","s38","s39","s40","s41","s42","s43","s44","s45","s46","s47","s48","s49","s50","s51","s52","s53","s54","s55","s56","s57","s58","s59","s60","s61","s62","s63","s64","s65","s66","s67","s68","s69","s70","s71","s72","s73","s74","s75","s76","s77","s78","s79","s80","s81","s82","s83","s84","s85","s86","s87","s88","s89","s90","s91","s92","s93","s94","s95","s96","s97","s98","s99","s100","s101","vcc","memory");
}

extern "C" void kernel_launch(void* const* d_in, const int* in_sizes, int n_in, void* d_out, int out_size, void* d_ws, size_t ws_size, hipStream_t stream) {
  fused_s5_geglu<<<256, 256, 0, stream>>>(d_in[0], 2621440u, 0x20000u, d_in[3], 819200u, 0x20000u, d_in[4], 821248u, 0x20000u, d_in[10], 1966080u, 0x20000u, d_in[11], 1968128u, 0x20000u, d_in[12], 1966080u, 0x20000u, d_in[5], 822272u, 0x20000u, d_in[6], 822272u, 0x20000u, d_ws, 25427968u, 0x20000u, d_out, 5242880u, 0x20000u, d_in[1], 1280u, 0x20000u, d_in[2], d_in[7], d_in[8]);
}
```
